```python
import torch

def _softmax(x, axis=-1):
    return torch.softmax(x, dim=axis)

def _gelu(x):
    return torch.nn.functional.gelu(x, approximate='tanh')

def _silu(x):
    return torch.nn.functional.silu(x)

def _sigmoid(x):
    return torch.sigmoid(x)

def _layernorm(x, axis=-1, eps=1e-05):
    m = x.mean(dim=axis, keepdim=True)
    v = x.var(dim=axis, keepdim=True, unbiased=False)
    return (x - m) / torch.sqrt(v + eps)

def _scatter_add(tgt, idx, src, axis=0):
    return tgt.clone().index_add_(axis, idx, src)

def _segment_sum(src, seg, n):
    out = torch.zeros((n,) + src.shape[1:], dtype=src.dtype)
    return out.index_add_(0, seg, src)

def _pad(x, pw):
    flat = tuple((v for pair in reversed(pw) for v in pair))
    return torch.nn.functional.pad(x, flat)
kernel_name = 'standalone__autoregressive_conv_wavernn__speech'

def setup_inputs():

    _w = lambda a: (lambda t: t.to(torch.bfloat16) if t.is_floating_point() else t)(torch.as_tensor(a))
    B, T, D, K = (2, 16000, 512, 5)
    return {'x': _w((lambda *s: torch.randn(*s, dtype=torch.bfloat16) * 0.05)(B, T, D)), 'w': _w((lambda *s: torch.randn(*s, dtype=torch.bfloat16) * 0.05)(K, D, 2 * D))}

def reference(x, w):
    B, T, D = x.shape
    K = w.shape[0]
    dil = 2
    pad = (K - 1) * dil
    xp = _pad(x, ((0, 0), (pad, 0), (0, 0)))
    h = torch.einsum('btd,de->bte', xp[:, 0:T, :], w[0])
    for i in range(1, K):
        h = h + torch.einsum('btd,de->bte', xp[:, i * dil:i * dil + T, :], w[i])
    f, g = (h[..., :D].clone(), h[..., D:].clone())
    return torch.tanh(f) * _sigmoid(g)
```

```cpp
#include <hip/hip_runtime.h>
#include <hip/hip_bf16.h>

#define T_LEN 16000u
typedef unsigned u32x4 __attribute__((ext_vector_type(4)));
__device__ __forceinline__ u32x4 make_srd(unsigned long long base, unsigned bytes) {
  return u32x4{(unsigned)base, (unsigned)(base >> 32) & 0xffffu, bytes, 0x00020000u};
}

__global__ __launch_bounds__(256, 1) void wavernn_conv_main(const char* __restrict__ x, const char* __restrict__ wt, char* __restrict__ out ) {
  __shared__ __attribute__((aligned(16))) char smem[156672];
  const unsigned bid = blockIdx.x;
  unsigned w = __builtin_amdgcn_readfirstlane(threadIdx.x >> 6);
  unsigned lane = threadIdx.x & 63u;
  unsigned bb = bid;
  const unsigned lds0 = (unsigned)(size_t)smem;
  unsigned vcw = lane < 4u ? (((bb >> 3) * 4u + w) * 4u + lane) * 128u : 0x40000000u;
  u32x4 srdc;
  asm volatile("s_getpc_b64 s[44:45]\n\twavernn_code_begin:\n\ts_and_b32 s45, s45, 0xffff\n\ts_mov_b32 s46, wavernn_code_end-wavernn_code_begin\n\t"
               "s_mov_b32 m0, %[m]\n\ts_mov_b32 s47, 0x20000\n\tbuffer_load_dword %[v], s[44:47], 0 offen lds"
               : "+s"(bb), "+s"(w), "+v"(lane), "=&{s[44:47]}"(srdc) : [v] "{v247}"(vcw), [m] "s"(lds0 + 155648u + w * 256u) : "memory", "scc", "m0");
  const unsigned nt = ((bb >> 1) & 1u) * 4u + ((bb >> 3) & 3u);
  const unsigned ow = nt * 128u;
  const unsigned ismw = lds0 + 73728u + w * 2048u;
  const unsigned fw = ((lane >> 4) & 1u) | ((w & 1u) << 1);
  const unsigned vw = (w * 8u + (lane >> 3)) * 2048u + (((lane & 7u) ^ (fw << 1)) * 16u);

  asm volatile("" : "+s"(wt) : "s"(ow), "s"(ismw), "v"(vw));
  u32x4 srdw = make_srd((unsigned long long)wt + ow, 5u * 512u * 2048u - ow);
  asm volatile(
      "s_mov_b32 m0, %[ismw]\n\t"
      "s_mov_b32 s67, 0\n\t"
      "s_mov_b32 s68, 0x100000\n\t"
      "buffer_load_dwordx4 %[vw], s[56:59], s67 offen lds\n\t"
      "buffer_load_dwordx4 %[vw], s[56:59], s67 offen offset:1024 lds\n\t"
      "s_addk_i32 m0, 8192\n\t"
      "s_add_u32 s67, s67, s68\n\t"
      "buffer_load_dwordx4 %[vw], s[56:59], s67 offen lds\n\t"
      "buffer_load_dwordx4 %[vw], s[56:59], s67 offen offset:1024 lds\n\t"
      "s_addk_i32 m0, 8192\n\t"
      "s_add_u32 s67, s67, s68\n\t"
      "buffer_load_dwordx4 %[vw], s[56:59], s67 offen lds\n\t"
      "buffer_load_dwordx4 %[vw], s[56:59], s67 offen offset:1024 lds\n\t"
      "s_addk_i32 m0, 8192\n\t"
      "s_add_u32 s67, s67, s68\n\t"
      "buffer_load_dwordx4 %[vw], s[56:59], s67 offen lds\n\t"
      "buffer_load_dwordx4 %[vw], s[56:59], s67 offen offset:1024 lds\n\t"
      "s_addk_i32 m0, 8192\n\t"
      "s_add_u32 s67, s67, s68\n\t"
      "buffer_load_dwordx4 %[vw], s[56:59], s67 offen lds\n\t"
      "buffer_load_dwordx4 %[vw], s[56:59], s67 offen offset:1024 lds\n\t"
      : "+s"(bb), "+s"(w), "+v"(lane)
      : "{s[56:59]}"(srdw), [ismw] "s"(ismw), [vw] "v"(vw)
      : "memory", "scc", "m0", "s67", "s68");
  const unsigned odd = bb & 1u, bat = (bb >> 2) & 1u, seg = bb >> 5;
  unsigned ija = odd ? 31u : 32u;
  const unsigned ta = seg * 2000u + (odd ? (32u + 32u) * 16u : 0u);
  const unsigned nrow = seg * 2000u + 2000u - ta;
  const unsigned oxa = (bat * T_LEN + ta) * 1024u;
  u32x4 srdx = make_srd((unsigned long long)x + oxa - 8192u, (nrow + 8u) * 1024u);
  unsigned sx1 = ija << 13;
  unsigned islow = (ta == 0u && w == 0u) ? 0x80000000u : 0u;
  const unsigned ismx = lds0 + w * 9216u;
  unsigned vx = (w * ija * 2u + (lane >> 3)) * 1024u + (((lane & 7u) ^ (w << 1)) * 16u);
  asm volatile(
      "s_mov_b32 m0, %[ismx]\n\t"
      "s_nop 0\n\t"
      "buffer_load_dwordx4 %[vx], s[48:51], s74 offen lds\n\t"
      "s_movk_i32 s67, 7168\n\t"
      "buffer_load_dwordx4 %[vx], s[48:51], s67 offen offset:1024 lds\n\t"
      "s_movk_i32 s67, 14336\n\t"
      "buffer_load_dwordx4 %[vx], s[48:51], s67 offen offset:2048 lds\n\t"
      "s_add_u32 m0, %[ismx], 36864\n\t"
      "s_nop 0\n\t"
      "buffer_load_dwordx4 %[vx], s[48:51], s76 offen lds\n\t"
      "s_add_u32 s67, s76, 7168\n\t"
      "buffer_load_dwordx4 %[vx], s[48:51], s67 offen offset:1024 lds\n\t"
      "s_add_u32 s67, s76, 14336\n\t"
      "buffer_load_dwordx4 %[vx], s[48:51], s67 offen offset:2048 lds\n\t"
      "s_mov_b32 s68, 0x100000\n\t"
      "s_add_u32 m0, %[ismw], 40960\n\t"
      "s_mov_b32 s67, 0x10000\n\t"
      "buffer_load_dwordx4 %[vw], s[56:59], s67 offen lds\n\t"
      "buffer_load_dwordx4 %[vw], s[56:59], s67 offen offset:1024 lds\n\t"
      "s_addk_i32 m0, 8192\n\t"
      "s_add_u32 s67, s67, s68\n\t"
      "buffer_load_dwordx4 %[vw], s[56:59], s67 offen lds\n\t"
      "buffer_load_dwordx4 %[vw], s[56:59], s67 offen offset:1024 lds\n\t"
      "s_addk_i32 m0, 8192\n\t"
      "s_add_u32 s67, s67, s68\n\t"
      "buffer_load_dwordx4 %[vw], s[56:59], s67 offen lds\n\t"
      "buffer_load_dwordx4 %[vw], s[56:59], s67 offen offset:1024 lds\n\t"
      "s_addk_i32 m0, 8192\n\t"
      "s_add_u32 s67, s67, s68\n\t"
      "buffer_load_dwordx4 %[vw], s[56:59], s67 offen lds\n\t"
      "buffer_load_dwordx4 %[vw], s[56:59], s67 offen offset:1024 lds\n\t"
      "s_addk_i32 m0, 8192\n\t"
      "s_add_u32 s67, s67, s68\n\t"
      "buffer_load_dwordx4 %[vw], s[56:59], s67 offen lds\n\t"
      "buffer_load_dwordx4 %[vw], s[56:59], s67 offen offset:1024 lds\n\t"
      : "+s"(bb), "+s"(w), "+v"(lane)
      : "{s[56:59]}"(srdw), [ismw] "s"(ismw), [vw] "v"(vw), "{s[48:51]}"(srdx), "{s74}"(islow), "{s76}"(sx1), [ismx] "s"(ismx), [vx] "v"(vx)
      : "memory", "scc", "m0", "s67", "s68");
  const unsigned ijb = (bb & 1u) ? 30u : 32u;
  const unsigned colb = ow + w * 32u;
  u32x4 srdo = make_srd((unsigned long long)out + oxa + colb, nrow * 1024u - colb);
  unsigned sx0 = 0u, sw = 0u, sxh = sx1;
  const unsigned idx_ = w * (ijb - ija) * 2048u;
  const unsigned c = lane & 15u, rr = c >> 1, p = c & 1u, kq = lane >> 4;
  const unsigned rowp = lds0 + rr * 9216u + p * 128u;
  const unsigned lx0 = rowp + (((0u + kq) ^ ((rr & 3u) << 1)) * 16u);
  const unsigned lx1 = rowp + (((4u + kq) ^ ((rr & 3u) << 1)) * 16u);
  const unsigned tq = c >> 2, tp = lane & 3u;
  const unsigned fr = ((tq >> 1) & 1u) | ((kq & 1u) << 1);
  const unsigned lw0 = lds0 + 73728u + kq * 2048u + tq * 128u + (((2u * w + (tp >> 1)) ^ (fr << 1)) * 16u) + (tp & 1u) * 8u;
  const unsigned lw1 = lw0 + 40960u;
  const unsigned vod = rr * (ijb - ija) * 2048u;
  unsigned vo = rr * ija * 2048u + p * 1024u + kq * 8u;
  asm volatile(
      "v_accvgpr_write_b32 a176, v247\n\t"
      "s_waitcnt vmcnt(10)\n\t"
      "s_barrier\n\t"
      "ds_read_b64_tr_b16 a[32:33], %[lw0] offset:0\n\t"
      "ds_read_b64_tr_b16 a[34:35], %[lw0] offset:512\n\t"
      "ds_read_b128 a[112:115], %[lx0] offset:0\n\t"
      "ds_read_b64_tr_b16 a[40:41], %[lw0] offset:8192\n\t"
      "ds_read_b64_tr_b16 a[42:43], %[lw0] offset:8704\n\t"
      "ds_read_b128 a[116:119], %[lx0] offset:256\n\t"
      "ds_read_b64_tr_b16 a[48:49], %[lw0] offset:16384\n\t"
      "ds_read_b64_tr_b16 a[50:51], %[lw0] offset:16896\n\t"
      "ds_read_b128 a[120:123], %[lx0] offset:512\n\t"
      "ds_read_b64_tr_b16 a[56:57], %[lw0] offset:24576\n\t"
      "ds_read_b64_tr_b16 a[58:59], %[lw0] offset:25088\n\t"
      "ds_read_b128 a[124:127], %[lx0] offset:768\n\t"
      "ds_read_b64_tr_b16 a[64:65], %[lw0] offset:32768\n\t"
      "ds_read_b64_tr_b16 a[66:67], %[lw0] offset:33280\n\t"
      "ds_read_b128 a[128:131], %[lx0] offset:1024\n\t"
      "ds_read_b64_tr_b16 a[68:69], %[lw0] offset:33792\n\t"
      "ds_read_b64_tr_b16 a[70:71], %[lw0] offset:34304\n\t"
      "ds_read_b64_tr_b16 a[60:61], %[lw0] offset:25600\n\t"
      "ds_read_b64_tr_b16 a[62:63], %[lw0] offset:26112\n\t"
      "ds_read_b64_tr_b16 a[52:53], %[lw0] offset:17408\n\t"
      "ds_read_b64_tr_b16 a[54:55], %[lw0] offset:17920\n\t"
      "ds_read_b64_tr_b16 a[44:45], %[lw0] offset:9216\n\t"
      "ds_read_b64_tr_b16 a[46:47], %[lw0] offset:9728\n\t"
      "ds_read_b64_tr_b16 a[36:37], %[lw0] offset:1024\n\t"
      "ds_read_b64_tr_b16 a[38:39], %[lw0] offset:1536\n\t"
      "ds_read_b128 a[132:135], %[lx0] offset:1280\n\t"
      "ds_read_b128 a[136:139], %[lx0] offset:1536\n\t"
      "ds_read_b128 a[140:143], %[lx0] offset:1792\n\t"
      "s_waitcnt lgkmcnt(15)\n\t"
      "v_mfma_f32_16x16x32_bf16 v[0:3], a[32:35], a[112:115], 0\n\t"
      "v_mov_b32 v68, 0\n\t"
      "v_mov_b32 v69, 0\n\t"
      "v_mfma_f32_16x16x32_bf16 v[0:3], a[40:43], a[116:119], v[0:3]\n\t"
      "s_add_u32 m0, %[ismx], 3072\n\t"
      "v_mov_b32 v70, 0\n\t"
      "v_mov_b32 v71, 0\n\t"
      "v_mfma_f32_16x16x32_bf16 v[0:3], a[48:51], a[120:123], v[0:3]\n\t"
      "s_add_u32 s67, s71, 24576\n\t"
      "v_mov_b32 v72, 0\n\t"
      "v_mov_b32 v73, 0\n\t"
      "v_mfma_f32_16x16x32_bf16 v[0:3], a[56:59], a[124:127], v[0:3]\n\t"
      "s_waitcnt lgkmcnt(13)\n\t"
      "buffer_load_dwordx4 %[vx], s[48:51], s67 offen lds\n\t"
      "v_mov_b32 v74, 0\n\t"
      "v_mfma_f32_16x16x32_bf16 v[0:3], a[64:67], a[128:131], v[0:3]\n\t"
      "s_waitcnt lgkmcnt(11)\n\t"
      "v_mov_b32 v75, 0\n\t"
      "v_mov_b32 v76, 0\n\t"
      "v_mfma_f32_16x16x32_bf16 v[4:7], a[68:71], a[128:131], 0\n\t"
      "s_waitcnt lgkmcnt(9)\n\t"
      "v_mov_b32 v77, 0\n\t"
      "v_mov_b32 v78, 0\n\t"
      "v_mfma_f32_16x16x32_bf16 v[4:7], a[60:63], a[124:127], v[4:7]\n\t"
      "s_waitcnt lgkmcnt(7)\n\t"
      "v_mov_b32 v79, 0\n\t"
      "v_mov_b32 v80, 0\n\t"
      "v_mfma_f32_16x16x32_bf16 v[4:7], a[52:55], a[120:123], v[4:7]\n\t"
      "s_waitcnt lgkmcnt(5)\n\t"
      "v_mov_b32 v81, 0\n\t"
      "v_mov_b32 v82, 0\n\t"
      "v_mfma_f32_16x16x32_bf16 v[4:7], a[44:47], a[116:119], v[4:7]\n\t"
      "s_waitcnt lgkmcnt(3)\n\t"
      "v_mov_b32 v83, 0\n\t"
      "v_mov_b32 v84, 0\n\t"
      "v_mfma_f32_16x16x32_bf16 v[4:7], a[36:39], a[112:115], v[4:7]\n\t"
      "s_add_u32 m0, %[ismx], 39936\n\t"
      "v_mov_b32 v85, 0\n\t"
      "v_mov_b32 v86, 0\n\t"
      "v_mfma_f32_16x16x32_bf16 v[12:15], a[36:39], a[116:119], 0\n\t"
      "s_add_u32 s67, s76, 24576\n\t"
      "ds_read_b128 a[112:115], %[lx0] offset:2048\n\t"
      "v_mov_b32 v87, 0\n\t"
      "v_mfma_f32_16x16x32_bf16 v[12:15], a[44:47], a[120:123], v[12:15]\n\t"
      "buffer_load_dwordx4 %[vx], s[48:51], s67 offen lds\n\t"
      "v_mov_b32 v88, 0\n\t"
      "v_mfma_f32_16x16x32_bf16 v[12:15], a[52:55], a[124:127], v[12:15]\n\t"
      "v_mov_b32 v89, 0\n\t"
      "v_mov_b32 v90, 0\n\t"
      "v_mfma_f32_16x16x32_bf16 v[12:15], a[60:63], a[128:131], v[12:15]\n\t"
      "s_waitcnt lgkmcnt(3)\n\t"
      "v_mov_b32 v91, 0\n\t"
      "v_mov_b32 v92, 0\n\t"
      "v_mfma_f32_16x16x32_bf16 v[12:15], a[68:71], a[132:135], v[12:15]\n\t"
      "v_mov_b32 v93, 0\n\t"
      "v_mov_b32 v94, 0\n\t"
      "v_mfma_f32_16x16x32_bf16 v[8:11], a[64:67], a[132:135], 0\n\t"
      "s_add_u32 m0, %[ismx], 4096\n\t"
      "v_mov_b32 v95, 0\n\t"
      "v_mov_b32 v96, 0\n\t"
      "v_mfma_f32_16x16x32_bf16 v[8:11], a[56:59], a[128:131], v[8:11]\n\t"
      "s_add_u32 s67, s71, 32768\n\t"
      "v_mov_b32 v97, 0\n\t"
      "v_mov_b32 v98, 0\n\t"
      "v_mfma_f32_16x16x32_bf16 v[8:11], a[48:51], a[124:127], v[8:11]\n\t"
      "buffer_load_dwordx4 %[vx], s[48:51], s67 offen lds\n\t"
      "v_mov_b32 v99, 0\n\t"
      "v_mfma_f32_16x16x32_bf16 v[8:11], a[40:43], a[120:123], v[8:11]\n\t"
      "v_mov_b32 v100, 0\n\t"
      "v_mov_b32 v101, 0\n\t"
      "v_mfma_f32_16x16x32_bf16 v[8:11], a[32:35], a[116:119], v[8:11]\n\t"
      "v_mov_b32 v102, 0\n\t"
      "v_mov_b32 v103, 0\n\t"
      "v_mfma_f32_16x16x32_bf16 v[16:19], a[32:35], a[120:123], 0\n\t"
      "ds_read_b128 a[116:119], %[lx0] offset:2304\n\t"
      "v_mov_b32 v104, 0\n\t"
      "v_mfma_f32_16x16x32_bf16 v[16:19], a[40:43], a[124:127], v[16:19]\n\t"
      "v_mov_b32 v105, 0\n\t"
      "v_mov_b32 v106, 0\n\t"
      "v_mfma_f32_16x16x32_bf16 v[16:19], a[48:51], a[128:131], v[16:19]\n\t"
      "s_add_u32 m0, %[ismx], 40960\n\t"
      "v_mov_b32 v107, 0\n\t"
      "v_mov_b32 v108, 0\n\t"
      "v_mfma_f32_16x16x32_bf16 v[16:19], a[56:59], a[132:135], v[16:19]\n\t"
      "s_waitcnt lgkmcnt(3)\n\t"
      "s_add_u32 s67, s76, 32768\n\t"
      "v_mov_b32 v109, 0\n\t"
      "v_mfma_f32_16x16x32_bf16 v[16:19], a[64:67], a[136:139], v[16:19]\n\t"
      "buffer_load_dwordx4 %[vx], s[48:51], s67 offen lds\n\t"
      "v_mov_b32 v110, 0\n\t"
      "v_mfma_f32_16x16x32_bf16 v[20:23], a[68:71], a[136:139], 0\n\t"
      "v_mov_b32 v111, 0\n\t"
      "v_mov_b32 v112, 0\n\t"
      "v_mfma_f32_16x16x32_bf16 v[20:23], a[60:63], a[132:135], v[20:23]\n\t"
      "v_mov_b32 v113, 0\n\t"
      "v_mov_b32 v114, 0\n\t"
      "v_mfma_f32_16x16x32_bf16 v[20:23], a[52:55], a[128:131], v[20:23]\n\t"
      "v_mov_b32 v115, 0\n\t"
      "v_mov_b32 v116, 0\n\t"
      "v_mfma_f32_16x16x32_bf16 v[20:23], a[44:47], a[124:127], v[20:23]\n\t"
      "v_mov_b32 v117, 0\n\t"
      "v_mov_b32 v118, 0\n\t"
      "v_mfma_f32_16x16x32_bf16 v[20:23], a[36:39], a[120:123], v[20:23]\n\t"
      "s_add_u32 m0, %[ismx], 5120\n\t"
      "v_mov_b32 v119, 0\n\t"
      "v_mov_b32 v120, 0\n\t"
      "v_mfma_f32_16x16x32_bf16 v[28:31], a[36:39], a[124:127], 0\n\t"
      "s_add_u32 s67, s71, 40960\n\t"
      "ds_read_b128 a[120:123], %[lx0] offset:2560\n\t"
      "v_mov_b32 v121, 0\n\t"
      "v_mfma_f32_16x16x32_bf16 v[28:31], a[44:47], a[128:131], v[28:31]\n\t"
      "buffer_load_dwordx4 %[vx], s[48:51], s67 offen lds\n\t"
      "v_mov_b32 v122, 0\n\t"
      "v_mfma_f32_16x16x32_bf16 v[28:31], a[52:55], a[132:135], v[28:31]\n\t"
      "v_mov_b32 v123, 0\n\t"
      "v_mov_b32 v124, 0\n\t"
      "v_mfma_f32_16x16x32_bf16 v[28:31], a[60:63], a[136:139], v[28:31]\n\t"
      "s_waitcnt lgkmcnt(3)\n\t"
      "v_mov_b32 v125, 0\n\t"
      "v_mov_b32 v126, 0\n\t"
      "v_mfma_f32_16x16x32_bf16 v[28:31], a[68:71], a[140:143], v[28:31]\n\t"
      "v_mov_b32 v127, 0\n\t"
      "v_mov_b32 v128, 0\n\t"
      "v_mfma_f32_16x16x32_bf16 v[24:27], a[64:67], a[140:143], 0\n\t"
      "v_mov_b32 v129, 0\n\t"
      "v_mov_b32 v130, 0\n\t"
      "v_mfma_f32_16x16x32_bf16 v[24:27], a[56:59], a[136:139], v[24:27]\n\t"
      "s_add_u32 m0, %[ismx], 41984\n\t"
      "v_mov_b32 v131, 0\n\t"
      "v_mov_b32 v132, 0\n\t"
      "v_mfma_f32_16x16x32_bf16 v[24:27], a[48:51], a[132:135], v[24:27]\n\t"
      "s_add_u32 s67, s76, 40960\n\t"
      "v_mov_b32 v133, 0\n\t"
      "v_mov_b32 v134, 0\n\t"
      "v_mfma_f32_16x16x32_bf16 v[24:27], a[40:43], a[128:131], v[24:27]\n\t"
      "buffer_load_dwordx4 %[vx], s[48:51], s67 offen lds\n\t"
      "v_mov_b32 v135, 0\n\t"
      "v_mfma_f32_16x16x32_bf16 v[24:27], a[32:35], a[124:127], v[24:27]\n\t"
      "v_mov_b32 v136, 0\n\t"
      "v_mov_b32 v137, 0\n\t"
      "v_mfma_f32_16x16x32_bf16 v[32:35], a[32:35], a[128:131], 0\n\t"
      "ds_read_b128 a[124:127], %[lx0] offset:2816\n\t"
      "v_mov_b32 v138, 0\n\t"
      "v_mfma_f32_16x16x32_bf16 v[32:35], a[40:43], a[132:135], v[32:35]\n\t"
      "v_mov_b32 v139, 0\n\t"
      "v_mov_b32 v140, 0\n\t"
      "v_mfma_f32_16x16x32_bf16 v[32:35], a[48:51], a[136:139], v[32:35]\n\t"
      "v_mov_b32 v141, 0\n\t"
      "v_mov_b32 v142, 0\n\t"
      "v_mfma_f32_16x16x32_bf16 v[32:35], a[56:59], a[140:143], v[32:35]\n\t"
      "s_waitcnt lgkmcnt(3)\n\t"
      "v_mov_b32 v143, 0\n\t"
      "v_mov_b32 v144, 0\n\t"
      "v_mfma_f32_16x16x32_bf16 v[32:35], a[64:67], a[112:115], v[32:35]\n\t"
      "s_waitcnt vmcnt(6)\n\t"
      "s_barrier\n\t"
      "ds_read_b64_tr_b16 a[72:73], %[lw1] offset:0\n\t"
      "v_mfma_f32_16x16x32_bf16 v[36:39], a[68:71], a[112:115], 0\n\t"
      "ds_read_b64_tr_b16 a[74:75], %[lw1] offset:512\n\t"
      "v_mov_b32 v145, 0\n\t"
      "v_mfma_f32_16x16x32_bf16 v[36:39], a[60:63], a[140:143], v[36:39]\n\t"
      "ds_read_b128 a[144:147], %[lx1] offset:0\n\t"
      "v_mov_b32 v146, 0\n\t"
      "v_mfma_f32_16x16x32_bf16 v[36:39], a[52:55], a[136:139], v[36:39]\n\t"
      "ds_read_b64_tr_b16 a[80:81], %[lw1] offset:8192\n\t"
      "v_mov_b32 v147, 0\n\t"
      "v_mfma_f32_16x16x32_bf16 v[36:39], a[44:47], a[132:135], v[36:39]\n\t"
      "ds_read_b64_tr_b16 a[82:83], %[lw1] offset:8704\n\t"
      "v_mov_b32 v148, 0\n\t"
      "v_mfma_f32_16x16x32_bf16 v[36:39], a[36:39], a[128:131], v[36:39]\n\t"
      "ds_read_b128 a[148:151], %[lx1] offset:256\n\t"
      "v_mov_b32 v149, 0\n\t"
      "v_mfma_f32_16x16x32_bf16 v[44:47], a[36:39], a[132:135], 0\n\t"
      "ds_read_b64_tr_b16 a[88:89], %[lw1] offset:16384\n\t"
      "v_mov_b32 v150, 0\n\t"
      "v_mfma_f32_16x16x32_bf16 v[44:47], a[44:47], a[136:139], v[44:47]\n\t"
      "ds_read_b64_tr_b16 a[90:91], %[lw1] offset:16896\n\t"
      "v_mov_b32 v151, 0\n\t"
      "v_mfma_f32_16x16x32_bf16 v[44:47], a[52:55], a[140:143], v[44:47]\n\t"
      "ds_read_b128 a[152:155], %[lx1] offset:512\n\t"
      "v_mov_b32 v152, 0\n\t"
      "v_mfma_f32_16x16x32_bf16 v[44:47], a[60:63], a[112:115], v[44:47]\n\t"
      "s_waitcnt lgkmcnt(11)\n\t"
      "ds_read_b64_tr_b16 a[96:97], %[lw1] offset:24576\n\t"
      "v_mov_b32 v153, 0\n\t"
      "v_mfma_f32_16x16x32_bf16 v[44:47], a[68:71], a[116:119], v[44:47]\n\t"
      "ds_read_b64_tr_b16 a[98:99], %[lw1] offset:25088\n\t"
      "v_mov_b32 v154, 0\n\t"
      "v_mfma_f32_16x16x32_bf16 v[40:43], a[64:67], a[116:119], 0\n\t"
      "ds_read_b128 a[156:159], %[lx1] offset:768\n\t"
      "v_mov_b32 v155, 0\n\t"
      "v_mfma_f32_16x16x32_bf16 v[40:43], a[56:59], a[112:115], v[40:43]\n\t"
      "ds_read_b64_tr_b16 a[104:105], %[lw1] offset:32768\n\t"
      "v_mov_b32 v156, 0\n\t"
      "v_mfma_f32_16x16x32_bf16 v[40:43], a[48:51], a[140:143], v[40:43]\n\t"
      "ds_read_b64_tr_b16 a[106:107], %[lw1] offset:33280\n\t"
      "v_mov_b32 v157, 0\n\t"
      "v_mfma_f32_16x16x32_bf16 v[40:43], a[40:43], a[136:139], v[40:43]\n\t"
      "ds_read_b128 a[160:163], %[lx1] offset:1024\n\t"
      "v_mov_b32 v158, 0\n\t"
      "v_mfma_f32_16x16x32_bf16 v[40:43], a[32:35], a[132:135], v[40:43]\n\t"
      "ds_read_b64_tr_b16 a[108:109], %[lw1] offset:33792\n\t"
      "v_mov_b32 v159, 0\n\t"
      "v_mfma_f32_16x16x32_bf16 v[48:51], a[32:35], a[136:139], 0\n\t"
      "ds_read_b64_tr_b16 a[110:111], %[lw1] offset:34304\n\t"
      "v_mov_b32 v160, 0\n\t"
      "v_mfma_f32_16x16x32_bf16 v[48:51], a[40:43], a[140:143], v[48:51]\n\t"
      "ds_read_b64_tr_b16 a[100:101], %[lw1] offset:25600\n\t"
      "v_mov_b32 v161, 0\n\t"
      "v_mfma_f32_16x16x32_bf16 v[48:51], a[48:51], a[112:115], v[48:51]\n\t"
      "ds_read_b64_tr_b16 a[102:103], %[lw1] offset:26112\n\t"
      "v_mov_b32 v162, 0\n\t"
      "v_mfma_f32_16x16x32_bf16 v[48:51], a[56:59], a[116:119], v[48:51]\n\t"
      "s_waitcnt lgkmcnt(15)\n\t"
      "ds_read_b64_tr_b16 a[92:93], %[lw1] offset:17408\n\t"
      "v_mov_b32 v163, 0\n\t"
      "v_mfma_f32_16x16x32_bf16 v[48:51], a[64:67], a[120:123], v[48:51]\n\t"
      "ds_read_b64_tr_b16 a[94:95], %[lw1] offset:17920\n\t"
      "v_mov_b32 v164, 0\n\t"
      "v_mfma_f32_16x16x32_bf16 v[52:55], a[68:71], a[120:123], 0\n\t"
      "ds_read_b64_tr_b16 a[84:85], %[lw1] offset:9216\n\t"
      "v_mov_b32 v165, 0\n\t"
      "v_mfma_f32_16x16x32_bf16 v[52:55], a[60:63], a[116:119], v[52:55]\n\t"
      "ds_read_b64_tr_b16 a[86:87], %[lw1] offset:9728\n\t"
      "v_mov_b32 v166, 0\n\t"
      "v_mfma_f32_16x16x32_bf16 v[52:55], a[52:55], a[112:115], v[52:55]\n\t"
      "ds_read_b64_tr_b16 a[76:77], %[lw1] offset:1024\n\t"
      "v_mov_b32 v167, 0\n\t"
      "v_mfma_f32_16x16x32_bf16 v[52:55], a[44:47], a[140:143], v[52:55]\n\t"
      "ds_read_b64_tr_b16 a[78:79], %[lw1] offset:1536\n\t"
      "v_mov_b32 v168, 0\n\t"
      "v_mfma_f32_16x16x32_bf16 v[52:55], a[36:39], a[136:139], v[52:55]\n\t"
      "ds_read_b128 a[164:167], %[lx1] offset:1280\n\t"
      "v_mov_b32 v169, 0\n\t"
      "v_mfma_f32_16x16x32_bf16 v[60:63], a[36:39], a[140:143], 0\n\t"
      "ds_read_b128 a[168:171], %[lx1] offset:1536\n\t"
      "v_mov_b32 v170, 0\n\t"
      "v_mfma_f32_16x16x32_bf16 v[60:63], a[44:47], a[112:115], v[60:63]\n\t"
      "ds_read_b128 a[172:175], %[lx1] offset:1792\n\t"
      "v_mov_b32 v171, 0\n\t"
      "v_mfma_f32_16x16x32_bf16 v[60:63], a[52:55], a[116:119], v[60:63]\n\t"
      "v_mov_b32 v172, 0\n\t"
      "v_mov_b32 v173, 0\n\t"
      "v_mfma_f32_16x16x32_bf16 v[60:63], a[60:63], a[120:123], v[60:63]\n\t"
      "s_waitcnt lgkmcnt(15)\n\t"
      "v_mov_b32 v174, 0\n\t"
      "v_mov_b32 v175, 0\n\t"
      "v_mfma_f32_16x16x32_bf16 v[60:63], a[68:71], a[124:127], v[60:63]\n\t"
      "v_mov_b32 v176, 0\n\t"
      "v_mov_b32 v177, 0\n\t"
      "v_mfma_f32_16x16x32_bf16 v[56:59], a[64:67], a[124:127], 0\n\t"
      "v_mov_b32 v178, 0\n\t"
      "v_mov_b32 v179, 0\n\t"
      "v_mfma_f32_16x16x32_bf16 v[56:59], a[56:59], a[120:123], v[56:59]\n\t"
      "s_add_u32 m0, %[ismx], 6144\n\t"
      "v_mov_b32 v180, 0\n\t"
      "v_mov_b32 v181, 0\n\t"
      "v_mfma_f32_16x16x32_bf16 v[56:59], a[48:51], a[116:119], v[56:59]\n\t"
      "s_add_u32 s67, s71, 49152\n\t"
      "v_mov_b32 v182, 0\n\t"
      "v_mov_b32 v183, 0\n\t"
      "v_mfma_f32_16x16x32_bf16 v[56:59], a[40:43], a[112:115], v[56:59]\n\t"
      "s_mov_b32 s70, 2\n\t"
      "buffer_load_dwordx4 %[vx], s[48:51], s67 offen lds\n\t"
      "v_mov_b32 v184, 0\n\t"
      "v_mfma_f32_16x16x32_bf16 v[56:59], a[32:35], a[140:143], v[56:59]\n\t"
      "s_mov_b32 s75, 0x7fffffff\n\t"
      "v_mov_b32 v185, 0\n\t"
      "v_mov_b32 v186, 0\n\t"
      "v_mfma_f32_16x16x32_bf16 v[0:3], a[72:75], a[144:147], v[0:3]\n\t"
      "s_mov_b32 s79, s78\n\t"
      "v_mov_b32 v187, 0\n\t"
      "v_mov_b32 v188, 0\n\t"
      "v_mfma_f32_16x16x32_bf16 v[0:3], a[80:83], a[148:151], v[0:3]\n\t"
      "s_lshl_b32 s60, s78, 14\n\t"
      "v_mov_b32 v189, 0\n\t"
      "v_mov_b32 v190, 0\n\t"
      "v_mfma_f32_16x16x32_bf16 v[0:3], a[88:91], a[152:155], v[0:3]\n\t"
      "s_mov_b32 s84, 0xc038aa3b\n\t"
      "v_mov_b32 v191, 0\n\t"
      "v_mov_b32 v192, 0\n\t"
      "v_mfma_f32_16x16x32_bf16 v[0:3], a[96:99], a[156:159], v[0:3]\n\t"
      "s_waitcnt lgkmcnt(13)\n\t"
      "s_mov_b32 s85, 0xc038aa3b\n\t"
      "v_mov_b32 v193, 0\n\t"
      "v_mfma_f32_16x16x32_bf16 v[0:3], a[104:107], a[160:163], v[0:3]\n\t"
      "s_waitcnt lgkmcnt(11)\n\t"
      "s_mov_b32 s86, 0xbfb8aa3b\n\t"
      "v_mov_b32 v194, 0\n\t"
      "v_mfma_f32_16x16x32_bf16 v[4:7], a[108:111], a[160:163], v[4:7]\n\t"
      "s_waitcnt lgkmcnt(9)\n\t"
      "s_mov_b32 s87, 0xbfb8aa3b\n\t"
      "v_mov_b32 v195, 0\n\t"
      "v_mfma_f32_16x16x32_bf16 v[4:7], a[100:103], a[156:159], v[4:7]\n\t"
      "s_waitcnt lgkmcnt(7)\n\t"
      "s_add_u32 m0, %[ismx], 43008\n\t"
      "v_mov_b32 v196, 0\n\t"
      "v_mfma_f32_16x16x32_bf16 v[4:7], a[92:95], a[152:155], v[4:7]\n\t"
      "s_waitcnt lgkmcnt(5)\n\t"
      "s_add_u32 s67, s76, 49152\n\t"
      "v_mov_b32 v197, 0\n\t"
      "v_mfma_f32_16x16x32_bf16 v[4:7], a[84:87], a[148:151], v[4:7]\n\t"
      "s_waitcnt lgkmcnt(3)\n\t"
      "s_mov_b32 s88, 0.5\n\t"
      "buffer_load_dwordx4 %[vx], s[48:51], s67 offen lds\n\t"
      "v_mfma_f32_16x16x32_bf16 v[4:7], a[76:79], a[144:147], v[4:7]\n\t"
      "s_mov_b32 s89, 0.5\n\t"
      "v_mov_b32 v198, 0\n\t"
      "v_mov_b32 v199, 0\n\t"
      "v_mfma_f32_16x16x32_bf16 v[12:15], a[76:79], a[148:151], v[12:15]\n\t"
      "s_mov_b32 s80, 0x40000000\n\t"
      "ds_read_b128 a[144:147], %[lx1] offset:2048\n\t"
      "v_mov_b32 v200, 0\n\t"
      "v_mfma_f32_16x16x32_bf16 v[12:15], a[84:87], a[152:155], v[12:15]\n\t"
      "v_mov_b32 v201, 0\n\t"
      "v_mov_b32 v202, 0\n\t"
      "v_mfma_f32_16x16x32_bf16 v[12:15], a[92:95], a[156:159], v[12:15]\n\t"
      "v_mov_b32 v203, 0\n\t"
      "v_mov_b32 v204, 0\n\t"
      "v_mfma_f32_16x16x32_bf16 v[12:15], a[100:103], a[160:163], v[12:15]\n\t"
      "s_waitcnt lgkmcnt(3)\n\t"
      "v_mov_b32 v205, 0\n\t"
      "v_mov_b32 v206, 0\n\t"
      "v_mfma_f32_16x16x32_bf16 v[12:15], a[108:111], a[164:167], v[12:15]\n\t"
      "v_mov_b32 v207, 0\n\t"
      "v_mov_b32 v208, 0\n\t"
      "v_mfma_f32_16x16x32_bf16 v[8:11], a[104:107], a[164:167], v[8:11]\n\t"
      "v_mov_b32 v209, 0\n\t"
      "v_mov_b32 v210, 0\n\t"
      "v_mfma_f32_16x16x32_bf16 v[8:11], a[96:99], a[160:163], v[8:11]\n\t"
      "s_add_u32 m0, %[ismx], 7168\n\t"
      "v_mov_b32 v211, 0\n\t"
      "v_mov_b32 v212, 0\n\t"
      "v_mfma_f32_16x16x32_bf16 v[8:11], a[88:91], a[156:159], v[8:11]\n\t"
      "s_add_u32 s67, s71, 57344\n\t"
      "v_mov_b32 v213, 0\n\t"
      "v_mov_b32 v214, 0\n\t"
      "v_mfma_f32_16x16x32_bf16 v[8:11], a[80:83], a[152:155], v[8:11]\n\t"
      "buffer_load_dwordx4 %[vx], s[48:51], s67 offen lds\n\t"
      "v_mov_b32 v215, 0\n\t"
      "v_mfma_f32_16x16x32_bf16 v[8:11], a[72:75], a[148:151], v[8:11]\n\t"
      "v_mov_b32 v216, 0\n\t"
      "v_mov_b32 v217, 0\n\t"
      "v_mfma_f32_16x16x32_bf16 v[16:19], a[72:75], a[152:155], v[16:19]\n\t"
      "s_mov_b32 s69, 7\n\t"
      "ds_read_b128 a[148:151], %[lx1] offset:2304\n\t"
      "v_mov_b32 v218, 0\n\t"
      "v_mfma_f32_16x16x32_bf16 v[16:19], a[80:83], a[156:159], v[16:19]\n\t"
      "v_mov_b32 v219, 0\n\t"
      "v_mov_b32 v220, 0\n\t"
      "v_mfma_f32_16x16x32_bf16 v[16:19], a[88:91], a[160:163], v[16:19]\n\t"
      "v_mov_b32 v221, 0\n\t"
      "v_mov_b32 v222, 0\n\t"
      "v_mfma_f32_16x16x32_bf16 v[16:19], a[96:99], a[164:167], v[16:19]\n\t"
      "s_waitcnt lgkmcnt(3)\n\t"
      "v_mov_b32 v223, 0\n\t"
      "v_accvgpr_write_b32 a0, 0\n\t"
      "v_mfma_f32_16x16x32_bf16 v[16:19], a[104:107], a[168:171], v[16:19]\n\t"
      "v_accvgpr_write_b32 a1, 0\n\t"
      "v_accvgpr_write_b32 a2, 0\n\t"
      "v_mfma_f32_16x16x32_bf16 v[20:23], a[108:111], a[168:171], v[20:23]\n\t"
      "v_accvgpr_write_b32 a3, 0\n\t"
      "v_accvgpr_write_b32 a4, 0\n\t"
      "v_mfma_f32_16x16x32_bf16 v[20:23], a[100:103], a[164:167], v[20:23]\n\t"
      "s_add_u32 m0, %[ismx], 44032\n\t"
      "v_accvgpr_write_b32 a5, 0\n\t"
      "v_accvgpr_write_b32 a6, 0\n\t"
      "v_mfma_f32_16x16x32_bf16 v[20:23], a[92:95], a[160:163], v[20:23]\n\t"
      "s_add_u32 s67, s76, 57344\n\t"
      "v_accvgpr_write_b32 a7, 0\n\t"
      "v_accvgpr_write_b32 a8, 0\n\t"
      "v_mfma_f32_16x16x32_bf16 v[20:23], a[84:87], a[156:159], v[20:23]\n\t"
      "buffer_load_dwordx4 %[vx], s[48:51], s67 offen lds\n\t"
      "v_accvgpr_write_b32 a9, 0\n\t"
      "v_mfma_f32_16x16x32_bf16 v[20:23], a[76:79], a[152:155], v[20:23]\n\t"
      "v_accvgpr_write_b32 a10, 0\n\t"
      "v_accvgpr_write_b32 a11, 0\n\t"
      "v_mfma_f32_16x16x32_bf16 v[28:31], a[76:79], a[156:159], v[28:31]\n\t"
      "ds_read_b128 a[152:155], %[lx1] offset:2560\n\t"
      "v_accvgpr_write_b32 a12, 0\n\t"
      "v_mfma_f32_16x16x32_bf16 v[28:31], a[84:87], a[160:163], v[28:31]\n\t"
      "v_accvgpr_write_b32 a13, 0\n\t"
      "v_accvgpr_write_b32 a14, 0\n\t"
      "v_mfma_f32_16x16x32_bf16 v[28:31], a[92:95], a[164:167], v[28:31]\n\t"
      "v_accvgpr_write_b32 a15, 0\n\t"
      "v_accvgpr_write_b32 a16, 0\n\t"
      "v_mfma_f32_16x16x32_bf16 v[28:31], a[100:103], a[168:171], v[28:31]\n\t"
      "s_waitcnt lgkmcnt(3)\n\t"
      "v_accvgpr_write_b32 a17, 0\n\t"
      "v_accvgpr_write_b32 a18, 0\n\t"
      "v_mfma_f32_16x16x32_bf16 v[28:31], a[108:111], a[172:175], v[28:31]\n\t"
      "v_accvgpr_write_b32 a19, 0\n\t"
      "v_accvgpr_write_b32 a20, 0\n\t"
      "v_mfma_f32_16x16x32_bf16 v[24:27], a[104:107], a[172:175], v[24:27]\n\t"
      "v_accvgpr_write_b32 a21, 0\n\t"
      "v_accvgpr_write_b32 a22, 0\n\t"
      "v_mfma_f32_16x16x32_bf16 v[24:27], a[96:99], a[168:171], v[24:27]\n\t"
      "v_accvgpr_write_b32 a23, 0\n\t"
      "v_accvgpr_write_b32 a24, 0\n\t"
      "v_mfma_f32_16x16x32_bf16 v[24:27], a[88:91], a[164:167], v[24:27]\n\t"
      "v_accvgpr_write_b32 a25, 0\n\t"
      "v_accvgpr_write_b32 a26, 0\n\t"
      "v_mfma_f32_16x16x32_bf16 v[24:27], a[80:83], a[160:163], v[24:27]\n\t"
      "v_accvgpr_write_b32 a27, 0\n\t"
      "v_accvgpr_write_b32 a28, 0\n\t"
      "v_mfma_f32_16x16x32_bf16 v[24:27], a[72:75], a[156:159], v[24:27]\n\t"
      "v_accvgpr_write_b32 a29, 0\n\t"
      "v_accvgpr_write_b32 a30, 0\n\t"
      "v_mfma_f32_16x16x32_bf16 v[32:35], a[72:75], a[160:163], v[32:35]\n\t"
      "ds_read_b128 a[156:159], %[lx1] offset:2816\n\t"
      "v_accvgpr_write_b32 a31, 0\n\t"
      "v_mfma_f32_16x16x32_bf16 v[32:35], a[80:83], a[164:167], v[32:35]\n\t"
      "s_add_u32 m0, %[ismx], 8192\n\t"
      "v_mfma_f32_16x16x32_bf16 v[32:35], a[88:91], a[168:171], v[32:35]\n\t"
      "s_add_u32 s67, s71, 65536\n\t"
      "v_mfma_f32_16x16x32_bf16 v[32:35], a[96:99], a[172:175], v[32:35]\n\t"
      "s_waitcnt lgkmcnt(3)\n\t"
      "buffer_load_dwordx4 %[vx], s[48:51], s67 offen lds\n\t"
      "v_mfma_f32_16x16x32_bf16 v[32:35], a[104:107], a[144:147], v[32:35]\n\t"
      "v_mfma_f32_16x16x32_bf16 v[36:39], a[108:111], a[144:147], v[36:39]\n\t"
      "v_mfma_f32_16x16x32_bf16 v[36:39], a[100:103], a[172:175], v[36:39]\n\t"
      "v_mfma_f32_16x16x32_bf16 v[36:39], a[92:95], a[168:171], v[36:39]\n\t"
      "v_mfma_f32_16x16x32_bf16 v[36:39], a[84:87], a[164:167], v[36:39]\n\t"
      "v_mfma_f32_16x16x32_bf16 v[36:39], a[76:79], a[160:163], v[36:39]\n\t"
      "v_mfma_f32_16x16x32_bf16 v[44:47], a[76:79], a[164:167], v[44:47]\n\t"
      "v_mfma_f32_16x16x32_bf16 v[44:47], a[84:87], a[168:171], v[44:47]\n\t"
      "s_waitcnt vmcnt(5) lgkmcnt(0)\n\t"
      "s_barrier\n\t"
      "ds_read_b128 a[128:131], %[lx0] offset:3072\n\t"
      "v_mfma_f32_16x16x32_bf16 v[44:47], a[92:95], a[172:175], v[44:47]\n\t"
      "s_add_u32 m0, %[ismx], 45056\n\t"
      "v_mfma_f32_16x16x32_bf16 v[44:47], a[100:103], a[144:147], v[44:47]\n\t"
      "s_add_u32 s67, s76, 65536\n\t"
      "v_mfma_f32_16x16x32_bf16 v[44:47], a[108:111], a[148:151], v[44:47]\n\t"
      "buffer_load_dwordx4 %[vx], s[48:51], s67 offen lds\n\t"
      "v_mfma_f32_16x16x32_bf16 v[40:43], a[104:107], a[148:151], v[40:43]\n\t"
      "ds_read_b128 a[132:135], %[lx0] offset:3328\n\t"
      "v_mfma_f32_16x16x32_bf16 v[40:43], a[96:99], a[144:147], v[40:43]\n\t"
      "v_mfma_f32_16x16x32_bf16 v[40:43], a[88:91], a[172:175], v[40:43]\n\t"
      "s_add_u32 s71, s71, 128\n\t"
      "v_mfma_f32_16x16x32_bf16 v[40:43], a[80:83], a[168:171], v[40:43]\n\t"
      "s_add_u32 s72, s72, 131072\n\t"
      "v_mfma_f32_16x16x32_bf16 v[40:43], a[72:75], a[164:167], v[40:43]\n\t"
      "s_and_b32 s71, s71, 1023\n\t"
      "ds_read_b128 a[136:139], %[lx0] offset:3584\n\t"
      "v_mfma_f32_16x16x32_bf16 v[48:51], a[72:75], a[168:171], v[48:51]\n\t"
      "s_and_b32 s72, s72, 0xfffff\n\t"
      "v_mfma_f32_16x16x32_bf16 v[48:51], a[80:83], a[172:175], v[48:51]\n\t"
      "s_add_u32 s76, s71, s77\n\t"
      "v_mfma_f32_16x16x32_bf16 v[48:51], a[88:91], a[144:147], v[48:51]\n\t"
      "v_mfma_f32_16x16x32_bf16 v[48:51], a[96:99], a[148:151], v[48:51]\n\t"
      "ds_read_b128 a[140:143], %[lx0] offset:3840\n\t"
      "v_mfma_f32_16x16x32_bf16 v[48:51], a[104:107], a[152:155], v[48:51]\n\t"
      "v_mfma_f32_16x16x32_bf16 v[52:55], a[108:111], a[152:155], v[52:55]\n\t"
      "v_mfma_f32_16x16x32_bf16 v[52:55], a[100:103], a[148:151], v[52:55]\n\t"
      "v_mfma_f32_16x16x32_bf16 v[52:55], a[92:95], a[144:147], v[52:55]\n\t"
      "s_add_u32 m0, %[ismx], 0\n\t"
      "v_mfma_f32_16x16x32_bf16 v[52:55], a[84:87], a[172:175], v[52:55]\n\t"
      "s_add_u32 s67, s71, s74\n\t"
      "v_mfma_f32_16x16x32_bf16 v[52:55], a[76:79], a[168:171], v[52:55]\n\t"
      "buffer_load_dwordx4 %[vx], s[48:51], s67 offen lds\n\t"
      "v_mfma_f32_16x16x32_bf16 v[60:63], a[76:79], a[172:175], v[60:63]\n\t"
      "v_mfma_f32_16x16x32_bf16 v[60:63], a[84:87], a[144:147], v[60:63]\n\t"
      "v_mfma_f32_16x16x32_bf16 v[60:63], a[92:95], a[148:151], v[60:63]\n\t"
      "v_mfma_f32_16x16x32_bf16 v[60:63], a[100:103], a[152:155], v[60:63]\n\t"
      "v_mfma_f32_16x16x32_bf16 v[60:63], a[108:111], a[156:159], v[60:63]\n\t"
      "v_mfma_f32_16x16x32_bf16 v[56:59], a[104:107], a[156:159], v[56:59]\n\t"
      "v_mfma_f32_16x16x32_bf16 v[56:59], a[96:99], a[152:155], v[56:59]\n\t"
      "v_mfma_f32_16x16x32_bf16 v[56:59], a[88:91], a[148:151], v[56:59]\n\t"
      "v_mfma_f32_16x16x32_bf16 v[56:59], a[80:83], a[144:147], v[56:59]\n\t"
      "s_add_u32 m0, %[ismx], 36864\n\t"
      "v_mfma_f32_16x16x32_bf16 v[56:59], a[72:75], a[172:175], v[56:59]\n\t"
      "s_add_u32 s67, s76, 0\n\t"
      "v_mfma_f32_16x16x32_bf16 v[64:67], a[32:35], a[112:115], 0\n\t"
      "buffer_load_dwordx4 %[vx], s[48:51], s67 offen lds\n\t"
      "v_mfma_f32_16x16x32_bf16 v[64:67], a[40:43], a[116:119], v[64:67]\n\t"
      "v_mfma_f32_16x16x32_bf16 v[64:67], a[48:51], a[120:123], v[64:67]\n\t"
      "ds_read_b128 a[160:163], %[lx1] offset:3072\n\t"
      "v_mfma_f32_16x16x32_bf16 v[64:67], a[56:59], a[124:127], v[64:67]\n\t"
      "s_waitcnt lgkmcnt(4)\n\t"
      "v_mfma_f32_16x16x32_bf16 v[64:67], a[64:67], a[128:131], v[64:67]\n\t"
      "LJOIN_%=:\n\t"
      "v_mfma_f32_16x16x32_bf16 v[68:71], a[68:71], a[128:131], v[68:71]\n\t"
      "v_mfma_f32_16x16x32_bf16 v[68:71], a[60:63], a[124:127], v[68:71]\n\t"
      "v_mfma_f32_16x16x32_bf16 v[68:71], a[52:55], a[120:123], v[68:71]\n\t"
      "v_mfma_f32_16x16x32_bf16 v[68:71], a[44:47], a[116:119], v[68:71]\n\t"
      "ds_read_b128 a[164:167], %[lx1] offset:3328\n\t"
      "v_mfma_f32_16x16x32_bf16 v[68:71], a[36:39], a[112:115], v[68:71]\n\t"
      "v_mfma_f32_16x16x32_bf16 v[76:79], a[36:39], a[116:119], v[76:79]\n\t"
      "s_add_u32 m0, %[ismx], 1024\n\t"
      "v_mfma_f32_16x16x32_bf16 v[76:79], a[44:47], a[120:123], v[76:79]\n\t"
      "s_add_u32 s67, s71, 8192\n\t"
      "v_mfma_f32_16x16x32_bf16 v[76:79], a[52:55], a[124:127], v[76:79]\n\t"
      "buffer_load_dwordx4 %[vx], s[48:51], s67 offen lds\n\t"
      "v_mfma_f32_16x16x32_bf16 v[76:79], a[60:63], a[128:131], v[76:79]\n\t"
      "s_waitcnt lgkmcnt(4)\n\t"
      "v_mfma_f32_16x16x32_bf16 v[76:79], a[68:71], a[132:135], v[76:79]\n\t"
      "ds_read_b128 a[168:171], %[lx1] offset:3584\n\t"
      "v_mfma_f32_16x16x32_bf16 v[72:75], a[64:67], a[132:135], v[72:75]\n\t"
      "v_mfma_f32_16x16x32_bf16 v[72:75], a[56:59], a[128:131], v[72:75]\n\t"
      "v_accvgpr_read_b32 v232, a176\n\t"
      "v_mfma_f32_16x16x32_bf16 v[72:75], a[48:51], a[124:127], v[72:75]\n\t"
      "buffer_load_dword a178, v232, s[44:47], 0 offen\n\t"
      "v_mfma_f32_16x16x32_bf16 v[72:75], a[40:43], a[120:123], v[72:75]\n\t"
      "v_mfma_f32_16x16x32_bf16 v[72:75], a[32:35], a[116:119], v[72:75]\n\t"
      "v_mfma_f32_16x16x32_bf16 v[80:83], a[32:35], a[120:123], v[80:83]\n\t"
      "ds_read_b128 a[172:175], %[lx1] offset:3840\n\t"
      "v_mfma_f32_16x16x32_bf16 v[80:83], a[40:43], a[124:127], v[80:83]\n\t"
      "s_add_u32 m0, %[ismx], 37888\n\t"
      "v_mfma_f32_16x16x32_bf16 v[80:83], a[48:51], a[128:131], v[80:83]\n\t"
      "s_add_u32 s67, s76, 8192\n\t"
      "v_mfma_f32_16x16x32_bf16 v[80:83], a[56:59], a[132:135], v[80:83]\n\t"
      "s_waitcnt lgkmcnt(5)\n\t"
      "buffer_load_dwordx4 %[vx], s[48:51], s67 offen lds\n\t"
      "v_mfma_f32_16x16x32_bf16 v[80:83], a[64:67], a[136:139], v[80:83]\n\t"
      "v_mfma_f32_16x16x32_bf16 v[84:87], a[68:71], a[136:139], v[84:87]\n\t"
      "v_mfma_f32_16x16x32_bf16 v[84:87], a[60:63], a[132:135], v[84:87]\n\t"
      "v_mfma_f32_16x16x32_bf16 v[84:87], a[52:55], a[128:131], v[84:87]\n\t"
      "v_mfma_f32_16x16x32_bf16 v[84:87], a[44:47], a[124:127], v[84:87]\n\t"
      "v_mfma_f32_16x16x32_bf16 v[84:87], a[36:39], a[120:123], v[84:87]\n\t"
      "v_mfma_f32_16x16x32_bf16 v[92:95], a[36:39], a[124:127], v[92:95]\n\t"
      "v_mfma_f32_16x16x32_bf16 v[92:95], a[44:47], a[128:131], v[92:95]\n\t"
      "v_mfma_f32_16x16x32_bf16 v[92:95], a[52:55], a[132:135], v[92:95]\n\t"
      "v_mfma_f32_16x16x32_bf16 v[92:95], a[60:63], a[136:139], v[92:95]\n\t"
      "s_waitcnt lgkmcnt(4)\n\t"
      "s_add_u32 m0, %[ismx], 2048\n\t"
      "v_mfma_f32_16x16x32_bf16 v[92:95], a[68:71], a[140:143], v[92:95]\n\t"
      "s_add_u32 s67, s71, 16384\n\t"
      "v_mfma_f32_16x16x32_bf16 v[88:91], a[64:67], a[140:143], v[88:91]\n\t"
      "buffer_load_dwordx4 %[vx], s[48:51], s67 offen lds\n\t"
      "v_mfma_f32_16x16x32_bf16 v[88:91], a[56:59], a[136:139], v[88:91]\n\t"
      "v_mfma_f32_16x16x32_bf16 v[88:91], a[48:51], a[132:135], v[88:91]\n\t"
      "v_mfma_f32_16x16x32_bf16 v[88:91], a[40:43], a[128:131], v[88:91]\n\t"
      "v_mfma_f32_16x16x32_bf16 v[88:91], a[32:35], a[124:127], v[88:91]\n\t"
      "v_mfma_f32_16x16x32_bf16 v[64:67], a[72:75], a[144:147], v[64:67]\n\t"
      "v_mfma_f32_16x16x32_bf16 v[64:67], a[80:83], a[148:151], v[64:67]\n\t"
      "v_mfma_f32_16x16x32_bf16 v[64:67], a[88:91], a[152:155], v[64:67]\n\t"
      "ds_read_b128 a[112:115], %[lx0] offset:4096\n\t"
      "v_mfma_f32_16x16x32_bf16 v[64:67], a[96:99], a[156:159], v[64:67]\n\t"
      "s_waitcnt lgkmcnt(4)\n\t"
      "v_mfma_f32_16x16x32_bf16 v[64:67], a[104:107], a[160:163], v[64:67]\n\t"
      "v_mfma_f32_16x16x32_bf16 v[68:71], a[108:111], a[160:163], v[68:71]\n\t"
      "s_add_u32 m0, %[ismx], 38912\n\t"
      "v_mfma_f32_16x16x32_bf16 v[68:71], a[100:103], a[156:159], v[68:71]\n\t"
      "s_add_u32 s67, s76, 16384\n\t"
      "v_mfma_f32_16x16x32_bf16 v[68:71], a[92:95], a[152:155], v[68:71]\n\t"
      "buffer_load_dwordx4 %[vx], s[48:51], s67 offen lds\n\t"
      "v_mfma_f32_16x16x32_bf16 v[68:71], a[84:87], a[148:151], v[68:71]\n\t"
      "ds_read_b128 a[116:119], %[lx0] offset:4352\n\t"
      "v_mfma_f32_16x16x32_bf16 v[68:71], a[76:79], a[144:147], v[68:71]\n\t"
      "v_mfma_f32_16x16x32_bf16 v[76:79], a[76:79], a[148:151], v[76:79]\n\t"
      "v_mfma_f32_16x16x32_bf16 v[76:79], a[84:87], a[152:155], v[76:79]\n\t"
      "v_mfma_f32_16x16x32_bf16 v[76:79], a[92:95], a[156:159], v[76:79]\n\t"
      "v_mfma_f32_16x16x32_bf16 v[76:79], a[100:103], a[160:163], v[76:79]\n\t"
      "s_waitcnt lgkmcnt(4)\n\t"
      "v_mfma_f32_16x16x32_bf16 v[76:79], a[108:111], a[164:167], v[76:79]\n\t"
      "ds_read_b128 a[120:123], %[lx0] offset:4608\n\t"
      "v_mfma_f32_16x16x32_bf16 v[72:75], a[104:107], a[164:167], v[72:75]\n\t"
      "v_mfma_f32_16x16x32_bf16 v[72:75], a[96:99], a[160:163], v[72:75]\n\t"
      "s_add_u32 m0, %[ismw], 0\n\t"
      "v_mfma_f32_16x16x32_bf16 v[72:75], a[88:91], a[156:159], v[72:75]\n\t"
      "s_add_u32 s67, s72, 0\n\t"
      "v_mfma_f32_16x16x32_bf16 v[72:75], a[80:83], a[152:155], v[72:75]\n\t"
      "buffer_load_dwordx4 %[vw], s[56:59], s67 offen lds\n\t"
      "v_mfma_f32_16x16x32_bf16 v[72:75], a[72:75], a[148:151], v[72:75]\n\t"
      "v_mfma_f32_16x16x32_bf16 v[80:83], a[72:75], a[152:155], v[80:83]\n\t"
      "ds_read_b128 a[124:127], %[lx0] offset:4864\n\t"
      "v_mfma_f32_16x16x32_bf16 v[80:83], a[80:83], a[156:159], v[80:83]\n\t"
      "v_mfma_f32_16x16x32_bf16 v[80:83], a[88:91], a[160:163], v[80:83]\n\t"
      "v_mfma_f32_16x16x32_bf16 v[80:83], a[96:99], a[164:167], v[80:83]\n\t"
      "s_waitcnt lgkmcnt(5)\n\t"
      "v_mfma_f32_16x16x32_bf16 v[80:83], a[104:107], a[168:171], v[80:83]\n\t"
      "v_mfma_f32_16x16x32_bf16 v[84:87], a[108:111], a[168:171], v[84:87]\n\t"
      "v_mfma_f32_16x16x32_bf16 v[84:87], a[100:103], a[164:167], v[84:87]\n\t"
      "v_mfma_f32_16x16x32_bf16 v[84:87], a[92:95], a[160:163], v[84:87]\n\t"
      "v_mfma_f32_16x16x32_bf16 v[84:87], a[84:87], a[156:159], v[84:87]\n\t"
      "v_mfma_f32_16x16x32_bf16 v[84:87], a[76:79], a[152:155], v[84:87]\n\t"
      "v_mfma_f32_16x16x32_bf16 v[92:95], a[76:79], a[156:159], v[92:95]\n\t"
      "buffer_load_dwordx4 %[vw], s[56:59], s67 offen offset:1024 lds\n\t"
      "v_mfma_f32_16x16x32_bf16 v[92:95], a[84:87], a[160:163], v[92:95]\n\t"
      "v_mfma_f32_16x16x32_bf16 v[92:95], a[92:95], a[164:167], v[92:95]\n\t"
      "v_mfma_f32_16x16x32_bf16 v[92:95], a[100:103], a[168:171], v[92:95]\n\t"
      "s_waitcnt lgkmcnt(4)\n\t"
      "v_mfma_f32_16x16x32_bf16 v[92:95], a[108:111], a[172:175], v[92:95]\n\t"
      "v_mfma_f32_16x16x32_bf16 v[88:91], a[104:107], a[172:175], v[88:91]\n\t"
      "v_mfma_f32_16x16x32_bf16 v[88:91], a[96:99], a[168:171], v[88:91]\n\t"
      "v_mfma_f32_16x16x32_bf16 v[88:91], a[88:91], a[164:167], v[88:91]\n\t"
      "v_mfma_f32_16x16x32_bf16 v[88:91], a[80:83], a[160:163], v[88:91]\n\t"
      "v_mfma_f32_16x16x32_bf16 v[88:91], a[72:75], a[156:159], v[88:91]\n\t"
      "s_add_u32 m0, %[ismw], 8192\n\t"
      "v_mfma_f32_16x16x32_bf16 v[96:99], a[32:35], a[128:131], v[96:99]\n\t"
      "s_add_u32 s67, s72, 1048576\n\t"
      "v_mfma_f32_16x16x32_bf16 v[96:99], a[40:43], a[132:135], v[96:99]\n\t"
      "buffer_load_dwordx4 %[vw], s[56:59], s67 offen lds\n\t"
      "v_mfma_f32_16x16x32_bf16 v[96:99], a[48:51], a[136:139], v[96:99]\n\t"
      "ds_read_b128 a[144:147], %[lx1] offset:4096\n\t"
      "v_mfma_f32_16x16x32_bf16 v[96:99], a[56:59], a[140:143], v[96:99]\n\t"
      "s_waitcnt lgkmcnt(4)\n\t"
      "v_mfma_f32_16x16x32_bf16 v[96:99], a[64:67], a[112:115], v[96:99]\n\t"
      "v_mfma_f32_16x16x32_bf16 v[100:103], a[68:71], a[112:115], v[100:103]\n\t"
      "v_mfma_f32_16x16x32_bf16 v[100:103], a[60:63], a[140:143], v[100:103]\n\t"
      "v_mfma_f32_16x16x32_bf16 v[100:103], a[52:55], a[136:139], v[100:103]\n\t"
      "v_mfma_f32_16x16x32_bf16 v[100:103], a[44:47], a[132:135], v[100:103]\n\t"
      "ds_read_b128 a[148:151], %[lx1] offset:4352\n\t"
      "v_mfma_f32_16x16x32_bf16 v[100:103], a[36:39], a[128:131], v[100:103]\n\t"
      "v_mfma_f32_16x16x32_bf16 v[108:111], a[36:39], a[132:135], v[108:111]\n\t"
      "v_mfma_f32_16x16x32_bf16 v[108:111], a[44:47], a[136:139], v[108:111]\n\t"
      "v_mfma_f32_16x16x32_bf16 v[108:111], a[52:55], a[140:143], v[108:111]\n\t"
      "v_mfma_f32_16x16x32_bf16 v[108:111], a[60:63], a[112:115], v[108:111]\n\t"
      "s_waitcnt lgkmcnt(4)\n\t"
      "buffer_load_dwordx4 %[vw], s[56:59], s67 offen offset:1024 lds\n\t"
      "v_mfma_f32_16x16x32_bf16 v[108:111], a[68:71], a[116:119], v[108:111]\n\t"
      "ds_read_b128 a[152:155], %[lx1] offset:4608\n\t"
      "v_mfma_f32_16x16x32_bf16 v[104:107], a[64:67], a[116:119], v[104:107]\n\t"
      "v_mfma_f32_16x16x32_bf16 v[104:107], a[56:59], a[112:115], v[104:107]\n\t"
      "v_mfma_f32_16x16x32_bf16 v[104:107], a[48:51], a[140:143], v[104:107]\n\t"
      "v_mfma_f32_16x16x32_bf16 v[104:107], a[40:43], a[136:139], v[104:107]\n\t"
      "v_mfma_f32_16x16x32_bf16 v[104:107], a[32:35], a[132:135], v[104:107]\n\t"
      "v_mfma_f32_16x16x32_bf16 v[112:115], a[32:35], a[136:139], v[112:115]\n\t"
      "ds_read_b128 a[156:159], %[lx1] offset:4864\n\t"
      "v_mfma_f32_16x16x32_bf16 v[112:115], a[40:43], a[140:143], v[112:115]\n\t"
      "v_mfma_f32_16x16x32_bf16 v[112:115], a[48:51], a[112:115], v[112:115]\n\t"
      "v_mfma_f32_16x16x32_bf16 v[112:115], a[56:59], a[116:119], v[112:115]\n\t"
      "s_waitcnt lgkmcnt(5)\n\t"
      "s_add_u32 m0, %[ismw], 16384\n\t"
      "v_mfma_f32_16x16x32_bf16 v[112:115], a[64:67], a[120:123], v[112:115]\n\t"
      "s_add_u32 s67, s72, 2097152\n\t"
      "v_mfma_f32_16x16x32_bf16 v[116:119], a[68:71], a[120:123], v[116:119]\n\t"
      "buffer_load_dwordx4 %[vw], s[56:59], s67 offen lds\n\t"
      "v_mfma_f32_16x16x32_bf16 v[116:119], a[60:63], a[116:119], v[116:119]\n\t"
      "v_mfma_f32_16x16x32_bf16 v[116:119], a[52:55], a[112:115], v[116:119]\n\t"
      "v_mfma_f32_16x16x32_bf16 v[116:119], a[44:47], a[140:143], v[116:119]\n\t"
      "v_mfma_f32_16x16x32_bf16 v[116:119], a[36:39], a[136:139], v[116:119]\n\t"
      "v_mfma_f32_16x16x32_bf16 v[124:127], a[36:39], a[140:143], v[124:127]\n\t"
      "v_mfma_f32_16x16x32_bf16 v[124:127], a[44:47], a[112:115], v[124:127]\n\t"
      "v_mfma_f32_16x16x32_bf16 v[124:127], a[52:55], a[116:119], v[124:127]\n\t"
      "v_mfma_f32_16x16x32_bf16 v[124:127], a[60:63], a[120:123], v[124:127]\n\t"
      "s_waitcnt lgkmcnt(4)\n\t"
      "v_mfma_f32_16x16x32_bf16 v[124:127], a[68:71], a[124:127], v[124:127]\n\t"
      "v_mfma_f32_16x16x32_bf16 v[120:123], a[64:67], a[124:127], v[120:123]\n\t"
      "v_mfma_f32_16x16x32_bf16 v[120:123], a[56:59], a[120:123], v[120:123]\n\t"
      "buffer_load_dwordx4 %[vw], s[56:59], s67 offen offset:1024 lds\n\t"
      "v_mfma_f32_16x16x32_bf16 v[120:123], a[48:51], a[116:119], v[120:123]\n\t"
      "v_mfma_f32_16x16x32_bf16 v[120:123], a[40:43], a[112:115], v[120:123]\n\t"
      "v_mfma_f32_16x16x32_bf16 v[120:123], a[32:35], a[140:143], v[120:123]\n\t"
      "v_mfma_f32_16x16x32_bf16 v[96:99], a[72:75], a[160:163], v[96:99]\n\t"
      "v_mfma_f32_16x16x32_bf16 v[96:99], a[80:83], a[164:167], v[96:99]\n\t"
      "v_mfma_f32_16x16x32_bf16 v[96:99], a[88:91], a[168:171], v[96:99]\n\t"
      "ds_read_b128 a[128:131], %[lx0] offset:5120\n\t"
      "v_mfma_f32_16x16x32_bf16 v[96:99], a[96:99], a[172:175], v[96:99]\n\t"
      "s_waitcnt lgkmcnt(4)\n\t"
      "v_mfma_f32_16x16x32_bf16 v[96:99], a[104:107], a[144:147], v[96:99]\n\t"
      "v_mfma_f32_16x16x32_bf16 v[100:103], a[108:111], a[144:147], v[100:103]\n\t"
      "v_mfma_f32_16x16x32_bf16 v[100:103], a[100:103], a[172:175], v[100:103]\n\t"
      "v_mfma_f32_16x16x32_bf16 v[100:103], a[92:95], a[168:171], v[100:103]\n\t"
      "s_add_u32 m0, %[ismw], 24576\n\t"
      "v_mfma_f32_16x16x32_bf16 v[100:103], a[84:87], a[164:167], v[100:103]\n\t"
      "s_add_u32 s67, s72, 3145728\n\t"
      "ds_read_b128 a[132:135], %[lx0] offset:5376\n\t"
      "v_mfma_f32_16x16x32_bf16 v[100:103], a[76:79], a[160:163], v[100:103]\n\t"
      "buffer_load_dwordx4 %[vw], s[56:59], s67 offen lds\n\t"
      "v_mfma_f32_16x16x32_bf16 v[108:111], a[76:79], a[164:167], v[108:111]\n\t"
      "v_mfma_f32_16x16x32_bf16 v[108:111], a[84:87], a[168:171], v[108:111]\n\t"
      "v_mfma_f32_16x16x32_bf16 v[108:111], a[92:95], a[172:175], v[108:111]\n\t"
      "v_mfma_f32_16x16x32_bf16 v[108:111], a[100:103], a[144:147], v[108:111]\n\t"
      "s_waitcnt lgkmcnt(4)\n\t"
      "v_mfma_f32_16x16x32_bf16 v[108:111], a[108:111], a[148:151], v[108:111]\n\t"
      "ds_read_b128 a[136:139], %[lx0] offset:5632\n\t"
      "v_mfma_f32_16x16x32_bf16 v[104:107], a[104:107], a[148:151], v[104:107]\n\t"
      "v_mfma_f32_16x16x32_bf16 v[104:107], a[96:99], a[144:147], v[104:107]\n\t"
      "v_mfma_f32_16x16x32_bf16 v[104:107], a[88:91], a[172:175], v[104:107]\n\t"
      "v_mfma_f32_16x16x32_bf16 v[104:107], a[80:83], a[168:171], v[104:107]\n\t"
      "v_mfma_f32_16x16x32_bf16 v[104:107], a[72:75], a[164:167], v[104:107]\n\t"
      "v_mfma_f32_16x16x32_bf16 v[112:115], a[72:75], a[168:171], v[112:115]\n\t"
      "s_sub_u32 s69, s69, 1\n\t"
      "ds_read_b128 a[140:143], %[lx0] offset:5888\n\t"
      "v_mfma_f32_16x16x32_bf16 v[112:115], a[80:83], a[172:175], v[112:115]\n\t"
      "buffer_load_dwordx4 %[vw], s[56:59], s67 offen offset:1024 lds\n\t"
      "v_mfma_f32_16x16x32_bf16 v[112:115], a[88:91], a[144:147], v[112:115]\n\t"
      "v_mfma_f32_16x16x32_bf16 v[112:115], a[96:99], a[148:151], v[112:115]\n\t"
      "s_waitcnt lgkmcnt(5)\n\t"
      "v_mfma_f32_16x16x32_bf16 v[112:115], a[104:107], a[152:155], v[112:115]\n\t"
      "v_mfma_f32_16x16x32_bf16 v[116:119], a[108:111], a[152:155], v[116:119]\n\t"
      "v_mfma_f32_16x16x32_bf16 v[116:119], a[100:103], a[148:151], v[116:119]\n\t"
      "v_mfma_f32_16x16x32_bf16 v[116:119], a[92:95], a[144:147], v[116:119]\n\t"
      "v_mfma_f32_16x16x32_bf16 v[116:119], a[84:87], a[172:175], v[116:119]\n\t"
      "v_mfma_f32_16x16x32_bf16 v[116:119], a[76:79], a[168:171], v[116:119]\n\t"
      "s_add_u32 m0, %[ismw], 32768\n\t"
      "v_mfma_f32_16x16x32_bf16 v[124:127], a[76:79], a[172:175], v[124:127]\n\t"
      "s_add_u32 s67, s72, 4194304\n\t"
      "v_mfma_f32_16x16x32_bf16 v[124:127], a[84:87], a[144:147], v[124:127]\n\t"
      "buffer_load_dwordx4 %[vw], s[56:59], s67 offen lds\n\t"
      "v_mfma_f32_16x16x32_bf16 v[124:127], a[92:95], a[148:151], v[124:127]\n\t"
      "v_mfma_f32_16x16x32_bf16 v[124:127], a[100:103], a[152:155], v[124:127]\n\t"
      "s_waitcnt lgkmcnt(4)\n\t"
      "v_mfma_f32_16x16x32_bf16 v[124:127], a[108:111], a[156:159], v[124:127]\n\t"
      "v_mfma_f32_16x16x32_bf16 v[120:123], a[104:107], a[156:159], v[120:123]\n\t"
      "v_mfma_f32_16x16x32_bf16 v[120:123], a[96:99], a[152:155], v[120:123]\n\t"
      "v_mfma_f32_16x16x32_bf16 v[120:123], a[88:91], a[148:151], v[120:123]\n\t"
      "v_mfma_f32_16x16x32_bf16 v[120:123], a[80:83], a[144:147], v[120:123]\n\t"
      "v_mfma_f32_16x16x32_bf16 v[120:123], a[72:75], a[172:175], v[120:123]\n\t"
      "v_mfma_f32_16x16x32_bf16 v[128:131], a[32:35], a[112:115], v[128:131]\n\t"
      "v_mfma_f32_16x16x32_bf16 v[128:131], a[40:43], a[116:119], v[128:131]\n\t"
      "v_mfma_f32_16x16x32_bf16 v[128:131], a[48:51], a[120:123], v[128:131]\n\t"
      "ds_read_b128 a[160:163], %[lx1] offset:5120\n\t"
      "v_mfma_f32_16x16x32_bf16 v[128:131], a[56:59], a[124:127], v[128:131]\n\t"
      "s_waitcnt lgkmcnt(4)\n\t"
      "buffer_load_dwordx4 %[vw], s[56:59], s67 offen offset:1024 lds\n\t"
      "v_mfma_f32_16x16x32_bf16 v[128:131], a[64:67], a[128:131], v[128:131]\n\t"
      "v_mfma_f32_16x16x32_bf16 v[132:135], a[68:71], a[128:131], v[132:135]\n\t"
      "v_mfma_f32_16x16x32_bf16 v[132:135], a[60:63], a[124:127], v[132:135]\n\t"
      "v_mfma_f32_16x16x32_bf16 v[132:135], a[52:55], a[120:123], v[132:135]\n\t"
      "v_mfma_f32_16x16x32_bf16 v[132:135], a[44:47], a[116:119], v[132:135]\n\t"
      "ds_read_b128 a[164:167], %[lx1] offset:5376\n\t"
      "v_mfma_f32_16x16x32_bf16 v[132:135], a[36:39], a[112:115], v[132:135]\n\t"
      "v_mfma_f32_16x16x32_bf16 v[140:143], a[36:39], a[116:119], v[140:143]\n\t"
      "v_mfma_f32_16x16x32_bf16 v[140:143], a[44:47], a[120:123], v[140:143]\n\t"
      "v_mfma_f32_16x16x32_bf16 v[140:143], a[52:55], a[124:127], v[140:143]\n\t"
      "v_mfma_f32_16x16x32_bf16 v[140:143], a[60:63], a[128:131], v[140:143]\n\t"
      "s_waitcnt lgkmcnt(4)\n\t"
      "s_add_u32 m0, %[ismw], 40960\n\t"
      "v_mfma_f32_16x16x32_bf16 v[140:143], a[68:71], a[132:135], v[140:143]\n\t"
      "s_add_u32 s67, s72, 65536\n\t"
      "ds_read_b128 a[168:171], %[lx1] offset:5632\n\t"
      "v_mfma_f32_16x16x32_bf16 v[136:139], a[64:67], a[132:135], v[136:139]\n\t"
      "buffer_load_dwordx4 %[vw], s[56:59], s67 offen lds\n\t"
      "v_mfma_f32_16x16x32_bf16 v[136:139], a[56:59], a[128:131], v[136:139]\n\t"
      "v_mfma_f32_16x16x32_bf16 v[136:139], a[48:51], a[124:127], v[136:139]\n\t"
      "v_mfma_f32_16x16x32_bf16 v[136:139], a[40:43], a[120:123], v[136:139]\n\t"
      "v_mfma_f32_16x16x32_bf16 v[136:139], a[32:35], a[116:119], v[136:139]\n\t"
      "v_mfma_f32_16x16x32_bf16 v[144:147], a[32:35], a[120:123], v[144:147]\n\t"
      "ds_read_b128 a[172:175], %[lx1] offset:5888\n\t"
      "v_mfma_f32_16x16x32_bf16 v[144:147], a[40:43], a[124:127], v[144:147]\n\t"
      "v_mfma_f32_16x16x32_bf16 v[144:147], a[48:51], a[128:131], v[144:147]\n\t"
      "v_mfma_f32_16x16x32_bf16 v[144:147], a[56:59], a[132:135], v[144:147]\n\t"
      "s_waitcnt lgkmcnt(5)\n\t"
      "v_mfma_f32_16x16x32_bf16 v[144:147], a[64:67], a[136:139], v[144:147]\n\t"
      "v_mfma_f32_16x16x32_bf16 v[148:151], a[68:71], a[136:139], v[148:151]\n\t"
      "v_mfma_f32_16x16x32_bf16 v[148:151], a[60:63], a[132:135], v[148:151]\n\t"
      "buffer_load_dwordx4 %[vw], s[56:59], s67 offen offset:1024 lds\n\t"
      "v_mfma_f32_16x16x32_bf16 v[148:151], a[52:55], a[128:131], v[148:151]\n\t"
      "v_mfma_f32_16x16x32_bf16 v[148:151], a[44:47], a[124:127], v[148:151]\n\t"
      "v_mfma_f32_16x16x32_bf16 v[148:151], a[36:39], a[120:123], v[148:151]\n\t"
      "v_mfma_f32_16x16x32_bf16 v[156:159], a[36:39], a[124:127], v[156:159]\n\t"
      "v_mfma_f32_16x16x32_bf16 v[156:159], a[44:47], a[128:131], v[156:159]\n\t"
      "v_mfma_f32_16x16x32_bf16 v[156:159], a[52:55], a[132:135], v[156:159]\n\t"
      "v_mfma_f32_16x16x32_bf16 v[156:159], a[60:63], a[136:139], v[156:159]\n\t"
      "s_waitcnt lgkmcnt(4)\n\t"
      "v_mfma_f32_16x16x32_bf16 v[156:159], a[68:71], a[140:143], v[156:159]\n\t"
      "v_mfma_f32_16x16x32_bf16 v[152:155], a[64:67], a[140:143], v[152:155]\n\t"
      "v_mfma_f32_16x16x32_bf16 v[152:155], a[56:59], a[136:139], v[152:155]\n\t"
      "s_add_u32 m0, %[ismw], 49152\n\t"
      "v_mfma_f32_16x16x32_bf16 v[152:155], a[48:51], a[132:135], v[152:155]\n\t"
      "s_add_u32 s67, s72, 1114112\n\t"
      "v_mfma_f32_16x16x32_bf16 v[152:155], a[40:43], a[128:131], v[152:155]\n\t"
      "buffer_load_dwordx4 %[vw], s[56:59], s67 offen lds\n\t"
      "v_mfma_f32_16x16x32_bf16 v[152:155], a[32:35], a[124:127], v[152:155]\n\t"
      "s_waitcnt vmcnt(20) lgkmcnt(0)\n\t"
      "s_barrier\n\t"
      "v_mfma_f32_16x16x32_bf16 v[128:131], a[72:75], a[144:147], v[128:131]\n\t"
      "v_mfma_f32_16x16x32_bf16 v[128:131], a[80:83], a[148:151], v[128:131]\n\t"
      "v_mfma_f32_16x16x32_bf16 v[128:131], a[88:91], a[152:155], v[128:131]\n\t"
      "ds_read_b128 a[112:115], %[lx0] offset:6144\n\t"
      "v_mfma_f32_16x16x32_bf16 v[128:131], a[96:99], a[156:159], v[128:131]\n\t"
      "v_mfma_f32_16x16x32_bf16 v[128:131], a[104:107], a[160:163], v[128:131]\n\t"
      "v_mfma_f32_16x16x32_bf16 v[132:135], a[108:111], a[160:163], v[132:135]\n\t"
      "v_mfma_f32_16x16x32_bf16 v[132:135], a[100:103], a[156:159], v[132:135]\n\t"
      "v_mfma_f32_16x16x32_bf16 v[132:135], a[92:95], a[152:155], v[132:135]\n\t"
      "v_mfma_f32_16x16x32_bf16 v[132:135], a[84:87], a[148:151], v[132:135]\n\t"
      "ds_read_b128 a[116:119], %[lx0] offset:6400\n\t"
      "v_mfma_f32_16x16x32_bf16 v[132:135], a[76:79], a[144:147], v[132:135]\n\t"
      "buffer_load_dwordx4 %[vw], s[56:59], s67 offen offset:1024 lds\n\t"
      "v_mfma_f32_16x16x32_bf16 v[140:143], a[76:79], a[148:151], v[140:143]\n\t"
      "v_mfma_f32_16x16x32_bf16 v[140:143], a[84:87], a[152:155], v[140:143]\n\t"
      "v_mfma_f32_16x16x32_bf16 v[140:143], a[92:95], a[156:159], v[140:143]\n\t"
      "v_mfma_f32_16x16x32_bf16 v[140:143], a[100:103], a[160:163], v[140:143]\n\t"
      "v_mfma_f32_16x16x32_bf16 v[140:143], a[108:111], a[164:167], v[140:143]\n\t"
      "ds_read_b128 a[120:123], %[lx0] offset:6656\n\t"
      "v_mfma_f32_16x16x32_bf16 v[136:139], a[104:107], a[164:167], v[136:139]\n\t"
      "v_mfma_f32_16x16x32_bf16 v[136:139], a[96:99], a[160:163], v[136:139]\n\t"
      "v_mfma_f32_16x16x32_bf16 v[136:139], a[88:91], a[156:159], v[136:139]\n\t"
      "v_mfma_f32_16x16x32_bf16 v[136:139], a[80:83], a[152:155], v[136:139]\n\t"
      "v_mfma_f32_16x16x32_bf16 v[136:139], a[72:75], a[148:151], v[136:139]\n\t"
      "s_add_u32 m0, %[ismw], 57344\n\t"
      "v_mfma_f32_16x16x32_bf16 v[144:147], a[72:75], a[152:155], v[144:147]\n\t"
      "s_add_u32 s67, s72, 2162688\n\t"
      "ds_read_b128 a[124:127], %[lx0] offset:6912\n\t"
      "v_mfma_f32_16x16x32_bf16 v[144:147], a[80:83], a[156:159], v[144:147]\n\t"
      "buffer_load_dwordx4 %[vw], s[56:59], s67 offen lds\n\t"
      "v_mfma_f32_16x16x32_bf16 v[144:147], a[88:91], a[160:163], v[144:147]\n\t"
      "v_mfma_f32_16x16x32_bf16 v[144:147], a[96:99], a[164:167], v[144:147]\n\t"
      "v_mfma_f32_16x16x32_bf16 v[144:147], a[104:107], a[168:171], v[144:147]\n\t"
      "v_mfma_f32_16x16x32_bf16 v[148:151], a[108:111], a[168:171], v[148:151]\n\t"
      "v_mfma_f32_16x16x32_bf16 v[148:151], a[100:103], a[164:167], v[148:151]\n\t"
      "v_mfma_f32_16x16x32_bf16 v[148:151], a[92:95], a[160:163], v[148:151]\n\t"
      "v_mfma_f32_16x16x32_bf16 v[148:151], a[84:87], a[156:159], v[148:151]\n\t"
      "v_mfma_f32_16x16x32_bf16 v[148:151], a[76:79], a[152:155], v[148:151]\n\t"
      "v_mfma_f32_16x16x32_bf16 v[156:159], a[76:79], a[156:159], v[156:159]\n\t"
      "v_mfma_f32_16x16x32_bf16 v[156:159], a[84:87], a[160:163], v[156:159]\n\t"
      "v_mfma_f32_16x16x32_bf16 v[156:159], a[92:95], a[164:167], v[156:159]\n\t"
      "buffer_load_dwordx4 %[vw], s[56:59], s67 offen offset:1024 lds\n\t"
      "v_mfma_f32_16x16x32_bf16 v[156:159], a[100:103], a[168:171], v[156:159]\n\t"
      "v_mfma_f32_16x16x32_bf16 v[156:159], a[108:111], a[172:175], v[156:159]\n\t"
      "v_mfma_f32_16x16x32_bf16 v[152:155], a[104:107], a[172:175], v[152:155]\n\t"
      "v_mfma_f32_16x16x32_bf16 v[152:155], a[96:99], a[168:171], v[152:155]\n\t"
      "v_mfma_f32_16x16x32_bf16 v[152:155], a[88:91], a[164:167], v[152:155]\n\t"
      "v_mfma_f32_16x16x32_bf16 v[152:155], a[80:83], a[160:163], v[152:155]\n\t"
      "v_mfma_f32_16x16x32_bf16 v[152:155], a[72:75], a[156:159], v[152:155]\n\t"
      "v_mfma_f32_16x16x32_bf16 v[160:163], a[32:35], a[128:131], v[160:163]\n\t"
      "v_mfma_f32_16x16x32_bf16 v[160:163], a[40:43], a[132:135], v[160:163]\n\t"
      "v_mfma_f32_16x16x32_bf16 v[160:163], a[48:51], a[136:139], v[160:163]\n\t"
      "s_add_u32 m0, %[ismw], 65536\n\t"
      "ds_read_b128 a[144:147], %[lx1] offset:6144\n\t"
      "v_mfma_f32_16x16x32_bf16 v[160:163], a[56:59], a[140:143], v[160:163]\n\t"
      "s_waitcnt lgkmcnt(4)\n\t"
      "s_add_u32 s67, s72, 3211264\n\t"
      "v_mfma_f32_16x16x32_bf16 v[160:163], a[64:67], a[112:115], v[160:163]\n\t"
      "buffer_load_dwordx4 %[vw], s[56:59], s67 offen lds\n\t"
      "v_mfma_f32_16x16x32_bf16 v[164:167], a[68:71], a[112:115], v[164:167]\n\t"
      "v_mfma_f32_16x16x32_bf16 v[164:167], a[60:63], a[140:143], v[164:167]\n\t"
      "v_mfma_f32_16x16x32_bf16 v[164:167], a[52:55], a[136:139], v[164:167]\n\t"
      "v_mfma_f32_16x16x32_bf16 v[164:167], a[44:47], a[132:135], v[164:167]\n\t"
      "ds_read_b128 a[148:151], %[lx1] offset:6400\n\t"
      "v_mfma_f32_16x16x32_bf16 v[164:167], a[36:39], a[128:131], v[164:167]\n\t"
      "v_mfma_f32_16x16x32_bf16 v[172:175], a[36:39], a[132:135], v[172:175]\n\t"
      "v_mfma_f32_16x16x32_bf16 v[172:175], a[44:47], a[136:139], v[172:175]\n\t"
      "v_mfma_f32_16x16x32_bf16 v[172:175], a[52:55], a[140:143], v[172:175]\n\t"
      "v_mfma_f32_16x16x32_bf16 v[172:175], a[60:63], a[112:115], v[172:175]\n\t"
      "s_waitcnt lgkmcnt(4)\n\t"
      "v_mfma_f32_16x16x32_bf16 v[172:175], a[68:71], a[116:119], v[172:175]\n\t"
      "ds_read_b128 a[152:155], %[lx1] offset:6656\n\t"
      "v_mfma_f32_16x16x32_bf16 v[168:171], a[64:67], a[116:119], v[168:171]\n\t"
      "v_mfma_f32_16x16x32_bf16 v[168:171], a[56:59], a[112:115], v[168:171]\n\t"
      "buffer_load_dwordx4 %[vw], s[56:59], s67 offen offset:1024 lds\n\t"
      "v_mfma_f32_16x16x32_bf16 v[168:171], a[48:51], a[140:143], v[168:171]\n\t"
      "v_mfma_f32_16x16x32_bf16 v[168:171], a[40:43], a[136:139], v[168:171]\n\t"
      "v_mfma_f32_16x16x32_bf16 v[168:171], a[32:35], a[132:135], v[168:171]\n\t"
      "v_mfma_f32_16x16x32_bf16 v[176:179], a[32:35], a[136:139], v[176:179]\n\t"
      "ds_read_b128 a[156:159], %[lx1] offset:6912\n\t"
      "v_mfma_f32_16x16x32_bf16 v[176:179], a[40:43], a[140:143], v[176:179]\n\t"
      "v_mfma_f32_16x16x32_bf16 v[176:179], a[48:51], a[112:115], v[176:179]\n\t"
      "v_mfma_f32_16x16x32_bf16 v[176:179], a[56:59], a[116:119], v[176:179]\n\t"
      "s_waitcnt lgkmcnt(5)\n\t"
      "v_mfma_f32_16x16x32_bf16 v[176:179], a[64:67], a[120:123], v[176:179]\n\t"
      "v_mfma_f32_16x16x32_bf16 v[180:183], a[68:71], a[120:123], v[180:183]\n\t"
      "s_add_u32 m0, %[ismw], 73728\n\t"
      "v_mfma_f32_16x16x32_bf16 v[180:183], a[60:63], a[116:119], v[180:183]\n\t"
      "s_add_u32 s67, s72, 4259840\n\t"
      "v_mfma_f32_16x16x32_bf16 v[180:183], a[52:55], a[112:115], v[180:183]\n\t"
      "buffer_load_dwordx4 %[vw], s[56:59], s67 offen lds\n\t"
      "v_mfma_f32_16x16x32_bf16 v[180:183], a[44:47], a[140:143], v[180:183]\n\t"
      "v_mfma_f32_16x16x32_bf16 v[180:183], a[36:39], a[136:139], v[180:183]\n\t"
      "v_mfma_f32_16x16x32_bf16 v[188:191], a[36:39], a[140:143], v[188:191]\n\t"
      "v_mfma_f32_16x16x32_bf16 v[188:191], a[44:47], a[112:115], v[188:191]\n\t"
      "v_mfma_f32_16x16x32_bf16 v[188:191], a[52:55], a[116:119], v[188:191]\n\t"
      "v_mfma_f32_16x16x32_bf16 v[188:191], a[60:63], a[120:123], v[188:191]\n\t"
      "s_waitcnt lgkmcnt(4)\n\t"
      "v_mfma_f32_16x16x32_bf16 v[188:191], a[68:71], a[124:127], v[188:191]\n\t"
      "v_mfma_f32_16x16x32_bf16 v[184:187], a[64:67], a[124:127], v[184:187]\n\t"
      "v_mfma_f32_16x16x32_bf16 v[184:187], a[56:59], a[120:123], v[184:187]\n\t"
      "v_mfma_f32_16x16x32_bf16 v[184:187], a[48:51], a[116:119], v[184:187]\n\t"
      "v_mfma_f32_16x16x32_bf16 v[184:187], a[40:43], a[112:115], v[184:187]\n\t"
      "v_mfma_f32_16x16x32_bf16 v[184:187], a[32:35], a[140:143], v[184:187]\n\t"
      "buffer_load_dwordx4 %[vw], s[56:59], s67 offen offset:1024 lds\n\t"
      "v_mfma_f32_16x16x32_bf16 v[160:163], a[72:75], a[160:163], v[160:163]\n\t"
      "v_mfma_f32_16x16x32_bf16 v[160:163], a[80:83], a[164:167], v[160:163]\n\t"
      "v_mfma_f32_16x16x32_bf16 v[160:163], a[88:91], a[168:171], v[160:163]\n\t"
      "ds_read_b128 a[128:131], %[lx0] offset:7168\n\t"
      "v_mfma_f32_16x16x32_bf16 v[160:163], a[96:99], a[172:175], v[160:163]\n\t"
      "s_waitcnt lgkmcnt(4)\n\t"
      "v_mfma_f32_16x16x32_bf16 v[160:163], a[104:107], a[144:147], v[160:163]\n\t"
      "v_mfma_f32_16x16x32_bf16 v[164:167], a[108:111], a[144:147], v[164:167]\n\t"
      "v_mfma_f32_16x16x32_bf16 v[164:167], a[100:103], a[172:175], v[164:167]\n\t"
      "v_mfma_f32_16x16x32_bf16 v[164:167], a[92:95], a[168:171], v[164:167]\n\t"
      "v_mfma_f32_16x16x32_bf16 v[164:167], a[84:87], a[164:167], v[164:167]\n\t"
      "ds_read_b128 a[132:135], %[lx0] offset:7424\n\t"
      "v_mfma_f32_16x16x32_bf16 v[164:167], a[76:79], a[160:163], v[164:167]\n\t"
      "v_mfma_f32_16x16x32_bf16 v[172:175], a[76:79], a[164:167], v[172:175]\n\t"
      "v_mfma_f32_16x16x32_bf16 v[172:175], a[84:87], a[168:171], v[172:175]\n\t"
      "v_mfma_f32_16x16x32_bf16 v[172:175], a[92:95], a[172:175], v[172:175]\n\t"
      "v_mfma_f32_16x16x32_bf16 v[172:175], a[100:103], a[144:147], v[172:175]\n\t"
      "s_waitcnt lgkmcnt(4)\n\t"
      "s_add_u32 m0, %[ismx], 3072\n\t"
      "v_mfma_f32_16x16x32_bf16 v[172:175], a[108:111], a[148:151], v[172:175]\n\t"
      "s_add_u32 s67, s71, 24576\n\t"
      "ds_read_b128 a[136:139], %[lx0] offset:7680\n\t"
      "v_mfma_f32_16x16x32_bf16 v[168:171], a[104:107], a[148:151], v[168:171]\n\t"
      "buffer_load_dwordx4 %[vx], s[48:51], s67 offen lds\n\t"
      "v_mfma_f32_16x16x32_bf16 v[168:171], a[96:99], a[144:147], v[168:171]\n\t"
      "v_mfma_f32_16x16x32_bf16 v[168:171], a[88:91], a[172:175], v[168:171]\n\t"
      "v_mfma_f32_16x16x32_bf16 v[168:171], a[80:83], a[168:171], v[168:171]\n\t"
      "v_mfma_f32_16x16x32_bf16 v[168:171], a[72:75], a[164:167], v[168:171]\n\t"
      "v_mfma_f32_16x16x32_bf16 v[176:179], a[72:75], a[168:171], v[176:179]\n\t"
      "ds_read_b128 a[140:143], %[lx0] offset:7936\n\t"
      "v_mfma_f32_16x16x32_bf16 v[176:179], a[80:83], a[172:175], v[176:179]\n\t"
      "v_mfma_f32_16x16x32_bf16 v[176:179], a[88:91], a[144:147], v[176:179]\n\t"
      "v_mfma_f32_16x16x32_bf16 v[176:179], a[96:99], a[148:151], v[176:179]\n\t"
      "s_waitcnt lgkmcnt(5)\n\t"
      "v_mfma_f32_16x16x32_bf16 v[176:179], a[104:107], a[152:155], v[176:179]\n\t"
      "v_mfma_f32_16x16x32_bf16 v[180:183], a[108:111], a[152:155], v[180:183]\n\t"
      "v_mfma_f32_16x16x32_bf16 v[180:183], a[100:103], a[148:151], v[180:183]\n\t"
      "v_mfma_f32_16x16x32_bf16 v[180:183], a[92:95], a[144:147], v[180:183]\n\t"
      "v_mfma_f32_16x16x32_bf16 v[180:183], a[84:87], a[172:175], v[180:183]\n\t"
      "v_mfma_f32_16x16x32_bf16 v[180:183], a[76:79], a[168:171], v[180:183]\n\t"
      "s_add_u32 m0, %[ismx], 39936\n\t"
      "v_mfma_f32_16x16x32_bf16 v[188:191], a[76:79], a[172:175], v[188:191]\n\t"
      "s_add_u32 s67, s76, 24576\n\t"
      "v_mfma_f32_16x16x32_bf16 v[188:191], a[84:87], a[144:147], v[188:191]\n\t"
      "buffer_load_dwordx4 %[vx], s[48:51], s67 offen lds\n\t"
      "v_mfma_f32_16x16x32_bf16 v[188:191], a[92:95], a[148:151], v[188:191]\n\t"
      "v_mfma_f32_16x16x32_bf16 v[188:191], a[100:103], a[152:155], v[188:191]\n\t"
      "s_waitcnt lgkmcnt(4)\n\t"
      "v_mfma_f32_16x16x32_bf16 v[188:191], a[108:111], a[156:159], v[188:191]\n\t"
      "v_mfma_f32_16x16x32_bf16 v[184:187], a[104:107], a[156:159], v[184:187]\n\t"
      "v_mfma_f32_16x16x32_bf16 v[184:187], a[96:99], a[152:155], v[184:187]\n\t"
      "v_mfma_f32_16x16x32_bf16 v[184:187], a[88:91], a[148:151], v[184:187]\n\t"
      "v_mfma_f32_16x16x32_bf16 v[184:187], a[80:83], a[144:147], v[184:187]\n\t"
      "v_mfma_f32_16x16x32_bf16 v[184:187], a[72:75], a[172:175], v[184:187]\n\t"
      "v_mfma_f32_16x16x32_bf16 v[192:195], a[32:35], a[112:115], v[192:195]\n\t"
      "v_mfma_f32_16x16x32_bf16 v[192:195], a[40:43], a[116:119], v[192:195]\n\t"
      "v_mfma_f32_16x16x32_bf16 v[192:195], a[48:51], a[120:123], v[192:195]\n\t"
      "v_mfma_f32_16x16x32_bf16 v[192:195], a[56:59], a[124:127], v[192:195]\n\t"
      "s_waitcnt lgkmcnt(3)\n\t"
      "v_mfma_f32_16x16x32_bf16 v[192:195], a[64:67], a[128:131], v[192:195]\n\t"
      "ds_read_b128 a[160:163], %[lx1] offset:7168\n\t"
      "v_mfma_f32_16x16x32_bf16 v[196:199], a[68:71], a[128:131], v[196:199]\n\t"
      "v_mfma_f32_16x16x32_bf16 v[196:199], a[60:63], a[124:127], v[196:199]\n\t"
      "v_mfma_f32_16x16x32_bf16 v[196:199], a[52:55], a[120:123], v[196:199]\n\t"
      "s_add_u32 m0, %[ismx], 4096\n\t"
      "v_mfma_f32_16x16x32_bf16 v[196:199], a[44:47], a[116:119], v[196:199]\n\t"
      "s_add_u32 s67, s71, 32768\n\t"
      "v_mfma_f32_16x16x32_bf16 v[196:199], a[36:39], a[112:115], v[196:199]\n\t"
      "buffer_load_dwordx4 %[vx], s[48:51], s67 offen lds\n\t"
      "LJOIN2_%=:\n\t"
      "v_mfma_f32_16x16x32_bf16 v[204:207], a[36:39], a[116:119], v[204:207]\n\t"
      "ds_read_b128 a[112:115], %[lx0] offset:8192\n\t"
      "v_mfma_f32_16x16x32_bf16 v[204:207], a[44:47], a[120:123], v[204:207]\n\t"
      "v_mfma_f32_16x16x32_bf16 v[204:207], a[52:55], a[124:127], v[204:207]\n\t"
      "v_mfma_f32_16x16x32_bf16 v[204:207], a[60:63], a[128:131], v[204:207]\n\t"
      "s_waitcnt lgkmcnt(4)\n\t"
      "v_mfma_f32_16x16x32_bf16 v[204:207], a[68:71], a[132:135], v[204:207]\n\t"
      "ds_read_b128 a[164:167], %[lx1] offset:7424\n\t"
      "v_mfma_f32_16x16x32_bf16 v[200:203], a[64:67], a[132:135], v[200:203]\n\t"
      "v_mfma_f32_16x16x32_bf16 v[200:203], a[56:59], a[128:131], v[200:203]\n\t"
      "v_mfma_f32_16x16x32_bf16 v[200:203], a[48:51], a[124:127], v[200:203]\n\t"
      "v_mfma_f32_16x16x32_bf16 v[200:203], a[40:43], a[120:123], v[200:203]\n\t"
      "v_mfma_f32_16x16x32_bf16 v[200:203], a[32:35], a[116:119], v[200:203]\n\t"
      "v_mfma_f32_16x16x32_bf16 v[208:211], a[32:35], a[120:123], v[208:211]\n\t"
      "ds_read_b128 a[116:119], %[lx0] offset:8448\n\t"
      "v_mfma_f32_16x16x32_bf16 v[208:211], a[40:43], a[124:127], v[208:211]\n\t"
      "v_mfma_f32_16x16x32_bf16 v[208:211], a[48:51], a[128:131], v[208:211]\n\t"
      "v_mfma_f32_16x16x32_bf16 v[208:211], a[56:59], a[132:135], v[208:211]\n\t"
      "s_waitcnt lgkmcnt(5)\n\t"
      "v_mfma_f32_16x16x32_bf16 v[208:211], a[64:67], a[136:139], v[208:211]\n\t"
      "s_add_u32 m0, %[ismx], 40960\n\t"
      "ds_read_b128 a[168:171], %[lx1] offset:7680\n\t"
      "v_mfma_f32_16x16x32_bf16 v[212:215], a[68:71], a[136:139], v[212:215]\n\t"
      "s_add_u32 s67, s76, 32768\n\t"
      "v_mfma_f32_16x16x32_bf16 v[212:215], a[60:63], a[132:135], v[212:215]\n\t"
      "buffer_load_dwordx4 %[vx], s[48:51], s67 offen lds\n\t"
      "v_mfma_f32_16x16x32_bf16 v[212:215], a[52:55], a[128:131], v[212:215]\n\t"
      "v_mfma_f32_16x16x32_bf16 v[212:215], a[44:47], a[124:127], v[212:215]\n\t"
      "v_mfma_f32_16x16x32_bf16 v[212:215], a[36:39], a[120:123], v[212:215]\n\t"
      "v_mfma_f32_16x16x32_bf16 v[220:223], a[36:39], a[124:127], v[220:223]\n\t"
      "ds_read_b128 a[120:123], %[lx0] offset:8704\n\t"
      "v_mfma_f32_16x16x32_bf16 v[220:223], a[44:47], a[128:131], v[220:223]\n\t"
      "v_mfma_f32_16x16x32_bf16 v[220:223], a[52:55], a[132:135], v[220:223]\n\t"
      "v_mfma_f32_16x16x32_bf16 v[220:223], a[60:63], a[136:139], v[220:223]\n\t"
      "s_waitcnt lgkmcnt(6)\n\t"
      "v_mfma_f32_16x16x32_bf16 v[220:223], a[68:71], a[140:143], v[220:223]\n\t"
      "ds_read_b128 a[172:175], %[lx1] offset:7936\n\t"
      "v_mfma_f32_16x16x32_bf16 v[216:219], a[64:67], a[140:143], v[216:219]\n\t"
      "v_mfma_f32_16x16x32_bf16 v[216:219], a[56:59], a[136:139], v[216:219]\n\t"
      "v_mfma_f32_16x16x32_bf16 v[216:219], a[48:51], a[132:135], v[216:219]\n\t"
      "v_mfma_f32_16x16x32_bf16 v[216:219], a[40:43], a[128:131], v[216:219]\n\t"
      "v_mfma_f32_16x16x32_bf16 v[216:219], a[32:35], a[124:127], v[216:219]\n\t"
      "v_mfma_f32_16x16x32_bf16 a[0:3], a[32:35], a[128:131], a[0:3]\n\t"
      "ds_read_b128 a[124:127], %[lx0] offset:8960\n\t"
      "v_mfma_f32_16x16x32_bf16 a[0:3], a[40:43], a[132:135], a[0:3]\n\t"
      "v_mfma_f32_16x16x32_bf16 a[0:3], a[48:51], a[136:139], a[0:3]\n\t"
      "s_add_u32 m0, %[ismx], 5120\n\t"
      "v_mfma_f32_16x16x32_bf16 a[0:3], a[56:59], a[140:143], a[0:3]\n\t"
      "s_waitcnt lgkmcnt(6)\n\t"
      "s_add_u32 s67, s71, 40960\n\t"
      "v_mfma_f32_16x16x32_bf16 a[0:3], a[64:67], a[112:115], a[0:3]\n\t"
      "buffer_load_dwordx4 %[vx], s[48:51], s67 offen lds\n\t"
      "v_mfma_f32_16x16x32_bf16 a[4:7], a[68:71], a[112:115], a[4:7]\n\t"
      "v_mfma_f32_16x16x32_bf16 a[4:7], a[60:63], a[140:143], a[4:7]\n\t"
      "v_mfma_f32_16x16x32_bf16 a[4:7], a[52:55], a[136:139], a[4:7]\n\t"
      "v_mfma_f32_16x16x32_bf16 a[4:7], a[44:47], a[132:135], a[4:7]\n\t"
      "v_mfma_f32_16x16x32_bf16 a[4:7], a[36:39], a[128:131], a[4:7]\n\t"
      "v_mfma_f32_16x16x32_bf16 a[12:15], a[36:39], a[132:135], a[12:15]\n\t"
      "v_mfma_f32_16x16x32_bf16 a[12:15], a[44:47], a[136:139], a[12:15]\n\t"
      "v_mfma_f32_16x16x32_bf16 a[12:15], a[52:55], a[140:143], a[12:15]\n\t"
      "v_mfma_f32_16x16x32_bf16 a[12:15], a[60:63], a[112:115], a[12:15]\n\t"
      "s_waitcnt lgkmcnt(4)\n\t"
      "v_mfma_f32_16x16x32_bf16 a[12:15], a[68:71], a[116:119], a[12:15]\n\t"
      "v_mfma_f32_16x16x32_bf16 a[8:11], a[64:67], a[116:119], a[8:11]\n\t"
      "v_mfma_f32_16x16x32_bf16 a[8:11], a[56:59], a[112:115], a[8:11]\n\t"
      "v_mfma_f32_16x16x32_bf16 a[8:11], a[48:51], a[140:143], a[8:11]\n\t"
      "v_mfma_f32_16x16x32_bf16 a[8:11], a[40:43], a[136:139], a[8:11]\n\t"
      "s_cmp_le_u32 s78, 30\n\t"
      "v_mfma_f32_16x16x32_bf16 a[8:11], a[32:35], a[132:135], a[8:11]\n\t"
      "s_cbranch_scc1 LSKSM480_%=\n\t"
      "v_mfma_f32_16x16x32_bf16 a[16:19], a[32:35], a[136:139], a[16:19]\n\t"
      "v_mfma_f32_16x16x32_bf16 a[16:19], a[40:43], a[140:143], a[16:19]\n\t"
      "v_mfma_f32_16x16x32_bf16 a[16:19], a[48:51], a[112:115], a[16:19]\n\t"
      "v_mfma_f32_16x16x32_bf16 a[16:19], a[56:59], a[116:119], a[16:19]\n\t"
      "s_waitcnt lgkmcnt(2)\n\t"
      "v_mfma_f32_16x16x32_bf16 a[16:19], a[64:67], a[120:123], a[16:19]\n\t"
      "v_mfma_f32_16x16x32_bf16 a[20:23], a[68:71], a[120:123], a[20:23]\n\t"
      "v_mfma_f32_16x16x32_bf16 a[20:23], a[60:63], a[116:119], a[20:23]\n\t"
      "v_mfma_f32_16x16x32_bf16 a[20:23], a[52:55], a[112:115], a[20:23]\n\t"
      "v_mfma_f32_16x16x32_bf16 a[20:23], a[44:47], a[140:143], a[20:23]\n\t"
      "s_cmp_le_u32 s78, 31\n\t"
      "v_mfma_f32_16x16x32_bf16 a[20:23], a[36:39], a[136:139], a[20:23]\n\t"
      "s_cbranch_scc1 LSKSM480_%=\n\t"
      "v_mfma_f32_16x16x32_bf16 a[28:31], a[36:39], a[140:143], a[28:31]\n\t"
      "v_mfma_f32_16x16x32_bf16 a[28:31], a[44:47], a[112:115], a[28:31]\n\t"
      "v_mfma_f32_16x16x32_bf16 a[28:31], a[52:55], a[116:119], a[28:31]\n\t"
      "v_mfma_f32_16x16x32_bf16 a[28:31], a[60:63], a[120:123], a[28:31]\n\t"
      "s_waitcnt lgkmcnt(0)\n\t"
      "v_mfma_f32_16x16x32_bf16 a[28:31], a[68:71], a[124:127], a[28:31]\n\t"
      "v_mfma_f32_16x16x32_bf16 a[24:27], a[64:67], a[124:127], a[24:27]\n\t"
      "v_mfma_f32_16x16x32_bf16 a[24:27], a[56:59], a[120:123], a[24:27]\n\t"
      "v_mfma_f32_16x16x32_bf16 a[24:27], a[48:51], a[116:119], a[24:27]\n\t"
      "v_mfma_f32_16x16x32_bf16 a[24:27], a[40:43], a[112:115], a[24:27]\n\t"
      "v_mfma_f32_16x16x32_bf16 a[24:27], a[32:35], a[140:143], a[24:27]\n\t"
      "LSKSM480_%=:\n\t"
      "v_mfma_f32_16x16x32_bf16 v[192:195], a[72:75], a[144:147], v[192:195]\n\t"
      "v_mfma_f32_16x16x32_bf16 v[192:195], a[80:83], a[148:151], v[192:195]\n\t"
      "v_mfma_f32_16x16x32_bf16 v[192:195], a[88:91], a[152:155], v[192:195]\n\t"
      "v_mfma_f32_16x16x32_bf16 v[192:195], a[96:99], a[156:159], v[192:195]\n\t"
      "v_mfma_f32_16x16x32_bf16 v[192:195], a[104:107], a[160:163], v[192:195]\n\t"
      "s_add_u32 m0, %[ismx], 41984\n\t"
      "v_mfma_f32_16x16x32_bf16 v[196:199], a[108:111], a[160:163], v[196:199]\n\t"
      "s_add_u32 s67, s76, 40960\n\t"
      "v_mfma_f32_16x16x32_bf16 v[196:199], a[100:103], a[156:159], v[196:199]\n\t"
      "buffer_load_dwordx4 %[vx], s[48:51], s67 offen lds\n\t"
      "v_mfma_f32_16x16x32_bf16 v[196:199], a[92:95], a[152:155], v[196:199]\n\t"
      "v_mfma_f32_16x16x32_bf16 v[196:199], a[84:87], a[148:151], v[196:199]\n\t"
      "v_mfma_f32_16x16x32_bf16 v[196:199], a[76:79], a[144:147], v[196:199]\n\t"
      "v_mfma_f32_16x16x32_bf16 v[204:207], a[76:79], a[148:151], v[204:207]\n\t"
      "ds_read_b128 a[144:147], %[lx1] offset:8192\n\t"
      "v_mfma_f32_16x16x32_bf16 v[204:207], a[84:87], a[152:155], v[204:207]\n\t"
      "v_mfma_f32_16x16x32_bf16 v[204:207], a[92:95], a[156:159], v[204:207]\n\t"
      "v_mfma_f32_16x16x32_bf16 v[204:207], a[100:103], a[160:163], v[204:207]\n\t"
      "v_mfma_f32_16x16x32_bf16 v[204:207], a[108:111], a[164:167], v[204:207]\n\t"
      "s_waitcnt vmcnt(6) lgkmcnt(2)\n\t"
      "s_barrier\n\t"
      "ds_read_b64_tr_b16 a[32:33], %[lw0] offset:0\n\t"
      "v_mfma_f32_16x16x32_bf16 v[200:203], a[104:107], a[164:167], v[200:203]\n\t"
      "ds_read_b64_tr_b16 a[34:35], %[lw0] offset:512\n\t"
      "v_mfma_f32_16x16x32_bf16 v[200:203], a[96:99], a[160:163], v[200:203]\n\t"
      "ds_read_b128 a[112:115], %[lx0] offset:0\n\t"
      "v_mfma_f32_16x16x32_bf16 v[200:203], a[88:91], a[156:159], v[200:203]\n\t"
      "ds_read_b64_tr_b16 a[40:41], %[lw0] offset:8192\n\t"
      "v_mfma_f32_16x16x32_bf16 v[200:203], a[80:83], a[152:155], v[200:203]\n\t"
      "ds_read_b64_tr_b16 a[42:43], %[lw0] offset:8704\n\t"
      "v_mfma_f32_16x16x32_bf16 v[200:203], a[72:75], a[148:151], v[200:203]\n\t"
      "ds_read_b128 a[116:119], %[lx0] offset:256\n\t"
      "v_mfma_f32_16x16x32_bf16 v[208:211], a[72:75], a[152:155], v[208:211]\n\t"
      "ds_read_b128 a[148:151], %[lx1] offset:8448\n\t"
      "v_mfma_f32_16x16x32_bf16 v[208:211], a[80:83], a[156:159], v[208:211]\n\t"
      "ds_read_b64_tr_b16 a[48:49], %[lw0] offset:16384\n\t"
      "v_mfma_f32_16x16x32_bf16 v[208:211], a[88:91], a[160:163], v[208:211]\n\t"
      "ds_read_b64_tr_b16 a[50:51], %[lw0] offset:16896\n\t"
      "v_mfma_f32_16x16x32_bf16 v[208:211], a[96:99], a[164:167], v[208:211]\n\t"
      "ds_read_b128 a[120:123], %[lx0] offset:512\n\t"
      "v_mfma_f32_16x16x32_bf16 v[208:211], a[104:107], a[168:171], v[208:211]\n\t"
      "ds_read_b64_tr_b16 a[56:57], %[lw0] offset:24576\n\t"
      "v_mfma_f32_16x16x32_bf16 v[212:215], a[108:111], a[168:171], v[212:215]\n\t"
      "ds_read_b64_tr_b16 a[58:59], %[lw0] offset:25088\n\t"
      "v_mfma_f32_16x16x32_bf16 v[212:215], a[100:103], a[164:167], v[212:215]\n\t"
      "ds_read_b128 a[124:127], %[lx0] offset:768\n\t"
      "v_mfma_f32_16x16x32_bf16 v[212:215], a[92:95], a[160:163], v[212:215]\n\t"
      "ds_read_b64_tr_b16 a[64:65], %[lw0] offset:32768\n\t"
      "v_mfma_f32_16x16x32_bf16 v[212:215], a[84:87], a[156:159], v[212:215]\n\t"
      "ds_read_b64_tr_b16 a[66:67], %[lw0] offset:33280\n\t"
      "v_mfma_f32_16x16x32_bf16 v[212:215], a[76:79], a[152:155], v[212:215]\n\t"
      "ds_read_b128 a[128:131], %[lx0] offset:1024\n\t"
      "v_mfma_f32_16x16x32_bf16 v[220:223], a[76:79], a[156:159], v[220:223]\n\t"
      "ds_read_b128 a[152:155], %[lx1] offset:8704\n\t"
      "v_mfma_f32_16x16x32_bf16 v[220:223], a[84:87], a[160:163], v[220:223]\n\t"
      "ds_read_b64_tr_b16 a[68:69], %[lw0] offset:33792\n\t"
      "v_mfma_f32_16x16x32_bf16 v[220:223], a[92:95], a[164:167], v[220:223]\n\t"
      "ds_read_b64_tr_b16 a[70:71], %[lw0] offset:34304\n\t"
      "v_mfma_f32_16x16x32_bf16 v[220:223], a[100:103], a[168:171], v[220:223]\n\t"
      "s_waitcnt lgkmcnt(15)\n\t"
      "ds_read_b64_tr_b16 a[60:61], %[lw0] offset:25600\n\t"
      "v_mfma_f32_16x16x32_bf16 v[220:223], a[108:111], a[172:175], v[220:223]\n\t"
      "ds_read_b64_tr_b16 a[62:63], %[lw0] offset:26112\n\t"
      "v_mfma_f32_16x16x32_bf16 v[216:219], a[104:107], a[172:175], v[216:219]\n\t"
      "ds_read_b64_tr_b16 a[52:53], %[lw0] offset:17408\n\t"
      "v_mfma_f32_16x16x32_bf16 v[216:219], a[96:99], a[168:171], v[216:219]\n\t"
      "ds_read_b64_tr_b16 a[54:55], %[lw0] offset:17920\n\t"
      "v_mfma_f32_16x16x32_bf16 v[216:219], a[88:91], a[164:167], v[216:219]\n\t"
      "ds_read_b64_tr_b16 a[44:45], %[lw0] offset:9216\n\t"
      "v_mfma_f32_16x16x32_bf16 v[216:219], a[80:83], a[160:163], v[216:219]\n\t"
      "ds_read_b64_tr_b16 a[46:47], %[lw0] offset:9728\n\t"
      "v_mfma_f32_16x16x32_bf16 v[216:219], a[72:75], a[156:159], v[216:219]\n\t"
      "ds_read_b64_tr_b16 a[36:37], %[lw0] offset:1024\n\t"
      "v_mfma_f32_16x16x32_bf16 a[0:3], a[72:75], a[160:163], a[0:3]\n\t"
      "ds_read_b128 a[156:159], %[lx1] offset:8960\n\t"
      "v_mfma_f32_16x16x32_bf16 a[0:3], a[80:83], a[164:167], a[0:3]\n\t"
      "ds_read_b64_tr_b16 a[38:39], %[lw0] offset:1536\n\t"
      "v_mfma_f32_16x16x32_bf16 a[0:3], a[88:91], a[168:171], a[0:3]\n\t"
      "ds_read_b128 a[132:135], %[lx0] offset:1280\n\t"
      "v_mfma_f32_16x16x32_bf16 a[0:3], a[96:99], a[172:175], a[0:3]\n\t"
      "s_waitcnt lgkmcnt(15)\n\t"
      "ds_read_b128 a[136:139], %[lx0] offset:1536\n\t"
      "v_mfma_f32_16x16x32_bf16 a[0:3], a[104:107], a[144:147], a[0:3]\n\t"
      "ds_read_b128 a[140:143], %[lx0] offset:1792\n\t"
      "v_mfma_f32_16x16x32_bf16 a[4:7], a[108:111], a[144:147], a[4:7]\n\t"
      "v_mfma_f32_16x16x32_bf16 a[4:7], a[100:103], a[172:175], a[4:7]\n\t"
      "v_mfma_f32_16x16x32_bf16 a[4:7], a[92:95], a[168:171], a[4:7]\n\t"
      "v_mfma_f32_16x16x32_bf16 a[4:7], a[84:87], a[164:167], a[4:7]\n\t"
      "v_mfma_f32_16x16x32_bf16 a[4:7], a[76:79], a[160:163], a[4:7]\n\t"
      "v_mfma_f32_16x16x32_bf16 a[12:15], a[76:79], a[164:167], a[12:15]\n\t"
      "v_mfma_f32_16x16x32_bf16 a[12:15], a[84:87], a[168:171], a[12:15]\n\t"
      "v_mfma_f32_16x16x32_bf16 a[12:15], a[92:95], a[172:175], a[12:15]\n\t"
      "v_mfma_f32_16x16x32_bf16 a[12:15], a[100:103], a[144:147], a[12:15]\n\t"
      "s_waitcnt lgkmcnt(15)\n\t"
      "v_mfma_f32_16x16x32_bf16 a[12:15], a[108:111], a[148:151], a[12:15]\n\t"
      "v_mfma_f32_16x16x32_bf16 a[8:11], a[104:107], a[148:151], a[8:11]\n\t"
      "v_mfma_f32_16x16x32_bf16 a[8:11], a[96:99], a[144:147], a[8:11]\n\t"
      "v_mfma_f32_16x16x32_bf16 a[8:11], a[88:91], a[172:175], a[8:11]\n\t"
      "v_mfma_f32_16x16x32_bf16 a[8:11], a[80:83], a[168:171], a[8:11]\n\t"
      "s_cmp_le_u32 s78, 30\n\t"
      "v_mfma_f32_16x16x32_bf16 a[8:11], a[72:75], a[164:167], a[8:11]\n\t"
      "s_cbranch_scc1 LSKSM560_%=\n\t"
      "v_mfma_f32_16x16x32_bf16 a[16:19], a[72:75], a[168:171], a[16:19]\n\t"
      "v_mfma_f32_16x16x32_bf16 a[16:19], a[80:83], a[172:175], a[16:19]\n\t"
      "v_mfma_f32_16x16x32_bf16 a[16:19], a[88:91], a[144:147], a[16:19]\n\t"
      "v_mfma_f32_16x16x32_bf16 a[16:19], a[96:99], a[148:151], a[16:19]\n\t"
      "s_waitcnt lgkmcnt(14)\n\t"
      "v_mfma_f32_16x16x32_bf16 a[16:19], a[104:107], a[152:155], a[16:19]\n\t"
      "v_mfma_f32_16x16x32_bf16 a[20:23], a[108:111], a[152:155], a[20:23]\n\t"
      "v_mfma_f32_16x16x32_bf16 a[20:23], a[100:103], a[148:151], a[20:23]\n\t"
      "v_mfma_f32_16x16x32_bf16 a[20:23], a[92:95], a[144:147], a[20:23]\n\t"
      "v_mfma_f32_16x16x32_bf16 a[20:23], a[84:87], a[172:175], a[20:23]\n\t"
      "s_cmp_le_u32 s78, 31\n\t"
      "v_mfma_f32_16x16x32_bf16 a[20:23], a[76:79], a[168:171], a[20:23]\n\t"
      "s_cbranch_scc1 LSKSM560_%=\n\t"
      "v_mfma_f32_16x16x32_bf16 a[28:31], a[76:79], a[172:175], a[28:31]\n\t"
      "v_mfma_f32_16x16x32_bf16 a[28:31], a[84:87], a[144:147], a[28:31]\n\t"
      "v_mfma_f32_16x16x32_bf16 a[28:31], a[92:95], a[148:151], a[28:31]\n\t"
      "v_mfma_f32_16x16x32_bf16 a[28:31], a[100:103], a[152:155], a[28:31]\n\t"
      "s_waitcnt lgkmcnt(4)\n\t"
      "v_mfma_f32_16x16x32_bf16 a[28:31], a[108:111], a[156:159], a[28:31]\n\t"
      "v_mfma_f32_16x16x32_bf16 a[24:27], a[104:107], a[156:159], a[24:27]\n\t"
      "v_mfma_f32_16x16x32_bf16 a[24:27], a[96:99], a[152:155], a[24:27]\n\t"
      "v_mfma_f32_16x16x32_bf16 a[24:27], a[88:91], a[148:151], a[24:27]\n\t"
      "v_mfma_f32_16x16x32_bf16 a[24:27], a[80:83], a[144:147], a[24:27]\n\t"
      "v_mfma_f32_16x16x32_bf16 a[24:27], a[72:75], a[172:175], a[24:27]\n\t"
      "LSKSM560_%=:\n\t"
      "s_cmp_eq_u32 s69, 0\n\t"
      "s_cbranch_scc1 LEXIT_%=\n\t"
      "v_mfma_f32_16x16x32_bf16 v[0:3], a[32:35], a[112:115], v[0:3]\n\t"
      "ds_read_b64_tr_b16 a[72:73], %[lw1] offset:0\n\t"
      "v_mfma_f32_16x16x32_bf16 v[0:3], a[40:43], a[116:119], v[0:3]\n\t"
      "s_waitcnt lgkmcnt(15)\n\t"
      "ds_read_b64_tr_b16 a[74:75], %[lw1] offset:512\n\t"
      "v_mfma_f32_16x16x32_bf16 v[0:3], a[48:51], a[120:123], v[0:3]\n\t"
      "s_waitcnt lgkmcnt(15)\n\t"
      "ds_read_b128 a[144:147], %[lx1] offset:0\n\t"
      "v_mfma_f32_16x16x32_bf16 v[0:3], a[56:59], a[124:127], v[0:3]\n\t"
      "s_waitcnt lgkmcnt(15)\n\t"
      "ds_read_b64_tr_b16 a[80:81], %[lw1] offset:8192\n\t"
      "v_mfma_f32_16x16x32_bf16 v[0:3], a[64:67], a[128:131], v[0:3]\n\t"
      "s_waitcnt lgkmcnt(15)\n\t"
      "ds_read_b64_tr_b16 a[82:83], %[lw1] offset:8704\n\t"
      "v_mfma_f32_16x16x32_bf16 v[4:7], a[68:71], a[128:131], v[4:7]\n\t"
      "s_waitcnt lgkmcnt(15)\n\t"
      "ds_read_b128 a[148:151], %[lx1] offset:256\n\t"
      "v_mfma_f32_16x16x32_bf16 v[4:7], a[60:63], a[124:127], v[4:7]\n\t"
      "s_waitcnt lgkmcnt(14)\n\t"
      "ds_read_b64_tr_b16 a[88:89], %[lw1] offset:16384\n\t"
      "v_mfma_f32_16x16x32_bf16 v[4:7], a[52:55], a[120:123], v[4:7]\n\t"
      "s_waitcnt lgkmcnt(13)\n\t"
      "ds_read_b64_tr_b16 a[90:91], %[lw1] offset:16896\n\t"
      "v_mfma_f32_16x16x32_bf16 v[4:7], a[44:47], a[116:119], v[4:7]\n\t"
      "s_waitcnt lgkmcnt(11)\n\t"
      "ds_read_b128 a[152:155], %[lx1] offset:512\n\t"
      "v_mfma_f32_16x16x32_bf16 v[4:7], a[36:39], a[112:115], v[4:7]\n\t"
      "ds_read_b64_tr_b16 a[96:97], %[lw1] offset:24576\n\t"
      "v_mfma_f32_16x16x32_bf16 v[12:15], a[36:39], a[116:119], v[12:15]\n\t"
      "ds_read_b64_tr_b16 a[98:99], %[lw1] offset:25088\n\t"
      "v_mfma_f32_16x16x32_bf16 v[12:15], a[44:47], a[120:123], v[12:15]\n\t"
      "ds_read_b128 a[156:159], %[lx1] offset:768\n\t"
      "v_mfma_f32_16x16x32_bf16 v[12:15], a[52:55], a[124:127], v[12:15]\n\t"
      "ds_read_b64_tr_b16 a[104:105], %[lw1] offset:32768\n\t"
      "v_mfma_f32_16x16x32_bf16 v[12:15], a[60:63], a[128:131], v[12:15]\n\t"
      "s_waitcnt lgkmcnt(15)\n\t"
      "ds_read_b64_tr_b16 a[106:107], %[lw1] offset:33280\n\t"
      "v_mfma_f32_16x16x32_bf16 v[12:15], a[68:71], a[132:135], v[12:15]\n\t"
      "ds_read_b128 a[160:163], %[lx1] offset:1024\n\t"
      "v_mfma_f32_16x16x32_bf16 v[8:11], a[64:67], a[132:135], v[8:11]\n\t"
      "ds_read_b64_tr_b16 a[108:109], %[lw1] offset:33792\n\t"
      "v_mfma_f32_16x16x32_bf16 v[8:11], a[56:59], a[128:131], v[8:11]\n\t"
      "ds_read_b64_tr_b16 a[110:111], %[lw1] offset:34304\n\t"
      "v_mfma_f32_16x16x32_bf16 v[8:11], a[48:51], a[124:127], v[8:11]\n\t"
      "ds_read_b64_tr_b16 a[100:101], %[lw1] offset:25600\n\t"
      "v_mfma_f32_16x16x32_bf16 v[8:11], a[40:43], a[120:123], v[8:11]\n\t"
      "ds_read_b64_tr_b16 a[102:103], %[lw1] offset:26112\n\t"
      "v_mfma_f32_16x16x32_bf16 v[8:11], a[32:35], a[116:119], v[8:11]\n\t"
      "ds_read_b64_tr_b16 a[92:93], %[lw1] offset:17408\n\t"
      "v_mfma_f32_16x16x32_bf16 v[16:19], a[32:35], a[120:123], v[16:19]\n\t"
      "ds_read_b64_tr_b16 a[94:95], %[lw1] offset:17920\n\t"
      "v_mfma_f32_16x16x32_bf16 v[16:19], a[40:43], a[124:127], v[16:19]\n\t"
      "ds_read_b64_tr_b16 a[84:85], %[lw1] offset:9216\n\t"
      "v_mfma_f32_16x16x32_bf16 v[16:19], a[48:51], a[128:131], v[16:19]\n\t"
      "ds_read_b64_tr_b16 a[86:87], %[lw1] offset:9728\n\t"
      "v_mfma_f32_16x16x32_bf16 v[16:19], a[56:59], a[132:135], v[16:19]\n\t"
      "s_waitcnt lgkmcnt(15)\n\t"
      "ds_read_b64_tr_b16 a[76:77], %[lw1] offset:1024\n\t"
      "v_mfma_f32_16x16x32_bf16 v[16:19], a[64:67], a[136:139], v[16:19]\n\t"
      "ds_read_b64_tr_b16 a[78:79], %[lw1] offset:1536\n\t"
      "v_mfma_f32_16x16x32_bf16 v[20:23], a[68:71], a[136:139], v[20:23]\n\t"
      "ds_read_b128 a[164:167], %[lx1] offset:1280\n\t"
      "v_mfma_f32_16x16x32_bf16 v[20:23], a[60:63], a[132:135], v[20:23]\n\t"
      "ds_read_b128 a[168:171], %[lx1] offset:1536\n\t"
      "v_mfma_f32_16x16x32_bf16 v[20:23], a[52:55], a[128:131], v[20:23]\n\t"
      "ds_read_b128 a[172:175], %[lx1] offset:1792\n\t"
      "v_mfma_f32_16x16x32_bf16 v[20:23], a[44:47], a[124:127], v[20:23]\n\t"
      "v_mfma_f32_16x16x32_bf16 v[20:23], a[36:39], a[120:123], v[20:23]\n\t"
      "v_mfma_f32_16x16x32_bf16 v[28:31], a[36:39], a[124:127], v[28:31]\n\t"
      "v_mfma_f32_16x16x32_bf16 v[28:31], a[44:47], a[128:131], v[28:31]\n\t"
      "v_mfma_f32_16x16x32_bf16 v[28:31], a[52:55], a[132:135], v[28:31]\n\t"
      "s_add_u32 m0, %[ismx], 6144\n\t"
      "v_mfma_f32_16x16x32_bf16 v[28:31], a[60:63], a[136:139], v[28:31]\n\t"
      "s_waitcnt lgkmcnt(15)\n\t"
      "s_add_u32 s67, s71, 49152\n\t"
      "v_mfma_f32_16x16x32_bf16 v[28:31], a[68:71], a[140:143], v[28:31]\n\t"
      "buffer_load_dwordx4 %[vx], s[48:51], s67 offen lds\n\t"
      "v_mfma_f32_16x16x32_bf16 v[24:27], a[64:67], a[140:143], v[24:27]\n\t"
      "v_mfma_f32_16x16x32_bf16 v[24:27], a[56:59], a[136:139], v[24:27]\n\t"
      "v_mfma_f32_16x16x32_bf16 v[24:27], a[48:51], a[132:135], v[24:27]\n\t"
      "v_mfma_f32_16x16x32_bf16 v[24:27], a[40:43], a[128:131], v[24:27]\n\t"
      "v_mfma_f32_16x16x32_bf16 v[24:27], a[32:35], a[124:127], v[24:27]\n\t"
      "v_mfma_f32_16x16x32_bf16 v[0:3], a[72:75], a[144:147], v[0:3]\n\t"
      "v_mfma_f32_16x16x32_bf16 v[0:3], a[80:83], a[148:151], v[0:3]\n\t"
      "v_mfma_f32_16x16x32_bf16 v[0:3], a[88:91], a[152:155], v[0:3]\n\t"
      "ds_read_b128 a[112:115], %[lx0] offset:2048\n\t"
      "v_mfma_f32_16x16x32_bf16 v[0:3], a[96:99], a[156:159], v[0:3]\n\t"
      "s_waitcnt lgkmcnt(14)\n\t"
      "v_mfma_f32_16x16x32_bf16 v[0:3], a[104:107], a[160:163], v[0:3]\n\t"
      "s_waitcnt lgkmcnt(12)\n\t"
      "v_mfma_f32_16x16x32_bf16 v[4:7], a[108:111], a[160:163], v[4:7]\n\t"
      "s_waitcnt lgkmcnt(10)\n\t"
      "v_mfma_f32_16x16x32_bf16 v[4:7], a[100:103], a[156:159], v[4:7]\n\t"
      "s_waitcnt lgkmcnt(8)\n\t"
      "v_mfma_f32_16x16x32_bf16 v[4:7], a[92:95], a[152:155], v[4:7]\n\t"
      "s_waitcnt lgkmcnt(6)\n\t"
      "v_mfma_f32_16x16x32_bf16 v[4:7], a[84:87], a[148:151], v[4:7]\n\t"
      "s_waitcnt lgkmcnt(4)\n\t"
      "ds_read_b128 a[116:119], %[lx0] offset:2304\n\t"
      "v_mfma_f32_16x16x32_bf16 v[4:7], a[76:79], a[144:147], v[4:7]\n\t"
      "v_mfma_f32_16x16x32_bf16 v[12:15], a[76:79], a[148:151], v[12:15]\n\t"
      "v_mfma_f32_16x16x32_bf16 v[12:15], a[84:87], a[152:155], v[12:15]\n\t"
      "v_mfma_f32_16x16x32_bf16 v[12:15], a[92:95], a[156:159], v[12:15]\n\t"
      "v_mfma_f32_16x16x32_bf16 v[12:15], a[100:103], a[160:163], v[12:15]\n\t"
      "s_waitcnt lgkmcnt(4)\n\t"
      "s_add_u32 m0, %[ismx], 43008\n\t"
      "v_mfma_f32_16x16x32_bf16 v[12:15], a[108:111], a[164:167], v[12:15]\n\t"
      "s_add_u32 s67, s76, 49152\n\t"
      "ds_read_b128 a[120:123], %[lx0] offset:2560\n\t"
      "v_mfma_f32_16x16x32_bf16 v[8:11], a[104:107], a[164:167], v[8:11]\n\t"
      "buffer_load_dwordx4 %[vx], s[48:51], s67 offen lds\n\t"
      "v_mfma_f32_16x16x32_bf16 v[8:11], a[96:99], a[160:163], v[8:11]\n\t"
      "v_mfma_f32_16x16x32_bf16 v[8:11], a[88:91], a[156:159], v[8:11]\n\t"
      "v_mfma_f32_16x16x32_bf16 v[8:11], a[80:83], a[152:155], v[8:11]\n\t"
      "v_mfma_f32_16x16x32_bf16 v[8:11], a[72:75], a[148:151], v[8:11]\n\t"
      "v_mfma_f32_16x16x32_bf16 v[16:19], a[72:75], a[152:155], v[16:19]\n\t"
      "ds_read_b128 a[124:127], %[lx0] offset:2816\n\t"
      "v_mfma_f32_16x16x32_bf16 v[16:19], a[80:83], a[156:159], v[16:19]\n\t"
      "v_mfma_f32_16x16x32_bf16 v[16:19], a[88:91], a[160:163], v[16:19]\n\t"
      "v_mfma_f32_16x16x32_bf16 v[16:19], a[96:99], a[164:167], v[16:19]\n\t"
      "s_waitcnt lgkmcnt(5)\n\t"
      "v_mfma_f32_16x16x32_bf16 v[16:19], a[104:107], a[168:171], v[16:19]\n\t"
      "v_mfma_f32_16x16x32_bf16 v[20:23], a[108:111], a[168:171], v[20:23]\n\t"
      "v_mfma_f32_16x16x32_bf16 v[20:23], a[100:103], a[164:167], v[20:23]\n\t"
      "v_mfma_f32_16x16x32_bf16 v[20:23], a[92:95], a[160:163], v[20:23]\n\t"
      "v_mfma_f32_16x16x32_bf16 v[20:23], a[84:87], a[156:159], v[20:23]\n\t"
      "v_mfma_f32_16x16x32_bf16 v[20:23], a[76:79], a[152:155], v[20:23]\n\t"
      "v_mfma_f32_16x16x32_bf16 v[28:31], a[76:79], a[156:159], v[28:31]\n\t"
      "v_mfma_f32_16x16x32_bf16 v[28:31], a[84:87], a[160:163], v[28:31]\n\t"
      "v_mfma_f32_16x16x32_bf16 v[28:31], a[92:95], a[164:167], v[28:31]\n\t"
      "v_mfma_f32_16x16x32_bf16 v[28:31], a[100:103], a[168:171], v[28:31]\n\t"
      "s_waitcnt lgkmcnt(4)\n\t"
      "v_mfma_f32_16x16x32_bf16 v[28:31], a[108:111], a[172:175], v[28:31]\n\t"
      "s_add_u32 m0, %[ismx], 7168\n\t"
      "v_mfma_f32_16x16x32_bf16 v[24:27], a[104:107], a[172:175], v[24:27]\n\t"
      "s_add_u32 s67, s71, 57344\n\t"
      "v_mfma_f32_16x16x32_bf16 v[24:27], a[96:99], a[168:171], v[24:27]\n\t"
      "buffer_load_dwordx4 %[vx], s[48:51], s67 offen lds\n\t"
      "v_mfma_f32_16x16x32_bf16 v[24:27], a[88:91], a[164:167], v[24:27]\n\t"
      "v_mfma_f32_16x16x32_bf16 v[24:27], a[80:83], a[160:163], v[24:27]\n\t"
      "v_mfma_f32_16x16x32_bf16 v[24:27], a[72:75], a[156:159], v[24:27]\n\t"
      "v_mfma_f32_16x16x32_bf16 v[32:35], a[32:35], a[128:131], v[32:35]\n\t"
      "v_mfma_f32_16x16x32_bf16 v[32:35], a[40:43], a[132:135], v[32:35]\n\t"
      "v_mfma_f32_16x16x32_bf16 v[32:35], a[48:51], a[136:139], v[32:35]\n\t"
      "ds_read_b128 a[144:147], %[lx1] offset:2048\n\t"
      "v_mfma_f32_16x16x32_bf16 v[32:35], a[56:59], a[140:143], v[32:35]\n\t"
      "s_waitcnt lgkmcnt(4)\n\t"
      "v_mfma_f32_16x16x32_bf16 v[32:35], a[64:67], a[112:115], v[32:35]\n\t"
      "v_mfma_f32_16x16x32_bf16 v[36:39], a[68:71], a[112:115], v[36:39]\n\t"
      "v_mfma_f32_16x16x32_bf16 v[36:39], a[60:63], a[140:143], v[36:39]\n\t"
      "v_mfma_f32_16x16x32_bf16 v[36:39], a[52:55], a[136:139], v[36:39]\n\t"
      "v_mfma_f32_16x16x32_bf16 v[36:39], a[44:47], a[132:135], v[36:39]\n\t"
      "ds_read_b128 a[148:151], %[lx1] offset:2304\n\t"
      "v_mfma_f32_16x16x32_bf16 v[36:39], a[36:39], a[128:131], v[36:39]\n\t"
      "v_mfma_f32_16x16x32_bf16 v[44:47], a[36:39], a[132:135], v[44:47]\n\t"
      "v_mfma_f32_16x16x32_bf16 v[44:47], a[44:47], a[136:139], v[44:47]\n\t"
      "v_mfma_f32_16x16x32_bf16 v[44:47], a[52:55], a[140:143], v[44:47]\n\t"
      "v_mfma_f32_16x16x32_bf16 v[44:47], a[60:63], a[112:115], v[44:47]\n\t"
      "s_waitcnt lgkmcnt(4)\n\t"
      "v_mfma_f32_16x16x32_bf16 v[44:47], a[68:71], a[116:119], v[44:47]\n\t"
      "ds_read_b128 a[152:155], %[lx1] offset:2560\n\t"
      "v_mfma_f32_16x16x32_bf16 v[40:43], a[64:67], a[116:119], v[40:43]\n\t"
      "s_add_u32 m0, %[ismx], 44032\n\t"
      "v_mfma_f32_16x16x32_bf16 v[40:43], a[56:59], a[112:115], v[40:43]\n\t"
      "s_add_u32 s67, s76, 57344\n\t"
      "v_mfma_f32_16x16x32_bf16 v[40:43], a[48:51], a[140:143], v[40:43]\n\t"
      "buffer_load_dwordx4 %[vx], s[48:51], s67 offen lds\n\t"
      "v_mfma_f32_16x16x32_bf16 v[40:43], a[40:43], a[136:139], v[40:43]\n\t"
      "v_mfma_f32_16x16x32_bf16 v[40:43], a[32:35], a[132:135], v[40:43]\n\t"
      "v_mfma_f32_16x16x32_bf16 v[48:51], a[32:35], a[136:139], v[48:51]\n\t"
      "ds_read_b128 a[156:159], %[lx1] offset:2816\n\t"
      "v_mfma_f32_16x16x32_bf16 v[48:51], a[40:43], a[140:143], v[48:51]\n\t"
      "v_mfma_f32_16x16x32_bf16 v[48:51], a[48:51], a[112:115], v[48:51]\n\t"
      "v_mfma_f32_16x16x32_bf16 v[48:51], a[56:59], a[116:119], v[48:51]\n\t"
      "s_waitcnt lgkmcnt(5)\n\t"
      "v_mfma_f32_16x16x32_bf16 v[48:51], a[64:67], a[120:123], v[48:51]\n\t"
      "v_mfma_f32_16x16x32_bf16 v[52:55], a[68:71], a[120:123], v[52:55]\n\t"
      "v_mfma_f32_16x16x32_bf16 v[52:55], a[60:63], a[116:119], v[52:55]\n\t"
      "v_mfma_f32_16x16x32_bf16 v[52:55], a[52:55], a[112:115], v[52:55]\n\t"
      "v_mfma_f32_16x16x32_bf16 v[52:55], a[44:47], a[140:143], v[52:55]\n\t"
      "v_mfma_f32_16x16x32_bf16 v[52:55], a[36:39], a[136:139], v[52:55]\n\t"
      "v_mfma_f32_16x16x32_bf16 v[60:63], a[36:39], a[140:143], v[60:63]\n\t"
      "v_mfma_f32_16x16x32_bf16 v[60:63], a[44:47], a[112:115], v[60:63]\n\t"
      "v_mfma_f32_16x16x32_bf16 v[60:63], a[52:55], a[116:119], v[60:63]\n\t"
      "v_mfma_f32_16x16x32_bf16 v[60:63], a[60:63], a[120:123], v[60:63]\n\t"
      "s_waitcnt lgkmcnt(4)\n\t"
      "v_mfma_f32_16x16x32_bf16 v[60:63], a[68:71], a[124:127], v[60:63]\n\t"
      "v_mfma_f32_16x16x32_bf16 v[56:59], a[64:67], a[124:127], v[56:59]\n\t"
      "v_mfma_f32_16x16x32_bf16 v[56:59], a[56:59], a[120:123], v[56:59]\n\t"
      "v_mfma_f32_16x16x32_bf16 v[56:59], a[48:51], a[116:119], v[56:59]\n\t"
      "v_mfma_f32_16x16x32_bf16 v[56:59], a[40:43], a[112:115], v[56:59]\n\t"
      "v_mfma_f32_16x16x32_bf16 v[56:59], a[32:35], a[140:143], v[56:59]\n\t"
      "s_waitcnt vmcnt(4) lgkmcnt(0)\n\t"
      "s_barrier\n\t"
      "v_mfma_f32_16x16x32_bf16 v[32:35], a[72:75], a[160:163], v[32:35]\n\t"
      "v_mfma_f32_16x16x32_bf16 v[32:35], a[80:83], a[164:167], v[32:35]\n\t"
      "s_add_u32 m0, %[ismx], 8192\n\t"
      "v_mfma_f32_16x16x32_bf16 v[32:35], a[88:91], a[168:171], v[32:35]\n\t"
      "s_add_u32 s67, s71, 65536\n\t"
      "ds_read_b128 a[128:131], %[lx0] offset:3072\n\t"
      "v_mfma_f32_16x16x32_bf16 v[32:35], a[96:99], a[172:175], v[32:35]\n\t"
      "buffer_load_dwordx4 %[vx], s[48:51], s67 offen lds\n\t"
      "v_mfma_f32_16x16x32_bf16 v[32:35], a[104:107], a[144:147], v[32:35]\n\t"
      "v_mfma_f32_16x16x32_bf16 v[36:39], a[108:111], a[144:147], v[36:39]\n\t"
      "v_mfma_f32_16x16x32_bf16 v[36:39], a[100:103], a[172:175], v[36:39]\n\t"
      "v_mfma_f32_16x16x32_bf16 v[36:39], a[92:95], a[168:171], v[36:39]\n\t"
      "v_mfma_f32_16x16x32_bf16 v[36:39], a[84:87], a[164:167], v[36:39]\n\t"
      "ds_read_b128 a[132:135], %[lx0] offset:3328\n\t"
      "v_mfma_f32_16x16x32_bf16 v[36:39], a[76:79], a[160:163], v[36:39]\n\t"
      "v_mfma_f32_16x16x32_bf16 v[44:47], a[76:79], a[164:167], v[44:47]\n\t"
      "v_mfma_f32_16x16x32_bf16 v[44:47], a[84:87], a[168:171], v[44:47]\n\t"
      "v_mfma_f32_16x16x32_bf16 v[44:47], a[92:95], a[172:175], v[44:47]\n\t"
      "v_mfma_f32_16x16x32_bf16 v[44:47], a[100:103], a[144:147], v[44:47]\n\t"
      "s_add_u32 m0, %[ismx], 45056\n\t"
      "v_mfma_f32_16x16x32_bf16 v[44:47], a[108:111], a[148:151], v[44:47]\n\t"
      "s_add_u32 s67, s76, 65536\n\t"
      "ds_read_b128 a[136:139], %[lx0] offset:3584\n\t"
      "v_mfma_f32_16x16x32_bf16 v[40:43], a[104:107], a[148:151], v[40:43]\n\t"
      "buffer_load_dwordx4 %[vx], s[48:51], s67 offen lds\n\t"
      "v_mfma_f32_16x16x32_bf16 v[40:43], a[96:99], a[144:147], v[40:43]\n\t"
      "v_mfma_f32_16x16x32_bf16 v[40:43], a[88:91], a[172:175], v[40:43]\n\t"
      "s_add_u32 s71, s71, 128\n\t"
      "v_mfma_f32_16x16x32_bf16 v[40:43], a[80:83], a[168:171], v[40:43]\n\t"
      "s_add_u32 s72, s72, 131072\n\t"
      "v_mfma_f32_16x16x32_bf16 v[40:43], a[72:75], a[164:167], v[40:43]\n\t"
      "s_and_b32 s71, s71, 1023\n\t"
      "v_mfma_f32_16x16x32_bf16 v[48:51], a[72:75], a[168:171], v[48:51]\n\t"
      "s_and_b32 s72, s72, 0xfffff\n\t"
      "ds_read_b128 a[140:143], %[lx0] offset:3840\n\t"
      "v_mfma_f32_16x16x32_bf16 v[48:51], a[80:83], a[172:175], v[48:51]\n\t"
      "s_add_u32 s76, s71, s77\n\t"
      "v_mfma_f32_16x16x32_bf16 v[48:51], a[88:91], a[144:147], v[48:51]\n\t"
      "v_mfma_f32_16x16x32_bf16 v[48:51], a[96:99], a[148:151], v[48:51]\n\t"
      "v_mfma_f32_16x16x32_bf16 v[48:51], a[104:107], a[152:155], v[48:51]\n\t"
      "v_mfma_f32_16x16x32_bf16 v[52:55], a[108:111], a[152:155], v[52:55]\n\t"
      "v_mfma_f32_16x16x32_bf16 v[52:55], a[100:103], a[148:151], v[52:55]\n\t"
      "v_mfma_f32_16x16x32_bf16 v[52:55], a[92:95], a[144:147], v[52:55]\n\t"
      "s_add_u32 m0, %[ismx], 0\n\t"
      "v_mfma_f32_16x16x32_bf16 v[52:55], a[84:87], a[172:175], v[52:55]\n\t"
      "s_add_u32 s67, s71, s74\n\t"
      "v_mfma_f32_16x16x32_bf16 v[52:55], a[76:79], a[168:171], v[52:55]\n\t"
      "buffer_load_dwordx4 %[vx], s[48:51], s67 offen lds\n\t"
      "v_mfma_f32_16x16x32_bf16 v[60:63], a[76:79], a[172:175], v[60:63]\n\t"
      "v_mfma_f32_16x16x32_bf16 v[60:63], a[84:87], a[144:147], v[60:63]\n\t"
      "v_mfma_f32_16x16x32_bf16 v[60:63], a[92:95], a[148:151], v[60:63]\n\t"
      "v_mfma_f32_16x16x32_bf16 v[60:63], a[100:103], a[152:155], v[60:63]\n\t"
      "v_mfma_f32_16x16x32_bf16 v[60:63], a[108:111], a[156:159], v[60:63]\n\t"
      "v_mfma_f32_16x16x32_bf16 v[56:59], a[104:107], a[156:159], v[56:59]\n\t"
      "v_mfma_f32_16x16x32_bf16 v[56:59], a[96:99], a[152:155], v[56:59]\n\t"
      "v_mfma_f32_16x16x32_bf16 v[56:59], a[88:91], a[148:151], v[56:59]\n\t"
      "v_mfma_f32_16x16x32_bf16 v[56:59], a[80:83], a[144:147], v[56:59]\n\t"
      "s_add_u32 m0, %[ismx], 36864\n\t"
      "v_mfma_f32_16x16x32_bf16 v[56:59], a[72:75], a[172:175], v[56:59]\n\t"
      "s_add_u32 s67, s76, 0\n\t"
      "v_mfma_f32_16x16x32_bf16 v[64:67], a[32:35], a[112:115], v[64:67]\n\t"
      "buffer_load_dwordx4 %[vx], s[48:51], s67 offen lds\n\t"
      "v_mfma_f32_16x16x32_bf16 v[64:67], a[40:43], a[116:119], v[64:67]\n\t"
      "v_mfma_f32_16x16x32_bf16 v[64:67], a[48:51], a[120:123], v[64:67]\n\t"
      "ds_read_b128 a[160:163], %[lx1] offset:3072\n\t"
      "v_mfma_f32_16x16x32_bf16 v[64:67], a[56:59], a[124:127], v[64:67]\n\t"
      "s_waitcnt lgkmcnt(4)\n\t"
      "v_mfma_f32_16x16x32_bf16 v[64:67], a[64:67], a[128:131], v[64:67]\n\t"
      "s_branch LJOIN_%=\n\t"
      "LEXIT_%=:\n\t"
      "s_cmp_lg_u32 s70, 2\n\t"
      "s_cbranch_scc1 LSL_%=\n\t"
      "LS7_%=:\n\t"
      "v_mfma_f32_16x16x32_bf16 v[0:3], a[32:35], a[112:115], v[0:3]\n\t"
      "ds_read_b64_tr_b16 a[72:73], %[lw1] offset:0\n\t"
      "v_mfma_f32_16x16x32_bf16 v[0:3], a[40:43], a[116:119], v[0:3]\n\t"
      "s_waitcnt lgkmcnt(15)\n\t"
      "ds_read_b64_tr_b16 a[74:75], %[lw1] offset:512\n\t"
      "v_mfma_f32_16x16x32_bf16 v[0:3], a[48:51], a[120:123], v[0:3]\n\t"
      "s_waitcnt lgkmcnt(15)\n\t"
      "ds_read_b128 a[144:147], %[lx1] offset:0\n\t"
      "v_mfma_f32_16x16x32_bf16 v[0:3], a[56:59], a[124:127], v[0:3]\n\t"
      "s_waitcnt lgkmcnt(15)\n\t"
      "ds_read_b64_tr_b16 a[80:81], %[lw1] offset:8192\n\t"
      "v_mfma_f32_16x16x32_bf16 v[0:3], a[64:67], a[128:131], v[0:3]\n\t"
      "s_waitcnt lgkmcnt(15)\n\t"
      "ds_read_b64_tr_b16 a[82:83], %[lw1] offset:8704\n\t"
      "v_mfma_f32_16x16x32_bf16 v[4:7], a[68:71], a[128:131], v[4:7]\n\t"
      "s_waitcnt lgkmcnt(15)\n\t"
      "ds_read_b128 a[148:151], %[lx1] offset:256\n\t"
      "v_mfma_f32_16x16x32_bf16 v[4:7], a[60:63], a[124:127], v[4:7]\n\t"
      "s_waitcnt lgkmcnt(14)\n\t"
      "ds_read_b64_tr_b16 a[88:89], %[lw1] offset:16384\n\t"
      "v_mfma_f32_16x16x32_bf16 v[4:7], a[52:55], a[120:123], v[4:7]\n\t"
      "s_waitcnt lgkmcnt(13)\n\t"
      "ds_read_b64_tr_b16 a[90:91], %[lw1] offset:16896\n\t"
      "v_mfma_f32_16x16x32_bf16 v[4:7], a[44:47], a[116:119], v[4:7]\n\t"
      "s_waitcnt lgkmcnt(11)\n\t"
      "ds_read_b128 a[152:155], %[lx1] offset:512\n\t"
      "v_mfma_f32_16x16x32_bf16 v[4:7], a[36:39], a[112:115], v[4:7]\n\t"
      "ds_read_b64_tr_b16 a[96:97], %[lw1] offset:24576\n\t"
      "v_mfma_f32_16x16x32_bf16 v[12:15], a[36:39], a[116:119], v[12:15]\n\t"
      "ds_read_b64_tr_b16 a[98:99], %[lw1] offset:25088\n\t"
      "v_mfma_f32_16x16x32_bf16 v[12:15], a[44:47], a[120:123], v[12:15]\n\t"
      "ds_read_b128 a[156:159], %[lx1] offset:768\n\t"
      "v_mfma_f32_16x16x32_bf16 v[12:15], a[52:55], a[124:127], v[12:15]\n\t"
      "ds_read_b64_tr_b16 a[104:105], %[lw1] offset:32768\n\t"
      "v_mfma_f32_16x16x32_bf16 v[12:15], a[60:63], a[128:131], v[12:15]\n\t"
      "s_waitcnt lgkmcnt(15)\n\t"
      "ds_read_b64_tr_b16 a[106:107], %[lw1] offset:33280\n\t"
      "v_mfma_f32_16x16x32_bf16 v[12:15], a[68:71], a[132:135], v[12:15]\n\t"
      "ds_read_b128 a[160:163], %[lx1] offset:1024\n\t"
      "v_mfma_f32_16x16x32_bf16 v[8:11], a[64:67], a[132:135], v[8:11]\n\t"
      "ds_read_b64_tr_b16 a[108:109], %[lw1] offset:33792\n\t"
      "v_mfma_f32_16x16x32_bf16 v[8:11], a[56:59], a[128:131], v[8:11]\n\t"
      "ds_read_b64_tr_b16 a[110:111], %[lw1] offset:34304\n\t"
      "v_mfma_f32_16x16x32_bf16 v[8:11], a[48:51], a[124:127], v[8:11]\n\t"
      "ds_read_b64_tr_b16 a[100:101], %[lw1] offset:25600\n\t"
      "v_mfma_f32_16x16x32_bf16 v[8:11], a[40:43], a[120:123], v[8:11]\n\t"
      "ds_read_b64_tr_b16 a[102:103], %[lw1] offset:26112\n\t"
      "v_mfma_f32_16x16x32_bf16 v[8:11], a[32:35], a[116:119], v[8:11]\n\t"
      "ds_read_b64_tr_b16 a[92:93], %[lw1] offset:17408\n\t"
      "v_mfma_f32_16x16x32_bf16 v[16:19], a[32:35], a[120:123], v[16:19]\n\t"
      "ds_read_b64_tr_b16 a[94:95], %[lw1] offset:17920\n\t"
      "v_mfma_f32_16x16x32_bf16 v[16:19], a[40:43], a[124:127], v[16:19]\n\t"
      "ds_read_b64_tr_b16 a[84:85], %[lw1] offset:9216\n\t"
      "v_mfma_f32_16x16x32_bf16 v[16:19], a[48:51], a[128:131], v[16:19]\n\t"
      "ds_read_b64_tr_b16 a[86:87], %[lw1] offset:9728\n\t"
      "v_mfma_f32_16x16x32_bf16 v[16:19], a[56:59], a[132:135], v[16:19]\n\t"
      "s_waitcnt lgkmcnt(15)\n\t"
      "ds_read_b64_tr_b16 a[76:77], %[lw1] offset:1024\n\t"
      "v_mfma_f32_16x16x32_bf16 v[16:19], a[64:67], a[136:139], v[16:19]\n\t"
      "ds_read_b64_tr_b16 a[78:79], %[lw1] offset:1536\n\t"
      "v_mfma_f32_16x16x32_bf16 v[20:23], a[68:71], a[136:139], v[20:23]\n\t"
      "ds_read_b128 a[164:167], %[lx1] offset:1280\n\t"
      "v_mfma_f32_16x16x32_bf16 v[20:23], a[60:63], a[132:135], v[20:23]\n\t"
      "ds_read_b128 a[168:171], %[lx1] offset:1536\n\t"
      "v_mfma_f32_16x16x32_bf16 v[20:23], a[52:55], a[128:131], v[20:23]\n\t"
      "ds_read_b128 a[172:175], %[lx1] offset:1792\n\t"
      "v_mfma_f32_16x16x32_bf16 v[20:23], a[44:47], a[124:127], v[20:23]\n\t"
      "v_mfma_f32_16x16x32_bf16 v[20:23], a[36:39], a[120:123], v[20:23]\n\t"
      "v_mfma_f32_16x16x32_bf16 v[28:31], a[36:39], a[124:127], v[28:31]\n\t"
      "v_mfma_f32_16x16x32_bf16 v[28:31], a[44:47], a[128:131], v[28:31]\n\t"
      "v_mfma_f32_16x16x32_bf16 v[28:31], a[52:55], a[132:135], v[28:31]\n\t"
      "s_add_u32 m0, %[ismx], 6144\n\t"
      "v_mfma_f32_16x16x32_bf16 v[28:31], a[60:63], a[136:139], v[28:31]\n\t"
      "s_waitcnt lgkmcnt(15)\n\t"
      "s_add_u32 s67, s71, 49152\n\t"
      "v_mfma_f32_16x16x32_bf16 v[28:31], a[68:71], a[140:143], v[28:31]\n\t"
      "buffer_load_dwordx4 %[vx], s[48:51], s67 offen lds\n\t"
      "v_mfma_f32_16x16x32_bf16 v[24:27], a[64:67], a[140:143], v[24:27]\n\t"
      "v_mfma_f32_16x16x32_bf16 v[24:27], a[56:59], a[136:139], v[24:27]\n\t"
      "v_mfma_f32_16x16x32_bf16 v[24:27], a[48:51], a[132:135], v[24:27]\n\t"
      "v_mfma_f32_16x16x32_bf16 v[24:27], a[40:43], a[128:131], v[24:27]\n\t"
      "v_mfma_f32_16x16x32_bf16 v[24:27], a[32:35], a[124:127], v[24:27]\n\t"
      "v_mfma_f32_16x16x32_bf16 v[0:3], a[72:75], a[144:147], v[0:3]\n\t"
      "v_mfma_f32_16x16x32_bf16 v[0:3], a[80:83], a[148:151], v[0:3]\n\t"
      "v_mfma_f32_16x16x32_bf16 v[0:3], a[88:91], a[152:155], v[0:3]\n\t"
      "ds_read_b128 a[112:115], %[lx0] offset:2048\n\t"
      "v_mfma_f32_16x16x32_bf16 v[0:3], a[96:99], a[156:159], v[0:3]\n\t"
      "s_waitcnt lgkmcnt(14)\n\t"
      "v_mfma_f32_16x16x32_bf16 v[0:3], a[104:107], a[160:163], v[0:3]\n\t"
      "s_waitcnt lgkmcnt(12)\n\t"
      "v_mfma_f32_16x16x32_bf16 v[4:7], a[108:111], a[160:163], v[4:7]\n\t"
      "s_waitcnt lgkmcnt(10)\n\t"
      "v_mfma_f32_16x16x32_bf16 v[4:7], a[100:103], a[156:159], v[4:7]\n\t"
      "s_waitcnt lgkmcnt(8)\n\t"
      "v_mfma_f32_16x16x32_bf16 v[4:7], a[92:95], a[152:155], v[4:7]\n\t"
      "s_waitcnt lgkmcnt(6)\n\t"
      "v_mfma_f32_16x16x32_bf16 v[4:7], a[84:87], a[148:151], v[4:7]\n\t"
      "s_waitcnt lgkmcnt(4)\n\t"
      "ds_read_b128 a[116:119], %[lx0] offset:2304\n\t"
      "v_mfma_f32_16x16x32_bf16 v[4:7], a[76:79], a[144:147], v[4:7]\n\t"
      "v_mfma_f32_16x16x32_bf16 v[12:15], a[76:79], a[148:151], v[12:15]\n\t"
      "v_mfma_f32_16x16x32_bf16 v[12:15], a[84:87], a[152:155], v[12:15]\n\t"
      "v_mfma_f32_16x16x32_bf16 v[12:15], a[92:95], a[156:159], v[12:15]\n\t"
      "v_mfma_f32_16x16x32_bf16 v[12:15], a[100:103], a[160:163], v[12:15]\n\t"
      "s_waitcnt lgkmcnt(4)\n\t"
      "s_add_u32 m0, %[ismx], 43008\n\t"
      "v_mfma_f32_16x16x32_bf16 v[12:15], a[108:111], a[164:167], v[12:15]\n\t"
      "s_add_u32 s67, s76, 49152\n\t"
      "ds_read_b128 a[120:123], %[lx0] offset:2560\n\t"
      "v_mfma_f32_16x16x32_bf16 v[8:11], a[104:107], a[164:167], v[8:11]\n\t"
      "buffer_load_dwordx4 %[vx], s[48:51], s67 offen lds\n\t"
      "v_mfma_f32_16x16x32_bf16 v[8:11], a[96:99], a[160:163], v[8:11]\n\t"
      "v_mfma_f32_16x16x32_bf16 v[8:11], a[88:91], a[156:159], v[8:11]\n\t"
      "v_mfma_f32_16x16x32_bf16 v[8:11], a[80:83], a[152:155], v[8:11]\n\t"
      "v_mul_f32_e64 v224, s84, |v0|\n\t"
      "v_mul_f32_e64 v225, s84, |v1|\n\t"
      "v_mfma_f32_16x16x32_bf16 v[8:11], a[72:75], a[148:151], v[8:11]\n\t"
      "v_exp_f32_e32 v224, v224\n\t"
      "v_mfma_f32_16x16x32_bf16 v[16:19], a[72:75], a[152:155], v[16:19]\n\t"
      "ds_read_b128 a[124:127], %[lx0] offset:2816\n\t"
      "v_mul_f32_e64 v226, s84, |v2|\n\t"
      "v_mfma_f32_16x16x32_bf16 v[16:19], a[80:83], a[156:159], v[16:19]\n\t"
      "v_exp_f32_e32 v225, v225\n\t"
      "v_mfma_f32_16x16x32_bf16 v[16:19], a[88:91], a[160:163], v[16:19]\n\t"
      "v_exp_f32_e32 v226, v226\n\t"
      "v_mfma_f32_16x16x32_bf16 v[16:19], a[96:99], a[164:167], v[16:19]\n\t"
      "s_waitcnt lgkmcnt(5)\n\t"
      "v_mul_f32_e64 v227, s84, |v3|\n\t"
      "v_mul_f32_e32 v4, s86, v4\n\t"
      "v_mfma_f32_16x16x32_bf16 v[16:19], a[104:107], a[168:171], v[16:19]\n\t"
      "v_exp_f32_e32 v227, v227\n\t"
      "v_mfma_f32_16x16x32_bf16 v[20:23], a[108:111], a[168:171], v[20:23]\n\t"
      "v_exp_f32_e32 v4, v4\n\t"
      "v_mfma_f32_16x16x32_bf16 v[20:23], a[100:103], a[164:167], v[20:23]\n\t"
      "v_mul_f32_e32 v5, s86, v5\n\t"
      "v_mul_f32_e32 v6, s86, v6\n\t"
      "v_mfma_f32_16x16x32_bf16 v[20:23], a[92:95], a[160:163], v[20:23]\n\t"
      "v_exp_f32_e32 v5, v5\n\t"
      "v_mfma_f32_16x16x32_bf16 v[20:23], a[84:87], a[156:159], v[20:23]\n\t"
      "v_exp_f32_e32 v6, v6\n\t"
      "v_mfma_f32_16x16x32_bf16 v[20:23], a[76:79], a[152:155], v[20:23]\n\t"
      "v_mul_f32_e32 v7, s86, v7\n\t"
      "v_fma_f32 v224, v224, -0.5, -0.5\n\t"
      "v_mfma_f32_16x16x32_bf16 v[28:31], a[76:79], a[156:159], v[28:31]\n\t"
      "v_exp_f32_e32 v7, v7\n\t"
      "v_mfma_f32_16x16x32_bf16 v[28:31], a[84:87], a[160:163], v[28:31]\n\t"
      "v_fma_f32 v225, v225, -0.5, -0.5\n\t"
      "v_fma_f32 v226, v226, -0.5, -0.5\n\t"
      "v_mfma_f32_16x16x32_bf16 v[28:31], a[92:95], a[164:167], v[28:31]\n\t"
      "v_fma_f32 v227, v227, -0.5, -0.5\n\t"
      "v_fma_f32 v4, v224, v4, v224\n\t"
      "v_mfma_f32_16x16x32_bf16 v[28:31], a[100:103], a[168:171], v[28:31]\n\t"
      "s_waitcnt lgkmcnt(4)\n\t"
      "v_rcp_f32_e32 v4, v4\n\t"
      "v_mfma_f32_16x16x32_bf16 v[28:31], a[108:111], a[172:175], v[28:31]\n\t"
      "s_add_u32 m0, %[ismx], 7168\n\t"
      "v_fma_f32 v5, v225, v5, v225\n\t"
      "v_fma_f32 v6, v226, v6, v226\n\t"
      "v_mfma_f32_16x16x32_bf16 v[24:27], a[104:107], a[172:175], v[24:27]\n\t"
      "s_add_u32 s67, s71, 57344\n\t"
      "v_rcp_f32_e32 v5, v5\n\t"
      "v_mfma_f32_16x16x32_bf16 v[24:27], a[96:99], a[168:171], v[24:27]\n\t"
      "buffer_load_dwordx4 %[vx], s[48:51], s67 offen lds\n\t"
      "v_fma_f32 v7, v227, v7, v227\n\t"
      "v_mfma_f32_16x16x32_bf16 v[24:27], a[88:91], a[164:167], v[24:27]\n\t"
      "v_rcp_f32_e32 v6, v6\n\t"
      "v_mfma_f32_16x16x32_bf16 v[24:27], a[80:83], a[160:163], v[24:27]\n\t"
      "v_rcp_f32_e32 v7, v7\n\t"
      "v_mfma_f32_16x16x32_bf16 v[24:27], a[72:75], a[156:159], v[24:27]\n\t"
      "v_bfi_b32 v0, s75, v4, v0\n\t"
      "v_bfi_b32 v1, s75, v5, v1\n\t"
      "v_mfma_f32_16x16x32_bf16 v[32:35], a[32:35], a[128:131], v[32:35]\n\t"
      "v_bfi_b32 v2, s75, v6, v2\n\t"
      "v_bfi_b32 v3, s75, v7, v3\n\t"
      "v_mfma_f32_16x16x32_bf16 v[32:35], a[40:43], a[132:135], v[32:35]\n\t"
      "v_fmac_f32_e32 v0, v224, v0\n\t"
      "v_fmac_f32_e32 v1, v225, v1\n\t"
      "v_mfma_f32_16x16x32_bf16 v[32:35], a[48:51], a[136:139], v[32:35]\n\t"
      "ds_read_b128 a[144:147], %[lx1] offset:2048\n\t"
      "v_fmac_f32_e32 v2, v226, v2\n\t"
      "v_mfma_f32_16x16x32_bf16 v[32:35], a[56:59], a[140:143], v[32:35]\n\t"
      "s_waitcnt lgkmcnt(4)\n\t"
      "v_fmac_f32_e32 v3, v227, v3\n\t"
      "v_cvt_pk_bf16_f32 v4, v0, v1\n\t"
      "v_mfma_f32_16x16x32_bf16 v[32:35], a[64:67], a[112:115], v[32:35]\n\t"
      "s_mov_b32 s68, 0\n\t"
      "v_cvt_pk_bf16_f32 v5, v2, v3\n\t"
      "v_mul_f32_e64 v228, s84, |v8|\n\t"
      "v_mfma_f32_16x16x32_bf16 v[36:39], a[68:71], a[112:115], v[36:39]\n\t"
      "buffer_store_dwordx2 v[4:5], %[vo], s[52:55], s68 offen offset:0\n\t"
      "v_mfma_f32_16x16x32_bf16 v[36:39], a[60:63], a[140:143], v[36:39]\n\t"
      "v_exp_f32_e32 v228, v228\n\t"
      "v_mfma_f32_16x16x32_bf16 v[36:39], a[52:55], a[136:139], v[36:39]\n\t"
      "v_mul_f32_e64 v229, s84, |v9|\n\t"
      "v_mul_f32_e64 v230, s84, |v10|\n\t"
      "v_mfma_f32_16x16x32_bf16 v[36:39], a[44:47], a[132:135], v[36:39]\n\t"
      "ds_read_b128 a[148:151], %[lx1] offset:2304\n\t"
      "v_mul_f32_e64 v231, s84, |v11|\n\t"
      "v_mfma_f32_16x16x32_bf16 v[36:39], a[36:39], a[128:131], v[36:39]\n\t"
      "v_exp_f32_e32 v229, v229\n\t"
      "v_mfma_f32_16x16x32_bf16 v[44:47], a[36:39], a[132:135], v[44:47]\n\t"
      "v_exp_f32_e32 v230, v230\n\t"
      "v_mfma_f32_16x16x32_bf16 v[44:47], a[44:47], a[136:139], v[44:47]\n\t"
      "v_exp_f32_e32 v231, v231\n\t"
      "v_mfma_f32_16x16x32_bf16 v[44:47], a[52:55], a[140:143], v[44:47]\n\t"
      "v_mul_f32_e32 v12, s86, v12\n\t"
      "v_mul_f32_e32 v13, s86, v13\n\t"
      "v_mfma_f32_16x16x32_bf16 v[44:47], a[60:63], a[112:115], v[44:47]\n\t"
      "s_waitcnt lgkmcnt(4)\n\t"
      "v_exp_f32_e32 v12, v12\n\t"
      "v_mfma_f32_16x16x32_bf16 v[44:47], a[68:71], a[116:119], v[44:47]\n\t"
      "ds_read_b128 a[152:155], %[lx1] offset:2560\n\t"
      "v_mul_f32_e32 v14, s86, v14\n\t"
      "v_mfma_f32_16x16x32_bf16 v[40:43], a[64:67], a[116:119], v[40:43]\n\t"
      "s_add_u32 m0, %[ismx], 44032\n\t"
      "v_exp_f32_e32 v13, v13\n\t"
      "v_mfma_f32_16x16x32_bf16 v[40:43], a[56:59], a[112:115], v[40:43]\n\t"
      "s_add_u32 s67, s76, 57344\n\t"
      "v_exp_f32_e32 v14, v14\n\t"
      "v_mfma_f32_16x16x32_bf16 v[40:43], a[48:51], a[140:143], v[40:43]\n\t"
      "buffer_load_dwordx4 %[vx], s[48:51], s67 offen lds\n\t"
      "v_mul_f32_e32 v15, s86, v15\n\t"
      "v_mfma_f32_16x16x32_bf16 v[40:43], a[40:43], a[136:139], v[40:43]\n\t"
      "v_exp_f32_e32 v15, v15\n\t"
      "v_mfma_f32_16x16x32_bf16 v[40:43], a[32:35], a[132:135], v[40:43]\n\t"
      "v_fma_f32 v228, v228, -0.5, -0.5\n\t"
      "v_fma_f32 v229, v229, -0.5, -0.5\n\t"
      "v_mfma_f32_16x16x32_bf16 v[48:51], a[32:35], a[136:139], v[48:51]\n\t"
      "ds_read_b128 a[156:159], %[lx1] offset:2816\n\t"
      "v_fma_f32 v230, v230, -0.5, -0.5\n\t"
      "v_mfma_f32_16x16x32_bf16 v[48:51], a[40:43], a[140:143], v[48:51]\n\t"
      "v_fma_f32 v231, v231, -0.5, -0.5\n\t"
      "v_fma_f32 v12, v228, v12, v228\n\t"
      "v_mfma_f32_16x16x32_bf16 v[48:51], a[48:51], a[112:115], v[48:51]\n\t"
      "v_rcp_f32_e32 v12, v12\n\t"
      "v_mfma_f32_16x16x32_bf16 v[48:51], a[56:59], a[116:119], v[48:51]\n\t"
      "s_waitcnt lgkmcnt(5)\n\t"
      "v_fma_f32 v13, v229, v13, v229\n\t"
      "v_fma_f32 v14, v230, v14, v230\n\t"
      "v_mfma_f32_16x16x32_bf16 v[48:51], a[64:67], a[120:123], v[48:51]\n\t"
      "v_rcp_f32_e32 v13, v13\n\t"
      "v_mfma_f32_16x16x32_bf16 v[52:55], a[68:71], a[120:123], v[52:55]\n\t"
      "v_rcp_f32_e32 v14, v14\n\t"
      "v_mfma_f32_16x16x32_bf16 v[52:55], a[60:63], a[116:119], v[52:55]\n\t"
      "v_fma_f32 v15, v231, v15, v231\n\t"
      "v_bfi_b32 v8, s75, v12, v8\n\t"
      "v_mfma_f32_16x16x32_bf16 v[52:55], a[52:55], a[112:115], v[52:55]\n\t"
      "v_rcp_f32_e32 v15, v15\n\t"
      "v_mfma_f32_16x16x32_bf16 v[52:55], a[44:47], a[140:143], v[52:55]\n\t"
      "v_bfi_b32 v9, s75, v13, v9\n\t"
      "v_bfi_b32 v10, s75, v14, v10\n\t"
      "v_mfma_f32_16x16x32_bf16 v[52:55], a[36:39], a[136:139], v[52:55]\n\t"
      "v_bfi_b32 v11, s75, v15, v11\n\t"
      "v_fmac_f32_e32 v8, v228, v8\n\t"
      "v_mfma_f32_16x16x32_bf16 v[60:63], a[36:39], a[140:143], v[60:63]\n\t"
      "v_fmac_f32_e32 v9, v229, v9\n\t"
      "v_fmac_f32_e32 v10, v230, v10\n\t"
      "v_mfma_f32_16x16x32_bf16 v[60:63], a[44:47], a[112:115], v[60:63]\n\t"
      "v_fmac_f32_e32 v11, v231, v11\n\t"
      "v_cvt_pk_bf16_f32 v12, v8, v9\n\t"
      "v_mfma_f32_16x16x32_bf16 v[60:63], a[52:55], a[116:119], v[60:63]\n\t"
      "s_mov_b32 s68, 0\n\t"
      "v_cvt_pk_bf16_f32 v13, v10, v11\n\t"
      "v_mul_f32_e64 v224, s84, |v16|\n\t"
      "v_mfma_f32_16x16x32_bf16 v[60:63], a[60:63], a[120:123], v[60:63]\n\t"
      "s_waitcnt lgkmcnt(4)\n\t"
      "buffer_store_dwordx2 v[12:13], %[vo], s[52:55], s68 offen offset:2048\n\t"
      "v_mfma_f32_16x16x32_bf16 v[60:63], a[68:71], a[124:127], v[60:63]\n\t"
      "v_exp_f32_e32 v224, v224\n\t"
      "v_mfma_f32_16x16x32_bf16 v[56:59], a[64:67], a[124:127], v[56:59]\n\t"
      "v_mul_f32_e64 v225, s84, |v17|\n\t"
      "v_mul_f32_e64 v226, s84, |v18|\n\t"
      "v_mfma_f32_16x16x32_bf16 v[56:59], a[56:59], a[120:123], v[56:59]\n\t"
      "v_exp_f32_e32 v225, v225\n\t"
      "v_mfma_f32_16x16x32_bf16 v[56:59], a[48:51], a[116:119], v[56:59]\n\t"
      "v_exp_f32_e32 v226, v226\n\t"
      "v_mfma_f32_16x16x32_bf16 v[56:59], a[40:43], a[112:115], v[56:59]\n\t"
      "v_mul_f32_e64 v227, s84, |v19|\n\t"
      "v_mul_f32_e32 v20, s86, v20\n\t"
      "v_mfma_f32_16x16x32_bf16 v[56:59], a[32:35], a[140:143], v[56:59]\n\t"
      "s_waitcnt vmcnt(6) lgkmcnt(0)\n\t"
      "s_barrier\n\t"
      "v_mul_f32_e32 v21, s86, v21\n\t"
      "v_mfma_f32_16x16x32_bf16 v[32:35], a[72:75], a[160:163], v[32:35]\n\t"
      "v_exp_f32_e32 v227, v227\n\t"
      "v_mfma_f32_16x16x32_bf16 v[32:35], a[80:83], a[164:167], v[32:35]\n\t"
      "s_add_u32 m0, %[ismx], 8192\n\t"
      "v_exp_f32_e32 v20, v20\n\t"
      "v_mfma_f32_16x16x32_bf16 v[32:35], a[88:91], a[168:171], v[32:35]\n\t"
      "s_add_u32 s67, s71, 65536\n\t"
      "ds_read_b128 a[128:131], %[lx0] offset:3072\n\t"
      "v_mul_f32_e32 v22, s86, v22\n\t"
      "v_mfma_f32_16x16x32_bf16 v[32:35], a[96:99], a[172:175], v[32:35]\n\t"
      "buffer_load_dwordx4 %[vx], s[48:51], s67 offen lds\n\t"
      "v_mul_f32_e32 v23, s86, v23\n\t"
      "v_mfma_f32_16x16x32_bf16 v[32:35], a[104:107], a[144:147], v[32:35]\n\t"
      "v_exp_f32_e32 v21, v21\n\t"
      "v_mfma_f32_16x16x32_bf16 v[36:39], a[108:111], a[144:147], v[36:39]\n\t"
      "v_exp_f32_e32 v22, v22\n\t"
      "v_mfma_f32_16x16x32_bf16 v[36:39], a[100:103], a[172:175], v[36:39]\n\t"
      "v_exp_f32_e32 v23, v23\n\t"
      "v_mfma_f32_16x16x32_bf16 v[36:39], a[92:95], a[168:171], v[36:39]\n\t"
      "v_fma_f32 v224, v224, -0.5, -0.5\n\t"
      "v_fma_f32 v225, v225, -0.5, -0.5\n\t"
      "v_mfma_f32_16x16x32_bf16 v[36:39], a[84:87], a[164:167], v[36:39]\n\t"
      "ds_read_b128 a[132:135], %[lx0] offset:3328\n\t"
      "v_fma_f32 v226, v226, -0.5, -0.5\n\t"
      "v_mfma_f32_16x16x32_bf16 v[36:39], a[76:79], a[160:163], v[36:39]\n\t"
      "v_fma_f32 v227, v227, -0.5, -0.5\n\t"
      "v_fma_f32 v20, v224, v20, v224\n\t"
      "v_mfma_f32_16x16x32_bf16 v[44:47], a[76:79], a[164:167], v[44:47]\n\t"
      "v_rcp_f32_e32 v20, v20\n\t"
      "v_mfma_f32_16x16x32_bf16 v[44:47], a[84:87], a[168:171], v[44:47]\n\t"
      "v_fma_f32 v21, v225, v21, v225\n\t"
      "v_fma_f32 v22, v226, v22, v226\n\t"
      "v_mfma_f32_16x16x32_bf16 v[44:47], a[92:95], a[172:175], v[44:47]\n\t"
      "v_rcp_f32_e32 v21, v21\n\t"
      "v_mfma_f32_16x16x32_bf16 v[44:47], a[100:103], a[144:147], v[44:47]\n\t"
      "s_add_u32 m0, %[ismx], 45056\n\t"
      "v_rcp_f32_e32 v22, v22\n\t"
      "v_mfma_f32_16x16x32_bf16 v[44:47], a[108:111], a[148:151], v[44:47]\n\t"
      "s_add_u32 s67, s76, 65536\n\t"
      "ds_read_b128 a[136:139], %[lx0] offset:3584\n\t"
      "v_fma_f32 v23, v227, v23, v227\n\t"
      "v_mfma_f32_16x16x32_bf16 v[40:43], a[104:107], a[148:151], v[40:43]\n\t"
      "buffer_load_dwordx4 %[vx], s[48:51], s67 offen lds\n\t"
      "v_bfi_b32 v16, s75, v20, v16\n\t"
      "v_mfma_f32_16x16x32_bf16 v[40:43], a[96:99], a[144:147], v[40:43]\n\t"
      "s_add_u32 s71, s71, 128\n\t"
      "s_add_u32 s72, s72, 131072\n\t"
      "v_bfi_b32 v17, s75, v21, v17\n\t"
      "v_mfma_f32_16x16x32_bf16 v[40:43], a[88:91], a[172:175], v[40:43]\n\t"
      "s_and_b32 s71, s71, 1023\n\t"
      "s_and_b32 s72, s72, 0xfffff\n\t"
      "v_bfi_b32 v18, s75, v22, v18\n\t"
      "v_mfma_f32_16x16x32_bf16 v[40:43], a[80:83], a[168:171], v[40:43]\n\t"
      "s_lshl_b32 s77, %[ijb], 13\n\t"
      "s_add_u32 s76, s71, s77\n\t"
      "v_fmac_f32_e32 v16, v224, v16\n\t"
      "v_mfma_f32_16x16x32_bf16 v[40:43], a[72:75], a[164:167], v[40:43]\n\t"
      "s_add_u32 s48, s48, s60\n\t"
      "s_addc_u32 s49, s49, 0\n\t"
      "v_fmac_f32_e32 v17, v225, v17\n\t"
      "v_mfma_f32_16x16x32_bf16 v[48:51], a[72:75], a[168:171], v[48:51]\n\t"
      "s_sub_u32 s50, s50, s60\n\t"
      "s_mov_b32 s74, 0\n\t"
      "ds_read_b128 a[140:143], %[lx0] offset:3840\n\t"
      "v_mfma_f32_16x16x32_bf16 v[48:51], a[80:83], a[172:175], v[48:51]\n\t"
      "v_add_u32 %[vx], %[idx], %[vx]\n\t"
      "v_fmac_f32_e32 v18, v226, v18\n\t"
      "v_mfma_f32_16x16x32_bf16 v[48:51], a[88:91], a[144:147], v[48:51]\n\t"
      "v_rcp_f32_e32 v23, v23\n\t"
      "v_mfma_f32_16x16x32_bf16 v[48:51], a[96:99], a[148:151], v[48:51]\n\t"
      "v_bfi_b32 v19, s75, v23, v19\n\t"
      "v_fmac_f32_e32 v19, v227, v19\n\t"
      "v_mfma_f32_16x16x32_bf16 v[48:51], a[104:107], a[152:155], v[48:51]\n\t"
      "v_cvt_pk_bf16_f32 v20, v16, v17\n\t"
      "v_cvt_pk_bf16_f32 v21, v18, v19\n\t"
      "v_mfma_f32_16x16x32_bf16 v[52:55], a[108:111], a[152:155], v[52:55]\n\t"
      "s_mov_b32 s68, 4096\n\t"
      "v_mul_f32_e64 v228, s84, |v24|\n\t"
      "v_mul_f32_e64 v229, s84, |v25|\n\t"
      "v_mfma_f32_16x16x32_bf16 v[52:55], a[100:103], a[148:151], v[52:55]\n\t"
      "buffer_store_dwordx2 v[20:21], %[vo], s[52:55], s68 offen offset:0\n\t"
      "v_mfma_f32_16x16x32_bf16 v[52:55], a[92:95], a[144:147], v[52:55]\n\t"
      "s_add_u32 m0, %[ismx], 0\n\t"
      "v_exp_f32_e32 v228, v228\n\t"
      "v_mfma_f32_16x16x32_bf16 v[52:55], a[84:87], a[172:175], v[52:55]\n\t"
      "s_add_u32 s67, s71, s74\n\t"
      "v_exp_f32_e32 v229, v229\n\t"
      "v_mfma_f32_16x16x32_bf16 v[52:55], a[76:79], a[168:171], v[52:55]\n\t"
      "buffer_load_dwordx4 %[vx], s[48:51], s67 offen lds\n\t"
      "v_mul_f32_e64 v230, s84, |v26|\n\t"
      "v_mfma_f32_16x16x32_bf16 v[60:63], a[76:79], a[172:175], v[60:63]\n\t"
      "v_exp_f32_e32 v230, v230\n\t"
      "v_mfma_f32_16x16x32_bf16 v[60:63], a[84:87], a[144:147], v[60:63]\n\t"
      "v_mul_f32_e64 v231, s84, |v27|\n\t"
      "v_mul_f32_e32 v28, s86, v28\n\t"
      "v_mfma_f32_16x16x32_bf16 v[60:63], a[92:95], a[148:151], v[60:63]\n\t"
      "v_exp_f32_e32 v231, v231\n\t"
      "v_mfma_f32_16x16x32_bf16 v[60:63], a[100:103], a[152:155], v[60:63]\n\t"
      "v_exp_f32_e32 v28, v28\n\t"
      "v_mfma_f32_16x16x32_bf16 v[60:63], a[108:111], a[156:159], v[60:63]\n\t"
      "v_mul_f32_e32 v29, s86, v29\n\t"
      "v_mul_f32_e32 v30, s86, v30\n\t"
      "v_mfma_f32_16x16x32_bf16 v[56:59], a[104:107], a[156:159], v[56:59]\n\t"
      "v_exp_f32_e32 v29, v29\n\t"
      "v_mfma_f32_16x16x32_bf16 v[56:59], a[96:99], a[152:155], v[56:59]\n\t"
      "v_exp_f32_e32 v30, v30\n\t"
      "v_mfma_f32_16x16x32_bf16 v[56:59], a[88:91], a[148:151], v[56:59]\n\t"
      "v_mul_f32_e32 v31, s86, v31\n\t"
      "v_fma_f32 v228, v228, -0.5, -0.5\n\t"
      "v_mfma_f32_16x16x32_bf16 v[56:59], a[80:83], a[144:147], v[56:59]\n\t"
      "s_add_u32 m0, %[ismx], 36864\n\t"
      "v_exp_f32_e32 v31, v31\n\t"
      "v_mfma_f32_16x16x32_bf16 v[56:59], a[72:75], a[172:175], v[56:59]\n\t"
      "s_add_u32 s67, s76, 0\n\t"
      "v_fma_f32 v229, v229, -0.5, -0.5\n\t"
      "v_fma_f32 v230, v230, -0.5, -0.5\n\t"
      "v_mfma_f32_16x16x32_bf16 v[64:67], a[32:35], a[112:115], v[64:67]\n\t"
      "buffer_load_dwordx4 %[vx], s[48:51], s67 offen lds\n\t"
      "v_fma_f32 v231, v231, -0.5, -0.5\n\t"
      "v_mfma_f32_16x16x32_bf16 v[64:67], a[40:43], a[116:119], v[64:67]\n\t"
      "v_fma_f32 v28, v228, v28, v228\n\t"
      "v_fma_f32 v29, v229, v29, v229\n\t"
      "v_mfma_f32_16x16x32_bf16 v[64:67], a[48:51], a[120:123], v[64:67]\n\t"
      "ds_read_b128 a[160:163], %[lx1] offset:3072\n\t"
      "v_fma_f32 v30, v230, v30, v230\n\t"
      "v_mfma_f32_16x16x32_bf16 v[64:67], a[56:59], a[124:127], v[64:67]\n\t"
      "s_waitcnt lgkmcnt(4)\n\t"
      "v_rcp_f32_e32 v28, v28\n\t"
      "v_mfma_f32_16x16x32_bf16 v[64:67], a[64:67], a[128:131], v[64:67]\n\t"
      "v_rcp_f32_e32 v29, v29\n\t"
      "v_mfma_f32_16x16x32_bf16 v[68:71], a[68:71], a[128:131], v[68:71]\n\t"
      "v_rcp_f32_e32 v30, v30\n\t"
      "v_mfma_f32_16x16x32_bf16 v[68:71], a[60:63], a[124:127], v[68:71]\n\t"
      "v_fma_f32 v31, v231, v31, v231\n\t"
      "v_bfi_b32 v24, s75, v28, v24\n\t"
      "v_mfma_f32_16x16x32_bf16 v[68:71], a[52:55], a[120:123], v[68:71]\n\t"
      "v_rcp_f32_e32 v31, v31\n\t"
      "v_mfma_f32_16x16x32_bf16 v[68:71], a[44:47], a[116:119], v[68:71]\n\t"
      "ds_read_b128 a[164:167], %[lx1] offset:3328\n\t"
      "v_bfi_b32 v25, s75, v29, v25\n\t"
      "v_mfma_f32_16x16x32_bf16 v[68:71], a[36:39], a[112:115], v[68:71]\n\t"
      "v_bfi_b32 v26, s75, v30, v26\n\t"
      "v_bfi_b32 v27, s75, v31, v27\n\t"
      "v_mfma_f32_16x16x32_bf16 v[76:79], a[36:39], a[116:119], v[76:79]\n\t"
      "s_add_u32 m0, %[ismx], 1024\n\t"
      "v_fmac_f32_e32 v24, v228, v24\n\t"
      "v_fmac_f32_e32 v25, v229, v25\n\t"
      "v_mfma_f32_16x16x32_bf16 v[76:79], a[44:47], a[120:123], v[76:79]\n\t"
      "s_add_u32 s67, s71, 8192\n\t"
      "v_fmac_f32_e32 v26, v230, v26\n\t"
      "v_fmac_f32_e32 v27, v231, v27\n\t"
      "v_mfma_f32_16x16x32_bf16 v[76:79], a[52:55], a[124:127], v[76:79]\n\t"
      "buffer_load_dwordx4 %[vx], s[48:51], s67 offen lds\n\t"
      "v_cvt_pk_bf16_f32 v28, v24, v25\n\t"
      "v_mfma_f32_16x16x32_bf16 v[76:79], a[60:63], a[128:131], v[76:79]\n\t"
      "s_waitcnt lgkmcnt(4)\n\t"
      "v_cvt_pk_bf16_f32 v29, v26, v27\n\t"
      "v_mul_f32_e64 v224, s84, |v32|\n\t"
      "v_mfma_f32_16x16x32_bf16 v[76:79], a[68:71], a[132:135], v[76:79]\n\t"
      "ds_read_b128 a[168:171], %[lx1] offset:3584\n\t"
      "v_mul_f32_e64 v225, s84, |v33|\n\t"
      "v_mfma_f32_16x16x32_bf16 v[72:75], a[64:67], a[132:135], v[72:75]\n\t"
      "s_mov_b32 s68, 4096\n\t"
      "v_exp_f32_e32 v224, v224\n\t"
      "v_mfma_f32_16x16x32_bf16 v[72:75], a[56:59], a[128:131], v[72:75]\n\t"
      "buffer_store_dwordx2 v[28:29], %[vo], s[52:55], s68 offen offset:2048\n\t"
      "v_mfma_f32_16x16x32_bf16 v[72:75], a[48:51], a[124:127], v[72:75]\n\t"
      "v_exp_f32_e32 v225, v225\n\t"
      "v_mfma_f32_16x16x32_bf16 v[72:75], a[40:43], a[120:123], v[72:75]\n\t"
      "v_mul_f32_e64 v226, s84, |v34|\n\t"
      "v_mul_f32_e64 v227, s84, |v35|\n\t"
      "v_mfma_f32_16x16x32_bf16 v[72:75], a[32:35], a[116:119], v[72:75]\n\t"
      "v_exp_f32_e32 v226, v226\n\t"
      "v_mfma_f32_16x16x32_bf16 v[80:83], a[32:35], a[120:123], v[80:83]\n\t"
      "ds_read_b128 a[172:175], %[lx1] offset:3840\n\t"
      "v_mul_f32_e32 v36, s86, v36\n\t"
      "v_mfma_f32_16x16x32_bf16 v[80:83], a[40:43], a[124:127], v[80:83]\n\t"
      "s_add_u32 m0, %[ismx], 37888\n\t"
      "v_exp_f32_e32 v227, v227\n\t"
      "v_mfma_f32_16x16x32_bf16 v[80:83], a[48:51], a[128:131], v[80:83]\n\t"
      "s_add_u32 s67, s76, 8192\n\t"
      "v_exp_f32_e32 v36, v36\n\t"
      "v_mfma_f32_16x16x32_bf16 v[80:83], a[56:59], a[132:135], v[80:83]\n\t"
      "s_waitcnt lgkmcnt(5)\n\t"
      "buffer_load_dwordx4 %[vx], s[48:51], s67 offen lds\n\t"
      "v_mul_f32_e32 v37, s86, v37\n\t"
      "v_mfma_f32_16x16x32_bf16 v[80:83], a[64:67], a[136:139], v[80:83]\n\t"
      "v_exp_f32_e32 v37, v37\n\t"
      "v_mfma_f32_16x16x32_bf16 v[84:87], a[68:71], a[136:139], v[84:87]\n\t"
      "v_mul_f32_e32 v38, s86, v38\n\t"
      "v_mul_f32_e32 v39, s86, v39\n\t"
      "v_mfma_f32_16x16x32_bf16 v[84:87], a[60:63], a[132:135], v[84:87]\n\t"
      "v_exp_f32_e32 v38, v38\n\t"
      "v_mfma_f32_16x16x32_bf16 v[84:87], a[52:55], a[128:131], v[84:87]\n\t"
      "v_exp_f32_e32 v39, v39\n\t"
      "v_mfma_f32_16x16x32_bf16 v[84:87], a[44:47], a[124:127], v[84:87]\n\t"
      "v_fma_f32 v224, v224, -0.5, -0.5\n\t"
      "v_fma_f32 v225, v225, -0.5, -0.5\n\t"
      "v_mfma_f32_16x16x32_bf16 v[84:87], a[36:39], a[120:123], v[84:87]\n\t"
      "v_fma_f32 v226, v226, -0.5, -0.5\n\t"
      "v_fma_f32 v227, v227, -0.5, -0.5\n\t"
      "v_mfma_f32_16x16x32_bf16 v[92:95], a[36:39], a[124:127], v[92:95]\n\t"
      "v_fma_f32 v36, v224, v36, v224\n\t"
      "v_fma_f32 v37, v225, v37, v225\n\t"
      "v_mfma_f32_16x16x32_bf16 v[92:95], a[44:47], a[128:131], v[92:95]\n\t"
      "v_rcp_f32_e32 v36, v36\n\t"
      "v_mfma_f32_16x16x32_bf16 v[92:95], a[52:55], a[132:135], v[92:95]\n\t"
      "v_rcp_f32_e32 v37, v37\n\t"
      "v_mfma_f32_16x16x32_bf16 v[92:95], a[60:63], a[136:139], v[92:95]\n\t"
      "s_waitcnt lgkmcnt(4)\n\t"
      "s_add_u32 m0, %[ismx], 2048\n\t"
      "v_fma_f32 v38, v226, v38, v226\n\t"
      "v_mfma_f32_16x16x32_bf16 v[92:95], a[68:71], a[140:143], v[92:95]\n\t"
      "s_add_u32 s67, s71, 16384\n\t"
      "v_rcp_f32_e32 v38, v38\n\t"
      "v_mfma_f32_16x16x32_bf16 v[88:91], a[64:67], a[140:143], v[88:91]\n\t"
      "buffer_load_dwordx4 %[vx], s[48:51], s67 offen lds\n\t"
      "v_fma_f32 v39, v227, v39, v227\n\t"
      "v_mfma_f32_16x16x32_bf16 v[88:91], a[56:59], a[136:139], v[88:91]\n\t"
      "v_rcp_f32_e32 v39, v39\n\t"
      "v_mfma_f32_16x16x32_bf16 v[88:91], a[48:51], a[132:135], v[88:91]\n\t"
      "v_bfi_b32 v32, s75, v36, v32\n\t"
      "v_bfi_b32 v33, s75, v37, v33\n\t"
      "v_mfma_f32_16x16x32_bf16 v[88:91], a[40:43], a[128:131], v[88:91]\n\t"
      "v_bfi_b32 v34, s75, v38, v34\n\t"
      "v_bfi_b32 v35, s75, v39, v35\n\t"
      "v_mfma_f32_16x16x32_bf16 v[88:91], a[32:35], a[124:127], v[88:91]\n\t"
      "v_fmac_f32_e32 v32, v224, v32\n\t"
      "v_fmac_f32_e32 v33, v225, v33\n\t"
      "v_mfma_f32_16x16x32_bf16 v[64:67], a[72:75], a[144:147], v[64:67]\n\t"
      "v_fmac_f32_e32 v34, v226, v34\n\t"
      "v_fmac_f32_e32 v35, v227, v35\n\t"
      "v_mfma_f32_16x16x32_bf16 v[64:67], a[80:83], a[148:151], v[64:67]\n\t"
      "v_cvt_pk_bf16_f32 v36, v32, v33\n\t"
      "v_cvt_pk_bf16_f32 v37, v34, v35\n\t"
      "v_mfma_f32_16x16x32_bf16 v[64:67], a[88:91], a[152:155], v[64:67]\n\t"
      "ds_read_b128 a[112:115], %[lx0] offset:4096\n\t"
      "v_mul_f32_e64 v228, s84, |v40|\n\t"
      "v_mfma_f32_16x16x32_bf16 v[64:67], a[96:99], a[156:159], v[64:67]\n\t"
      "s_waitcnt lgkmcnt(4)\n\t"
      "v_exp_f32_e32 v228, v228\n\t"
      "v_mfma_f32_16x16x32_bf16 v[64:67], a[104:107], a[160:163], v[64:67]\n\t"
      "s_mov_b32 s68, 8192\n\t"
      "v_mul_f32_e64 v229, s84, |v41|\n\t"
      "v_mul_f32_e64 v230, s84, |v42|\n\t"
      "v_mfma_f32_16x16x32_bf16 v[68:71], a[108:111], a[160:163], v[68:71]\n\t"
      "s_add_u32 m0, %[ismx], 38912\n\t"
      "buffer_store_dwordx2 v[36:37], %[vo], s[52:55], s68 offen offset:0\n\t"
      "v_mfma_f32_16x16x32_bf16 v[68:71], a[100:103], a[156:159], v[68:71]\n\t"
      "s_add_u32 s67, s76, 16384\n\t"
      "v_exp_f32_e32 v229, v229\n\t"
      "v_mfma_f32_16x16x32_bf16 v[68:71], a[92:95], a[152:155], v[68:71]\n\t"
      "buffer_load_dwordx4 %[vx], s[48:51], s67 offen lds\n\t"
      "v_mul_f32_e64 v231, s84, |v43|\n\t"
      "v_mfma_f32_16x16x32_bf16 v[68:71], a[84:87], a[148:151], v[68:71]\n\t"
      "ds_read_b128 a[116:119], %[lx0] offset:4352\n\t"
      "v_mul_f32_e32 v44, s86, v44\n\t"
      "v_mfma_f32_16x16x32_bf16 v[68:71], a[76:79], a[144:147], v[68:71]\n\t"
      "v_exp_f32_e32 v230, v230\n\t"
      "v_mfma_f32_16x16x32_bf16 v[76:79], a[76:79], a[148:151], v[76:79]\n\t"
      "v_exp_f32_e32 v231, v231\n\t"
      "v_mfma_f32_16x16x32_bf16 v[76:79], a[84:87], a[152:155], v[76:79]\n\t"
      "v_exp_f32_e32 v44, v44\n\t"
      "v_mfma_f32_16x16x32_bf16 v[76:79], a[92:95], a[156:159], v[76:79]\n\t"
      "v_mul_f32_e32 v45, s86, v45\n\t"
      "v_mul_f32_e32 v46, s86, v46\n\t"
      "v_mfma_f32_16x16x32_bf16 v[76:79], a[100:103], a[160:163], v[76:79]\n\t"
      "s_waitcnt lgkmcnt(4)\n\t"
      "v_exp_f32_e32 v45, v45\n\t"
      "v_mfma_f32_16x16x32_bf16 v[76:79], a[108:111], a[164:167], v[76:79]\n\t"
      "ds_read_b128 a[120:123], %[lx0] offset:4608\n\t"
      "v_mul_f32_e32 v47, s86, v47\n\t"
      "v_mfma_f32_16x16x32_bf16 v[72:75], a[104:107], a[164:167], v[72:75]\n\t"
      "v_exp_f32_e32 v46, v46\n\t"
      "v_mfma_f32_16x16x32_bf16 v[72:75], a[96:99], a[160:163], v[72:75]\n\t"
      "s_add_u32 m0, %[ismw], 0\n\t"
      "v_exp_f32_e32 v47, v47\n\t"
      "v_mfma_f32_16x16x32_bf16 v[72:75], a[88:91], a[156:159], v[72:75]\n\t"
      "s_add_u32 s67, s72, 0\n\t"
      "v_fma_f32 v228, v228, -0.5, -0.5\n\t"
      "v_fma_f32 v229, v229, -0.5, -0.5\n\t"
      "v_mfma_f32_16x16x32_bf16 v[72:75], a[80:83], a[152:155], v[72:75]\n\t"
      "buffer_load_dwordx4 %[vw], s[56:59], s67 offen lds\n\t"
      "v_fma_f32 v230, v230, -0.5, -0.5\n\t"
      "v_mfma_f32_16x16x32_bf16 v[72:75], a[72:75], a[148:151], v[72:75]\n\t"
      "v_fma_f32 v231, v231, -0.5, -0.5\n\t"
      "v_fma_f32 v44, v228, v44, v228\n\t"
      "v_mfma_f32_16x16x32_bf16 v[80:83], a[72:75], a[152:155], v[80:83]\n\t"
      "ds_read_b128 a[124:127], %[lx0] offset:4864\n\t"
      "v_fma_f32 v45, v229, v45, v229\n\t"
      "v_mfma_f32_16x16x32_bf16 v[80:83], a[80:83], a[156:159], v[80:83]\n\t"
      "v_rcp_f32_e32 v44, v44\n\t"
      "v_mfma_f32_16x16x32_bf16 v[80:83], a[88:91], a[160:163], v[80:83]\n\t"
      "v_rcp_f32_e32 v45, v45\n\t"
      "v_mfma_f32_16x16x32_bf16 v[80:83], a[96:99], a[164:167], v[80:83]\n\t"
      "s_waitcnt lgkmcnt(5)\n\t"
      "v_fma_f32 v46, v230, v46, v230\n\t"
      "v_fma_f32 v47, v231, v47, v231\n\t"
      "v_mfma_f32_16x16x32_bf16 v[80:83], a[104:107], a[168:171], v[80:83]\n\t"
      "v_rcp_f32_e32 v46, v46\n\t"
      "v_mfma_f32_16x16x32_bf16 v[84:87], a[108:111], a[168:171], v[84:87]\n\t"
      "v_rcp_f32_e32 v47, v47\n\t"
      "v_mfma_f32_16x16x32_bf16 v[84:87], a[100:103], a[164:167], v[84:87]\n\t"
      "v_bfi_b32 v40, s75, v44, v40\n\t"
      "v_bfi_b32 v41, s75, v45, v41\n\t"
      "v_mfma_f32_16x16x32_bf16 v[84:87], a[92:95], a[160:163], v[84:87]\n\t"
      "v_bfi_b32 v42, s75, v46, v42\n\t"
      "v_bfi_b32 v43, s75, v47, v43\n\t"
      "v_mfma_f32_16x16x32_bf16 v[84:87], a[84:87], a[156:159], v[84:87]\n\t"
      "v_fmac_f32_e32 v40, v228, v40\n\t"
      "v_fmac_f32_e32 v41, v229, v41\n\t"
      "v_mfma_f32_16x16x32_bf16 v[84:87], a[76:79], a[152:155], v[84:87]\n\t"
      "v_fmac_f32_e32 v42, v230, v42\n\t"
      "v_fmac_f32_e32 v43, v231, v43\n\t"
      "v_mfma_f32_16x16x32_bf16 v[92:95], a[76:79], a[156:159], v[92:95]\n\t"
      "buffer_load_dwordx4 %[vw], s[56:59], s67 offen offset:1024 lds\n\t"
      "v_cvt_pk_bf16_f32 v44, v40, v41\n\t"
      "v_mfma_f32_16x16x32_bf16 v[92:95], a[84:87], a[160:163], v[92:95]\n\t"
      "s_mov_b32 s68, 8192\n\t"
      "v_cvt_pk_bf16_f32 v45, v42, v43\n\t"
      "v_mul_f32_e64 v224, s84, |v48|\n\t"
      "v_mfma_f32_16x16x32_bf16 v[92:95], a[92:95], a[164:167], v[92:95]\n\t"
      "buffer_store_dwordx2 v[44:45], %[vo], s[52:55], s68 offen offset:2048\n\t"
      "v_mfma_f32_16x16x32_bf16 v[92:95], a[100:103], a[168:171], v[92:95]\n\t"
      "s_waitcnt lgkmcnt(4)\n\t"
      "v_exp_f32_e32 v224, v224\n\t"
      "v_mfma_f32_16x16x32_bf16 v[92:95], a[108:111], a[172:175], v[92:95]\n\t"
      "v_mul_f32_e64 v225, s84, |v49|\n\t"
      "v_mul_f32_e64 v226, s84, |v50|\n\t"
      "v_mfma_f32_16x16x32_bf16 v[88:91], a[104:107], a[172:175], v[88:91]\n\t"
      "v_exp_f32_e32 v225, v225\n\t"
      "v_mfma_f32_16x16x32_bf16 v[88:91], a[96:99], a[168:171], v[88:91]\n\t"
      "v_exp_f32_e32 v226, v226\n\t"
      "v_mfma_f32_16x16x32_bf16 v[88:91], a[88:91], a[164:167], v[88:91]\n\t"
      "v_mul_f32_e64 v227, s84, |v51|\n\t"
      "v_mul_f32_e32 v52, s86, v52\n\t"
      "v_mfma_f32_16x16x32_bf16 v[88:91], a[80:83], a[160:163], v[88:91]\n\t"
      "v_exp_f32_e32 v227, v227\n\t"
      "v_mfma_f32_16x16x32_bf16 v[88:91], a[72:75], a[156:159], v[88:91]\n\t"
      "s_add_u32 m0, %[ismw], 8192\n\t"
      "v_exp_f32_e32 v52, v52\n\t"
      "v_mfma_f32_16x16x32_bf16 v[96:99], a[32:35], a[128:131], v[96:99]\n\t"
      "s_add_u32 s67, s72, 1048576\n\t"
      "v_mul_f32_e32 v53, s86, v53\n\t"
      "v_mul_f32_e32 v54, s86, v54\n\t"
      "v_mfma_f32_16x16x32_bf16 v[96:99], a[40:43], a[132:135], v[96:99]\n\t"
      "buffer_load_dwordx4 %[vw], s[56:59], s67 offen lds\n\t"
      "v_mul_f32_e32 v55, s86, v55\n\t"
      "v_mfma_f32_16x16x32_bf16 v[96:99], a[48:51], a[136:139], v[96:99]\n\t"
      "ds_read_b128 a[144:147], %[lx1] offset:4096\n\t"
      "v_fma_f32 v224, v224, -0.5, -0.5\n\t"
      "v_mfma_f32_16x16x32_bf16 v[96:99], a[56:59], a[140:143], v[96:99]\n\t"
      "s_waitcnt lgkmcnt(4)\n\t"
      "v_exp_f32_e32 v53, v53\n\t"
      "v_mfma_f32_16x16x32_bf16 v[96:99], a[64:67], a[112:115], v[96:99]\n\t"
      "v_exp_f32_e32 v54, v54\n\t"
      "v_mfma_f32_16x16x32_bf16 v[100:103], a[68:71], a[112:115], v[100:103]\n\t"
      "v_exp_f32_e32 v55, v55\n\t"
      "v_mfma_f32_16x16x32_bf16 v[100:103], a[60:63], a[140:143], v[100:103]\n\t"
      "v_fma_f32 v225, v225, -0.5, -0.5\n\t"
      "v_fma_f32 v226, v226, -0.5, -0.5\n\t"
      "v_mfma_f32_16x16x32_bf16 v[100:103], a[52:55], a[136:139], v[100:103]\n\t"
      "v_fma_f32 v227, v227, -0.5, -0.5\n\t"
      "v_fma_f32 v52, v224, v52, v224\n\t"
      "v_mfma_f32_16x16x32_bf16 v[100:103], a[44:47], a[132:135], v[100:103]\n\t"
      "ds_read_b128 a[148:151], %[lx1] offset:4352\n\t"
      "v_fma_f32 v53, v225, v53, v225\n\t"
      "v_mfma_f32_16x16x32_bf16 v[100:103], a[36:39], a[128:131], v[100:103]\n\t"
      "v_rcp_f32_e32 v52, v52\n\t"
      "v_mfma_f32_16x16x32_bf16 v[108:111], a[36:39], a[132:135], v[108:111]\n\t"
      "v_rcp_f32_e32 v53, v53\n\t"
      "v_mfma_f32_16x16x32_bf16 v[108:111], a[44:47], a[136:139], v[108:111]\n\t"
      "v_fma_f32 v54, v226, v54, v226\n\t"
      "v_fma_f32 v55, v227, v55, v227\n\t"
      "v_mfma_f32_16x16x32_bf16 v[108:111], a[52:55], a[140:143], v[108:111]\n\t"
      "v_rcp_f32_e32 v54, v54\n\t"
      "v_mfma_f32_16x16x32_bf16 v[108:111], a[60:63], a[112:115], v[108:111]\n\t"
      "s_waitcnt lgkmcnt(4)\n\t"
      "buffer_load_dwordx4 %[vw], s[56:59], s67 offen offset:1024 lds\n\t"
      "v_bfi_b32 v48, s75, v52, v48\n\t"
      "v_mfma_f32_16x16x32_bf16 v[108:111], a[68:71], a[116:119], v[108:111]\n\t"
      "ds_read_b128 a[152:155], %[lx1] offset:4608\n\t"
      "v_bfi_b32 v49, s75, v53, v49\n\t"
      "v_mfma_f32_16x16x32_bf16 v[104:107], a[64:67], a[116:119], v[104:107]\n\t"
      "v_rcp_f32_e32 v55, v55\n\t"
      "v_mfma_f32_16x16x32_bf16 v[104:107], a[56:59], a[112:115], v[104:107]\n\t"
      "v_bfi_b32 v50, s75, v54, v50\n\t"
      "v_bfi_b32 v51, s75, v55, v51\n\t"
      "v_mfma_f32_16x16x32_bf16 v[104:107], a[48:51], a[140:143], v[104:107]\n\t"
      "v_fmac_f32_e32 v48, v224, v48\n\t"
      "v_fmac_f32_e32 v49, v225, v49\n\t"
      "v_mfma_f32_16x16x32_bf16 v[104:107], a[40:43], a[136:139], v[104:107]\n\t"
      "v_fmac_f32_e32 v50, v226, v50\n\t"
      "v_fmac_f32_e32 v51, v227, v51\n\t"
      "v_mfma_f32_16x16x32_bf16 v[104:107], a[32:35], a[132:135], v[104:107]\n\t"
      "v_cvt_pk_bf16_f32 v52, v48, v49\n\t"
      "v_cvt_pk_bf16_f32 v53, v50, v51\n\t"
      "v_mfma_f32_16x16x32_bf16 v[112:115], a[32:35], a[136:139], v[112:115]\n\t"
      "ds_read_b128 a[156:159], %[lx1] offset:4864\n\t"
      "v_mul_f32_e64 v228, s84, |v56|\n\t"
      "v_mfma_f32_16x16x32_bf16 v[112:115], a[40:43], a[140:143], v[112:115]\n\t"
      "s_mov_b32 s68, 12288\n\t"
      "v_exp_f32_e32 v228, v228\n\t"
      "v_mfma_f32_16x16x32_bf16 v[112:115], a[48:51], a[112:115], v[112:115]\n\t"
      "buffer_store_dwordx2 v[52:53], %[vo], s[52:55], s68 offen offset:0\n\t"
      "v_mfma_f32_16x16x32_bf16 v[112:115], a[56:59], a[116:119], v[112:115]\n\t"
      "s_waitcnt lgkmcnt(5)\n\t"
      "s_add_u32 m0, %[ismw], 16384\n\t"
      "v_mul_f32_e64 v229, s84, |v57|\n\t"
      "v_mfma_f32_16x16x32_bf16 v[112:115], a[64:67], a[120:123], v[112:115]\n\t"
      "s_add_u32 s67, s72, 2097152\n\t"
      "v_exp_f32_e32 v229, v229\n\t"
      "v_mfma_f32_16x16x32_bf16 v[116:119], a[68:71], a[120:123], v[116:119]\n\t"
      "buffer_load_dwordx4 %[vw], s[56:59], s67 offen lds\n\t"
      "v_mul_f32_e64 v230, s84, |v58|\n\t"
      "v_mfma_f32_16x16x32_bf16 v[116:119], a[60:63], a[116:119], v[116:119]\n\t"
      "v_exp_f32_e32 v230, v230\n\t"
      "v_mfma_f32_16x16x32_bf16 v[116:119], a[52:55], a[112:115], v[116:119]\n\t"
      "v_mul_f32_e64 v231, s84, |v59|\n\t"
      "v_mul_f32_e32 v60, s86, v60\n\t"
      "v_mfma_f32_16x16x32_bf16 v[116:119], a[44:47], a[140:143], v[116:119]\n\t"
      "v_exp_f32_e32 v231, v231\n\t"
      "v_mfma_f32_16x16x32_bf16 v[116:119], a[36:39], a[136:139], v[116:119]\n\t"
      "v_exp_f32_e32 v60, v60\n\t"
      "v_mfma_f32_16x16x32_bf16 v[124:127], a[36:39], a[140:143], v[124:127]\n\t"
      "v_mul_f32_e32 v61, s86, v61\n\t"
      "v_mul_f32_e32 v62, s86, v62\n\t"
      "v_mfma_f32_16x16x32_bf16 v[124:127], a[44:47], a[112:115], v[124:127]\n\t"
      "v_exp_f32_e32 v61, v61\n\t"
      "v_mfma_f32_16x16x32_bf16 v[124:127], a[52:55], a[116:119], v[124:127]\n\t"
      "v_exp_f32_e32 v62, v62\n\t"
      "v_mfma_f32_16x16x32_bf16 v[124:127], a[60:63], a[120:123], v[124:127]\n\t"
      "s_waitcnt lgkmcnt(4)\n\t"
      "v_mul_f32_e32 v63, s86, v63\n\t"
      "v_fma_f32 v228, v228, -0.5, -0.5\n\t"
      "v_mfma_f32_16x16x32_bf16 v[124:127], a[68:71], a[124:127], v[124:127]\n\t"
      "v_exp_f32_e32 v63, v63\n\t"
      "v_mfma_f32_16x16x32_bf16 v[120:123], a[64:67], a[124:127], v[120:123]\n\t"
      "v_fma_f32 v229, v229, -0.5, -0.5\n\t"
      "v_fma_f32 v230, v230, -0.5, -0.5\n\t"
      "v_mfma_f32_16x16x32_bf16 v[120:123], a[56:59], a[120:123], v[120:123]\n\t"
      "buffer_load_dwordx4 %[vw], s[56:59], s67 offen offset:1024 lds\n\t"
      "v_fma_f32 v231, v231, -0.5, -0.5\n\t"
      "v_mfma_f32_16x16x32_bf16 v[120:123], a[48:51], a[116:119], v[120:123]\n\t"
      "v_fma_f32 v60, v228, v60, v228\n\t"
      "v_fma_f32 v61, v229, v61, v229\n\t"
      "v_mfma_f32_16x16x32_bf16 v[120:123], a[40:43], a[112:115], v[120:123]\n\t"
      "v_rcp_f32_e32 v60, v60\n\t"
      "v_mfma_f32_16x16x32_bf16 v[120:123], a[32:35], a[140:143], v[120:123]\n\t"
      "v_rcp_f32_e32 v61, v61\n\t"
      "v_mfma_f32_16x16x32_bf16 v[96:99], a[72:75], a[160:163], v[96:99]\n\t"
      "v_fma_f32 v62, v230, v62, v230\n\t"
      "v_fma_f32 v63, v231, v63, v231\n\t"
      "v_mfma_f32_16x16x32_bf16 v[96:99], a[80:83], a[164:167], v[96:99]\n\t"
      "v_rcp_f32_e32 v62, v62\n\t"
      "v_mfma_f32_16x16x32_bf16 v[96:99], a[88:91], a[168:171], v[96:99]\n\t"
      "ds_read_b128 a[128:131], %[lx0] offset:5120\n\t"
      "v_bfi_b32 v56, s75, v60, v56\n\t"
      "v_mfma_f32_16x16x32_bf16 v[96:99], a[96:99], a[172:175], v[96:99]\n\t"
      "s_waitcnt lgkmcnt(4)\n\t"
      "v_rcp_f32_e32 v63, v63\n\t"
      "v_mfma_f32_16x16x32_bf16 v[96:99], a[104:107], a[144:147], v[96:99]\n\t"
      "v_bfi_b32 v57, s75, v61, v57\n\t"
      "v_bfi_b32 v58, s75, v62, v58\n\t"
      "v_mfma_f32_16x16x32_bf16 v[100:103], a[108:111], a[144:147], v[100:103]\n\t"
      "v_bfi_b32 v59, s75, v63, v59\n\t"
      "v_fmac_f32_e32 v56, v228, v56\n\t"
      "v_mfma_f32_16x16x32_bf16 v[100:103], a[100:103], a[172:175], v[100:103]\n\t"
      "v_fmac_f32_e32 v57, v229, v57\n\t"
      "v_fmac_f32_e32 v58, v230, v58\n\t"
      "v_mfma_f32_16x16x32_bf16 v[100:103], a[92:95], a[168:171], v[100:103]\n\t"
      "s_add_u32 m0, %[ismw], 24576\n\t"
      "v_fmac_f32_e32 v59, v231, v59\n\t"
      "v_cvt_pk_bf16_f32 v60, v56, v57\n\t"
      "v_mfma_f32_16x16x32_bf16 v[100:103], a[84:87], a[164:167], v[100:103]\n\t"
      "s_add_u32 s67, s72, 3145728\n\t"
      "ds_read_b128 a[132:135], %[lx0] offset:5376\n\t"
      "v_cvt_pk_bf16_f32 v61, v58, v59\n\t"
      "v_mfma_f32_16x16x32_bf16 v[100:103], a[76:79], a[160:163], v[100:103]\n\t"
      "buffer_load_dwordx4 %[vw], s[56:59], s67 offen lds\n\t"
      "v_mul_f32_e64 v224, s84, |v64|\n\t"
      "v_mfma_f32_16x16x32_bf16 v[108:111], a[76:79], a[164:167], v[108:111]\n\t"
      "s_mov_b32 s68, 12288\n\t"
      "v_exp_f32_e32 v224, v224\n\t"
      "v_mfma_f32_16x16x32_bf16 v[108:111], a[84:87], a[168:171], v[108:111]\n\t"
      "buffer_store_dwordx2 v[60:61], %[vo], s[52:55], s68 offen offset:2048\n\t"
      "v_mfma_f32_16x16x32_bf16 v[108:111], a[92:95], a[172:175], v[108:111]\n\t"
      "v_mul_f32_e64 v225, s84, |v65|\n\t"
      "v_mul_f32_e64 v226, s84, |v66|\n\t"
      "v_mfma_f32_16x16x32_bf16 v[108:111], a[100:103], a[144:147], v[108:111]\n\t"
      "s_waitcnt lgkmcnt(4)\n\t"
      "v_exp_f32_e32 v225, v225\n\t"
      "v_mfma_f32_16x16x32_bf16 v[108:111], a[108:111], a[148:151], v[108:111]\n\t"
      "ds_read_b128 a[136:139], %[lx0] offset:5632\n\t"
      "v_mul_f32_e64 v227, s84, |v67|\n\t"
      "v_mfma_f32_16x16x32_bf16 v[104:107], a[104:107], a[148:151], v[104:107]\n\t"
      "v_exp_f32_e32 v226, v226\n\t"
      "v_mfma_f32_16x16x32_bf16 v[104:107], a[96:99], a[144:147], v[104:107]\n\t"
      "v_exp_f32_e32 v227, v227\n\t"
      "v_mfma_f32_16x16x32_bf16 v[104:107], a[88:91], a[172:175], v[104:107]\n\t"
      "v_mul_f32_e32 v68, s86, v68\n\t"
      "v_mul_f32_e32 v69, s86, v69\n\t"
      "v_mfma_f32_16x16x32_bf16 v[104:107], a[80:83], a[168:171], v[104:107]\n\t"
      "v_exp_f32_e32 v68, v68\n\t"
      "v_mfma_f32_16x16x32_bf16 v[104:107], a[72:75], a[164:167], v[104:107]\n\t"
      "v_exp_f32_e32 v69, v69\n\t"
      "v_mfma_f32_16x16x32_bf16 v[112:115], a[72:75], a[168:171], v[112:115]\n\t"
      "s_sub_u32 s70, s70, 1\n\t"
      "ds_read_b128 a[140:143], %[lx0] offset:5888\n\t"
      "v_mul_f32_e32 v70, s86, v70\n\t"
      "v_mfma_f32_16x16x32_bf16 v[112:115], a[80:83], a[172:175], v[112:115]\n\t"
      "buffer_load_dwordx4 %[vw], s[56:59], s67 offen offset:1024 lds\n\t"
      "v_mul_f32_e32 v71, s86, v71\n\t"
      "v_mfma_f32_16x16x32_bf16 v[112:115], a[88:91], a[144:147], v[112:115]\n\t"
      "v_exp_f32_e32 v70, v70\n\t"
      "v_mfma_f32_16x16x32_bf16 v[112:115], a[96:99], a[148:151], v[112:115]\n\t"
      "s_waitcnt lgkmcnt(5)\n\t"
      "v_exp_f32_e32 v71, v71\n\t"
      "v_mfma_f32_16x16x32_bf16 v[112:115], a[104:107], a[152:155], v[112:115]\n\t"
      "v_fma_f32 v224, v224, -0.5, -0.5\n\t"
      "v_fma_f32 v225, v225, -0.5, -0.5\n\t"
      "v_mfma_f32_16x16x32_bf16 v[116:119], a[108:111], a[152:155], v[116:119]\n\t"
      "v_fma_f32 v226, v226, -0.5, -0.5\n\t"
      "v_fma_f32 v227, v227, -0.5, -0.5\n\t"
      "v_mfma_f32_16x16x32_bf16 v[116:119], a[100:103], a[148:151], v[116:119]\n\t"
      "v_fma_f32 v68, v224, v68, v224\n\t"
      "v_fma_f32 v69, v225, v69, v225\n\t"
      "v_mfma_f32_16x16x32_bf16 v[116:119], a[92:95], a[144:147], v[116:119]\n\t"
      "v_rcp_f32_e32 v68, v68\n\t"
      "v_mfma_f32_16x16x32_bf16 v[116:119], a[84:87], a[172:175], v[116:119]\n\t"
      "v_rcp_f32_e32 v69, v69\n\t"
      "v_mfma_f32_16x16x32_bf16 v[116:119], a[76:79], a[168:171], v[116:119]\n\t"
      "s_add_u32 m0, %[ismw], 32768\n\t"
      "v_fma_f32 v70, v226, v70, v226\n\t"
      "v_fma_f32 v71, v227, v71, v227\n\t"
      "v_mfma_f32_16x16x32_bf16 v[124:127], a[76:79], a[172:175], v[124:127]\n\t"
      "s_add_u32 s67, s72, 4194304\n\t"
      "v_rcp_f32_e32 v70, v70\n\t"
      "v_mfma_f32_16x16x32_bf16 v[124:127], a[84:87], a[144:147], v[124:127]\n\t"
      "buffer_load_dwordx4 %[vw], s[56:59], s67 offen lds\n\t"
      "v_bfi_b32 v64, s75, v68, v64\n\t"
      "v_mfma_f32_16x16x32_bf16 v[124:127], a[92:95], a[148:151], v[124:127]\n\t"
      "v_rcp_f32_e32 v71, v71\n\t"
      "v_mfma_f32_16x16x32_bf16 v[124:127], a[100:103], a[152:155], v[124:127]\n\t"
      "s_waitcnt lgkmcnt(4)\n\t"
      "v_bfi_b32 v65, s75, v69, v65\n\t"
      "v_bfi_b32 v66, s75, v70, v66\n\t"
      "v_mfma_f32_16x16x32_bf16 v[124:127], a[108:111], a[156:159], v[124:127]\n\t"
      "v_bfi_b32 v67, s75, v71, v67\n\t"
      "v_fmac_f32_e32 v64, v224, v64\n\t"
      "v_mfma_f32_16x16x32_bf16 v[120:123], a[104:107], a[156:159], v[120:123]\n\t"
      "v_fmac_f32_e32 v65, v225, v65\n\t"
      "v_fmac_f32_e32 v66, v226, v66\n\t"
      "v_mfma_f32_16x16x32_bf16 v[120:123], a[96:99], a[152:155], v[120:123]\n\t"
      "v_fmac_f32_e32 v67, v227, v67\n\t"
      "v_cvt_pk_bf16_f32 v68, v64, v65\n\t"
      "v_mfma_f32_16x16x32_bf16 v[120:123], a[88:91], a[148:151], v[120:123]\n\t"
      "s_mov_b32 s68, 16384\n\t"
      "v_cvt_pk_bf16_f32 v69, v66, v67\n\t"
      "v_mul_f32_e64 v228, s84, |v72|\n\t"
      "v_mfma_f32_16x16x32_bf16 v[120:123], a[80:83], a[144:147], v[120:123]\n\t"
      "buffer_store_dwordx2 v[68:69], %[vo], s[52:55], s68 offen offset:0\n\t"
      "v_mfma_f32_16x16x32_bf16 v[120:123], a[72:75], a[172:175], v[120:123]\n\t"
      "v_exp_f32_e32 v228, v228\n\t"
      "v_mfma_f32_16x16x32_bf16 v[128:131], a[32:35], a[112:115], v[128:131]\n\t"
      "v_mul_f32_e64 v229, s84, |v73|\n\t"
      "v_mul_f32_e64 v230, s84, |v74|\n\t"
      "v_mfma_f32_16x16x32_bf16 v[128:131], a[40:43], a[116:119], v[128:131]\n\t"
      "v_exp_f32_e32 v229, v229\n\t"
      "v_mfma_f32_16x16x32_bf16 v[128:131], a[48:51], a[120:123], v[128:131]\n\t"
      "ds_read_b128 a[160:163], %[lx1] offset:5120\n\t"
      "v_mul_f32_e64 v231, s84, |v75|\n\t"
      "v_mfma_f32_16x16x32_bf16 v[128:131], a[56:59], a[124:127], v[128:131]\n\t"
      "s_waitcnt lgkmcnt(4)\n\t"
      "buffer_load_dwordx4 %[vw], s[56:59], s67 offen offset:1024 lds\n\t"
      "v_mul_f32_e32 v76, s86, v76\n\t"
      "v_mfma_f32_16x16x32_bf16 v[128:131], a[64:67], a[128:131], v[128:131]\n\t"
      "v_exp_f32_e32 v230, v230\n\t"
      "v_mfma_f32_16x16x32_bf16 v[132:135], a[68:71], a[128:131], v[132:135]\n\t"
      "v_exp_f32_e32 v231, v231\n\t"
      "v_mfma_f32_16x16x32_bf16 v[132:135], a[60:63], a[124:127], v[132:135]\n\t"
      "v_exp_f32_e32 v76, v76\n\t"
      "v_mfma_f32_16x16x32_bf16 v[132:135], a[52:55], a[120:123], v[132:135]\n\t"
      "v_mul_f32_e32 v77, s86, v77\n\t"
      "v_mul_f32_e32 v78, s86, v78\n\t"
      "v_mfma_f32_16x16x32_bf16 v[132:135], a[44:47], a[116:119], v[132:135]\n\t"
      "ds_read_b128 a[164:167], %[lx1] offset:5376\n\t"
      "v_mul_f32_e32 v79, s86, v79\n\t"
      "v_mfma_f32_16x16x32_bf16 v[132:135], a[36:39], a[112:115], v[132:135]\n\t"
      "v_exp_f32_e32 v77, v77\n\t"
      "v_mfma_f32_16x16x32_bf16 v[140:143], a[36:39], a[116:119], v[140:143]\n\t"
      "v_exp_f32_e32 v78, v78\n\t"
      "v_mfma_f32_16x16x32_bf16 v[140:143], a[44:47], a[120:123], v[140:143]\n\t"
      "v_exp_f32_e32 v79, v79\n\t"
      "v_mfma_f32_16x16x32_bf16 v[140:143], a[52:55], a[124:127], v[140:143]\n\t"
      "v_fma_f32 v228, v228, -0.5, -0.5\n\t"
      "v_fma_f32 v229, v229, -0.5, -0.5\n\t"
      "v_mfma_f32_16x16x32_bf16 v[140:143], a[60:63], a[128:131], v[140:143]\n\t"
      "s_waitcnt lgkmcnt(4)\n\t"
      "s_add_u32 m0, %[ismw], 40960\n\t"
      "v_fma_f32 v230, v230, -0.5, -0.5\n\t"
      "v_mfma_f32_16x16x32_bf16 v[140:143], a[68:71], a[132:135], v[140:143]\n\t"
      "s_add_u32 s67, s72, 65536\n\t"
      "ds_read_b128 a[168:171], %[lx1] offset:5632\n\t"
      "v_fma_f32 v231, v231, -0.5, -0.5\n\t"
      "v_mfma_f32_16x16x32_bf16 v[136:139], a[64:67], a[132:135], v[136:139]\n\t"
      "buffer_load_dwordx4 %[vw], s[56:59], s67 offen lds\n\t"
      "v_fma_f32 v76, v228, v76, v228\n\t"
      "v_mfma_f32_16x16x32_bf16 v[136:139], a[56:59], a[128:131], v[136:139]\n\t"
      "v_rcp_f32_e32 v76, v76\n\t"
      "v_mfma_f32_16x16x32_bf16 v[136:139], a[48:51], a[124:127], v[136:139]\n\t"
      "v_fma_f32 v77, v229, v77, v229\n\t"
      "v_fma_f32 v78, v230, v78, v230\n\t"
      "v_mfma_f32_16x16x32_bf16 v[136:139], a[40:43], a[120:123], v[136:139]\n\t"
      "v_rcp_f32_e32 v77, v77\n\t"
      "v_mfma_f32_16x16x32_bf16 v[136:139], a[32:35], a[116:119], v[136:139]\n\t"
      "v_rcp_f32_e32 v78, v78\n\t"
      "v_mfma_f32_16x16x32_bf16 v[144:147], a[32:35], a[120:123], v[144:147]\n\t"
      "ds_read_b128 a[172:175], %[lx1] offset:5888\n\t"
      "v_fma_f32 v79, v231, v79, v231\n\t"
      "v_mfma_f32_16x16x32_bf16 v[144:147], a[40:43], a[124:127], v[144:147]\n\t"
      "v_rcp_f32_e32 v79, v79\n\t"
      "v_mfma_f32_16x16x32_bf16 v[144:147], a[48:51], a[128:131], v[144:147]\n\t"
      "v_bfi_b32 v72, s75, v76, v72\n\t"
      "v_bfi_b32 v73, s75, v77, v73\n\t"
      "v_mfma_f32_16x16x32_bf16 v[144:147], a[56:59], a[132:135], v[144:147]\n\t"
      "s_waitcnt lgkmcnt(5)\n\t"
      "v_bfi_b32 v74, s75, v78, v74\n\t"
      "v_bfi_b32 v75, s75, v79, v75\n\t"
      "v_mfma_f32_16x16x32_bf16 v[144:147], a[64:67], a[136:139], v[144:147]\n\t"
      "v_fmac_f32_e32 v72, v228, v72\n\t"
      "v_fmac_f32_e32 v73, v229, v73\n\t"
      "v_mfma_f32_16x16x32_bf16 v[148:151], a[68:71], a[136:139], v[148:151]\n\t"
      "v_fmac_f32_e32 v74, v230, v74\n\t"
      "v_fmac_f32_e32 v75, v231, v75\n\t"
      "v_mfma_f32_16x16x32_bf16 v[148:151], a[60:63], a[132:135], v[148:151]\n\t"
      "buffer_load_dwordx4 %[vw], s[56:59], s67 offen offset:1024 lds\n\t"
      "v_cvt_pk_bf16_f32 v76, v72, v73\n\t"
      "v_mfma_f32_16x16x32_bf16 v[148:151], a[52:55], a[128:131], v[148:151]\n\t"
      "s_mov_b32 s68, 16384\n\t"
      "v_cvt_pk_bf16_f32 v77, v74, v75\n\t"
      "v_mul_f32_e64 v224, s84, |v80|\n\t"
      "v_mfma_f32_16x16x32_bf16 v[148:151], a[44:47], a[124:127], v[148:151]\n\t"
      "buffer_store_dwordx2 v[76:77], %[vo], s[52:55], s68 offen offset:2048\n\t"
      "v_mfma_f32_16x16x32_bf16 v[148:151], a[36:39], a[120:123], v[148:151]\n\t"
      "v_exp_f32_e32 v224, v224\n\t"
      "v_mfma_f32_16x16x32_bf16 v[156:159], a[36:39], a[124:127], v[156:159]\n\t"
      "v_mul_f32_e64 v225, s84, |v81|\n\t"
      "v_mul_f32_e64 v226, s84, |v82|\n\t"
      "v_mfma_f32_16x16x32_bf16 v[156:159], a[44:47], a[128:131], v[156:159]\n\t"
      "v_exp_f32_e32 v225, v225\n\t"
      "v_mfma_f32_16x16x32_bf16 v[156:159], a[52:55], a[132:135], v[156:159]\n\t"
      "v_exp_f32_e32 v226, v226\n\t"
      "v_mfma_f32_16x16x32_bf16 v[156:159], a[60:63], a[136:139], v[156:159]\n\t"
      "s_waitcnt lgkmcnt(4)\n\t"
      "v_mul_f32_e64 v227, s84, |v83|\n\t"
      "v_mul_f32_e32 v84, s86, v84\n\t"
      "v_mfma_f32_16x16x32_bf16 v[156:159], a[68:71], a[140:143], v[156:159]\n\t"
      "v_exp_f32_e32 v227, v227\n\t"
      "v_mfma_f32_16x16x32_bf16 v[152:155], a[64:67], a[140:143], v[152:155]\n\t"
      "v_exp_f32_e32 v84, v84\n\t"
      "v_mfma_f32_16x16x32_bf16 v[152:155], a[56:59], a[136:139], v[152:155]\n\t"
      "s_add_u32 m0, %[ismw], 49152\n\t"
      "v_mul_f32_e32 v85, s86, v85\n\t"
      "v_mul_f32_e32 v86, s86, v86\n\t"
      "v_mfma_f32_16x16x32_bf16 v[152:155], a[48:51], a[132:135], v[152:155]\n\t"
      "s_add_u32 s67, s72, 1114112\n\t"
      "v_exp_f32_e32 v85, v85\n\t"
      "v_mfma_f32_16x16x32_bf16 v[152:155], a[40:43], a[128:131], v[152:155]\n\t"
      "buffer_load_dwordx4 %[vw], s[56:59], s67 offen lds\n\t"
      "v_mul_f32_e32 v87, s86, v87\n\t"
      "v_mfma_f32_16x16x32_bf16 v[152:155], a[32:35], a[124:127], v[152:155]\n\t"
      "s_waitcnt vmcnt(27) lgkmcnt(0)\n\t"
      "s_barrier\n\t"
      "v_fma_f32 v224, v224, -0.5, -0.5\n\t"
      "v_mfma_f32_16x16x32_bf16 v[128:131], a[72:75], a[144:147], v[128:131]\n\t"
      "v_exp_f32_e32 v86, v86\n\t"
      "v_mfma_f32_16x16x32_bf16 v[128:131], a[80:83], a[148:151], v[128:131]\n\t"
      "v_exp_f32_e32 v87, v87\n\t"
      "v_mfma_f32_16x16x32_bf16 v[128:131], a[88:91], a[152:155], v[128:131]\n\t"
      "ds_read_b128 a[112:115], %[lx0] offset:6144\n\t"
      "v_fma_f32 v225, v225, -0.5, -0.5\n\t"
      "v_mfma_f32_16x16x32_bf16 v[128:131], a[96:99], a[156:159], v[128:131]\n\t"
      "v_fma_f32 v226, v226, -0.5, -0.5\n\t"
      "v_fma_f32 v227, v227, -0.5, -0.5\n\t"
      "v_mfma_f32_16x16x32_bf16 v[128:131], a[104:107], a[160:163], v[128:131]\n\t"
      "v_fma_f32 v84, v224, v84, v224\n\t"
      "v_fma_f32 v85, v225, v85, v225\n\t"
      "v_mfma_f32_16x16x32_bf16 v[132:135], a[108:111], a[160:163], v[132:135]\n\t"
      "v_rcp_f32_e32 v84, v84\n\t"
      "v_mfma_f32_16x16x32_bf16 v[132:135], a[100:103], a[156:159], v[132:135]\n\t"
      "v_rcp_f32_e32 v85, v85\n\t"
      "v_mfma_f32_16x16x32_bf16 v[132:135], a[92:95], a[152:155], v[132:135]\n\t"
      "v_fma_f32 v86, v226, v86, v226\n\t"
      "v_fma_f32 v87, v227, v87, v227\n\t"
      "v_mfma_f32_16x16x32_bf16 v[132:135], a[84:87], a[148:151], v[132:135]\n\t"
      "ds_read_b128 a[116:119], %[lx0] offset:6400\n\t"
      "v_bfi_b32 v80, s75, v84, v80\n\t"
      "v_mfma_f32_16x16x32_bf16 v[132:135], a[76:79], a[144:147], v[132:135]\n\t"
      "buffer_load_dwordx4 %[vw], s[56:59], s67 offen offset:1024 lds\n\t"
      "v_bfi_b32 v81, s75, v85, v81\n\t"
      "v_mfma_f32_16x16x32_bf16 v[140:143], a[76:79], a[148:151], v[140:143]\n\t"
      "v_rcp_f32_e32 v86, v86\n\t"
      "v_mfma_f32_16x16x32_bf16 v[140:143], a[84:87], a[152:155], v[140:143]\n\t"
      "v_rcp_f32_e32 v87, v87\n\t"
      "v_mfma_f32_16x16x32_bf16 v[140:143], a[92:95], a[156:159], v[140:143]\n\t"
      "v_bfi_b32 v82, s75, v86, v82\n\t"
      "v_bfi_b32 v83, s75, v87, v83\n\t"
      "v_mfma_f32_16x16x32_bf16 v[140:143], a[100:103], a[160:163], v[140:143]\n\t"
      "v_fmac_f32_e32 v80, v224, v80\n\t"
      "v_fmac_f32_e32 v81, v225, v81\n\t"
      "v_mfma_f32_16x16x32_bf16 v[140:143], a[108:111], a[164:167], v[140:143]\n\t"
      "ds_read_b128 a[120:123], %[lx0] offset:6656\n\t"
      "v_fmac_f32_e32 v82, v226, v82\n\t"
      "v_mfma_f32_16x16x32_bf16 v[136:139], a[104:107], a[164:167], v[136:139]\n\t"
      "v_fmac_f32_e32 v83, v227, v83\n\t"
      "v_cvt_pk_bf16_f32 v84, v80, v81\n\t"
      "v_mfma_f32_16x16x32_bf16 v[136:139], a[96:99], a[160:163], v[136:139]\n\t"
      "s_mov_b32 s68, 20480\n\t"
      "v_cvt_pk_bf16_f32 v85, v82, v83\n\t"
      "v_mul_f32_e64 v228, s84, |v88|\n\t"
      "v_mfma_f32_16x16x32_bf16 v[136:139], a[88:91], a[156:159], v[136:139]\n\t"
      "buffer_store_dwordx2 v[84:85], %[vo], s[52:55], s68 offen offset:0\n\t"
      "v_mfma_f32_16x16x32_bf16 v[136:139], a[80:83], a[152:155], v[136:139]\n\t"
      "v_exp_f32_e32 v228, v228\n\t"
      "v_mfma_f32_16x16x32_bf16 v[136:139], a[72:75], a[148:151], v[136:139]\n\t"
      "s_add_u32 m0, %[ismw], 57344\n\t"
      "v_mul_f32_e64 v229, s84, |v89|\n\t"
      "v_mul_f32_e64 v230, s84, |v90|\n\t"
      "v_mfma_f32_16x16x32_bf16 v[144:147], a[72:75], a[152:155], v[144:147]\n\t"
      "s_add_u32 s67, s72, 2162688\n\t"
      "ds_read_b128 a[124:127], %[lx0] offset:6912\n\t"
      "v_mul_f32_e64 v231, s84, |v91|\n\t"
      "v_mfma_f32_16x16x32_bf16 v[144:147], a[80:83], a[156:159], v[144:147]\n\t"
      "buffer_load_dwordx4 %[vw], s[56:59], s67 offen lds\n\t"
      "v_mul_f32_e32 v92, s86, v92\n\t"
      "v_mfma_f32_16x16x32_bf16 v[144:147], a[88:91], a[160:163], v[144:147]\n\t"
      "v_exp_f32_e32 v229, v229\n\t"
      "v_mfma_f32_16x16x32_bf16 v[144:147], a[96:99], a[164:167], v[144:147]\n\t"
      "v_exp_f32_e32 v230, v230\n\t"
      "v_mfma_f32_16x16x32_bf16 v[144:147], a[104:107], a[168:171], v[144:147]\n\t"
      "v_exp_f32_e32 v231, v231\n\t"
      "v_mfma_f32_16x16x32_bf16 v[148:151], a[108:111], a[168:171], v[148:151]\n\t"
      "v_exp_f32_e32 v92, v92\n\t"
      "v_mfma_f32_16x16x32_bf16 v[148:151], a[100:103], a[164:167], v[148:151]\n\t"
      "v_mul_f32_e32 v93, s86, v93\n\t"
      "v_mul_f32_e32 v94, s86, v94\n\t"
      "v_mfma_f32_16x16x32_bf16 v[148:151], a[92:95], a[160:163], v[148:151]\n\t"
      "v_exp_f32_e32 v93, v93\n\t"
      "v_mfma_f32_16x16x32_bf16 v[148:151], a[84:87], a[156:159], v[148:151]\n\t"
      "v_exp_f32_e32 v94, v94\n\t"
      "v_mfma_f32_16x16x32_bf16 v[148:151], a[76:79], a[152:155], v[148:151]\n\t"
      "v_mul_f32_e32 v95, s86, v95\n\t"
      "v_fma_f32 v228, v228, -0.5, -0.5\n\t"
      "v_mfma_f32_16x16x32_bf16 v[156:159], a[76:79], a[156:159], v[156:159]\n\t"
      "v_exp_f32_e32 v95, v95\n\t"
      "v_mfma_f32_16x16x32_bf16 v[156:159], a[84:87], a[160:163], v[156:159]\n\t"
      "v_fma_f32 v229, v229, -0.5, -0.5\n\t"
      "v_fma_f32 v230, v230, -0.5, -0.5\n\t"
      "v_mfma_f32_16x16x32_bf16 v[156:159], a[92:95], a[164:167], v[156:159]\n\t"
      "buffer_load_dwordx4 %[vw], s[56:59], s67 offen offset:1024 lds\n\t"
      "v_fma_f32 v231, v231, -0.5, -0.5\n\t"
      "v_mfma_f32_16x16x32_bf16 v[156:159], a[100:103], a[168:171], v[156:159]\n\t"
      "v_fma_f32 v92, v228, v92, v228\n\t"
      "v_fma_f32 v93, v229, v93, v229\n\t"
      "v_mfma_f32_16x16x32_bf16 v[156:159], a[108:111], a[172:175], v[156:159]\n\t"
      "v_rcp_f32_e32 v92, v92\n\t"
      "v_mfma_f32_16x16x32_bf16 v[152:155], a[104:107], a[172:175], v[152:155]\n\t"
      "v_rcp_f32_e32 v93, v93\n\t"
      "v_mfma_f32_16x16x32_bf16 v[152:155], a[96:99], a[168:171], v[152:155]\n\t"
      "v_fma_f32 v94, v230, v94, v230\n\t"
      "v_fma_f32 v95, v231, v95, v231\n\t"
      "v_mfma_f32_16x16x32_bf16 v[152:155], a[88:91], a[164:167], v[152:155]\n\t"
      "v_rcp_f32_e32 v94, v94\n\t"
      "v_mfma_f32_16x16x32_bf16 v[152:155], a[80:83], a[160:163], v[152:155]\n\t"
      "v_rcp_f32_e32 v95, v95\n\t"
      "v_mfma_f32_16x16x32_bf16 v[152:155], a[72:75], a[156:159], v[152:155]\n\t"
      "v_bfi_b32 v88, s75, v92, v88\n\t"
      "v_bfi_b32 v89, s75, v93, v89\n\t"
      "v_mfma_f32_16x16x32_bf16 v[160:163], a[32:35], a[128:131], v[160:163]\n\t"
      "v_bfi_b32 v90, s75, v94, v90\n\t"
      "v_bfi_b32 v91, s75, v95, v91\n\t"
      "v_mfma_f32_16x16x32_bf16 v[160:163], a[40:43], a[132:135], v[160:163]\n\t"
      "v_fmac_f32_e32 v88, v228, v88\n\t"
      "v_fmac_f32_e32 v89, v229, v89\n\t"
      "v_mfma_f32_16x16x32_bf16 v[160:163], a[48:51], a[136:139], v[160:163]\n\t"
      "s_add_u32 m0, %[ismw], 65536\n\t"
      "ds_read_b128 a[144:147], %[lx1] offset:6144\n\t"
      "v_fmac_f32_e32 v90, v230, v90\n\t"
      "v_mfma_f32_16x16x32_bf16 v[160:163], a[56:59], a[140:143], v[160:163]\n\t"
      "s_waitcnt lgkmcnt(4)\n\t"
      "s_add_u32 s67, s72, 3211264\n\t"
      "v_fmac_f32_e32 v91, v231, v91\n\t"
      "v_mfma_f32_16x16x32_bf16 v[160:163], a[64:67], a[112:115], v[160:163]\n\t"
      "buffer_load_dwordx4 %[vw], s[56:59], s67 offen lds\n\t"
      "v_cvt_pk_bf16_f32 v92, v88, v89\n\t"
      "v_mfma_f32_16x16x32_bf16 v[164:167], a[68:71], a[112:115], v[164:167]\n\t"
      "s_mov_b32 s68, 20480\n\t"
      "v_cvt_pk_bf16_f32 v93, v90, v91\n\t"
      "v_mul_f32_e64 v224, s84, |v96|\n\t"
      "v_mfma_f32_16x16x32_bf16 v[164:167], a[60:63], a[140:143], v[164:167]\n\t"
      "buffer_store_dwordx2 v[92:93], %[vo], s[52:55], s68 offen offset:2048\n\t"
      "v_mfma_f32_16x16x32_bf16 v[164:167], a[52:55], a[136:139], v[164:167]\n\t"
      "v_exp_f32_e32 v224, v224\n\t"
      "v_mfma_f32_16x16x32_bf16 v[164:167], a[44:47], a[132:135], v[164:167]\n\t"
      "ds_read_b128 a[148:151], %[lx1] offset:6400\n\t"
      "v_mul_f32_e64 v225, s84, |v97|\n\t"
      "v_mfma_f32_16x16x32_bf16 v[164:167], a[36:39], a[128:131], v[164:167]\n\t"
      "v_exp_f32_e32 v225, v225\n\t"
      "v_mfma_f32_16x16x32_bf16 v[172:175], a[36:39], a[132:135], v[172:175]\n\t"
      "v_mul_f32_e64 v226, s84, |v98|\n\t"
      "v_mul_f32_e64 v227, s84, |v99|\n\t"
      "v_mfma_f32_16x16x32_bf16 v[172:175], a[44:47], a[136:139], v[172:175]\n\t"
      "v_exp_f32_e32 v226, v226\n\t"
      "v_mfma_f32_16x16x32_bf16 v[172:175], a[52:55], a[140:143], v[172:175]\n\t"
      "v_exp_f32_e32 v227, v227\n\t"
      "v_mfma_f32_16x16x32_bf16 v[172:175], a[60:63], a[112:115], v[172:175]\n\t"
      "s_waitcnt lgkmcnt(4)\n\t"
      "v_mul_f32_e32 v100, s86, v100\n\t"
      "v_mul_f32_e32 v101, s86, v101\n\t"
      "v_mfma_f32_16x16x32_bf16 v[172:175], a[68:71], a[116:119], v[172:175]\n\t"
      "ds_read_b128 a[152:155], %[lx1] offset:6656\n\t"
      "v_mul_f32_e32 v102, s86, v102\n\t"
      "v_mfma_f32_16x16x32_bf16 v[168:171], a[64:67], a[116:119], v[168:171]\n\t"
      "v_exp_f32_e32 v100, v100\n\t"
      "v_mfma_f32_16x16x32_bf16 v[168:171], a[56:59], a[112:115], v[168:171]\n\t"
      "buffer_load_dwordx4 %[vw], s[56:59], s67 offen offset:1024 lds\n\t"
      "v_mul_f32_e32 v103, s86, v103\n\t"
      "v_mfma_f32_16x16x32_bf16 v[168:171], a[48:51], a[140:143], v[168:171]\n\t"
      "v_exp_f32_e32 v101, v101\n\t"
      "v_mfma_f32_16x16x32_bf16 v[168:171], a[40:43], a[136:139], v[168:171]\n\t"
      "v_exp_f32_e32 v102, v102\n\t"
      "v_mfma_f32_16x16x32_bf16 v[168:171], a[32:35], a[132:135], v[168:171]\n\t"
      "v_exp_f32_e32 v103, v103\n\t"
      "v_mfma_f32_16x16x32_bf16 v[176:179], a[32:35], a[136:139], v[176:179]\n\t"
      "ds_read_b128 a[156:159], %[lx1] offset:6912\n\t"
      "v_fma_f32 v224, v224, -0.5, -0.5\n\t"
      "v_mfma_f32_16x16x32_bf16 v[176:179], a[40:43], a[140:143], v[176:179]\n\t"
      "v_fma_f32 v225, v225, -0.5, -0.5\n\t"
      "v_fma_f32 v226, v226, -0.5, -0.5\n\t"
      "v_mfma_f32_16x16x32_bf16 v[176:179], a[48:51], a[112:115], v[176:179]\n\t"
      "v_fma_f32 v227, v227, -0.5, -0.5\n\t"
      "v_fma_f32 v100, v224, v100, v224\n\t"
      "v_mfma_f32_16x16x32_bf16 v[176:179], a[56:59], a[116:119], v[176:179]\n\t"
      "s_waitcnt lgkmcnt(5)\n\t"
      "v_rcp_f32_e32 v100, v100\n\t"
      "v_mfma_f32_16x16x32_bf16 v[176:179], a[64:67], a[120:123], v[176:179]\n\t"
      "v_fma_f32 v101, v225, v101, v225\n\t"
      "v_fma_f32 v102, v226, v102, v226\n\t"
      "v_mfma_f32_16x16x32_bf16 v[180:183], a[68:71], a[120:123], v[180:183]\n\t"
      "s_add_u32 m0, %[ismw], 73728\n\t"
      "v_rcp_f32_e32 v101, v101\n\t"
      "v_mfma_f32_16x16x32_bf16 v[180:183], a[60:63], a[116:119], v[180:183]\n\t"
      "s_add_u32 s67, s72, 4259840\n\t"
      "v_rcp_f32_e32 v102, v102\n\t"
      "v_mfma_f32_16x16x32_bf16 v[180:183], a[52:55], a[112:115], v[180:183]\n\t"
      "buffer_load_dwordx4 %[vw], s[56:59], s67 offen lds\n\t"
      "v_fma_f32 v103, v227, v103, v227\n\t"
      "v_mfma_f32_16x16x32_bf16 v[180:183], a[44:47], a[140:143], v[180:183]\n\t"
      "v_rcp_f32_e32 v103, v103\n\t"
      "v_mfma_f32_16x16x32_bf16 v[180:183], a[36:39], a[136:139], v[180:183]\n\t"
      "v_bfi_b32 v96, s75, v100, v96\n\t"
      "v_bfi_b32 v97, s75, v101, v97\n\t"
      "v_mfma_f32_16x16x32_bf16 v[188:191], a[36:39], a[140:143], v[188:191]\n\t"
      "v_bfi_b32 v98, s75, v102, v98\n\t"
      "v_bfi_b32 v99, s75, v103, v99\n\t"
      "v_mfma_f32_16x16x32_bf16 v[188:191], a[44:47], a[112:115], v[188:191]\n\t"
      "v_fmac_f32_e32 v96, v224, v96\n\t"
      "v_fmac_f32_e32 v97, v225, v97\n\t"
      "v_mfma_f32_16x16x32_bf16 v[188:191], a[52:55], a[116:119], v[188:191]\n\t"
      "v_fmac_f32_e32 v98, v226, v98\n\t"
      "v_fmac_f32_e32 v99, v227, v99\n\t"
      "v_mfma_f32_16x16x32_bf16 v[188:191], a[60:63], a[120:123], v[188:191]\n\t"
      "s_waitcnt lgkmcnt(4)\n\t"
      "v_cvt_pk_bf16_f32 v100, v96, v97\n\t"
      "v_cvt_pk_bf16_f32 v101, v98, v99\n\t"
      "v_mfma_f32_16x16x32_bf16 v[188:191], a[68:71], a[124:127], v[188:191]\n\t"
      "s_mov_b32 s68, 24576\n\t"
      "v_mul_f32_e64 v228, s84, |v104|\n\t"
      "v_mul_f32_e64 v229, s84, |v105|\n\t"
      "v_mfma_f32_16x16x32_bf16 v[184:187], a[64:67], a[124:127], v[184:187]\n\t"
      "buffer_store_dwordx2 v[100:101], %[vo], s[52:55], s68 offen offset:0\n\t"
      "v_mfma_f32_16x16x32_bf16 v[184:187], a[56:59], a[120:123], v[184:187]\n\t"
      "v_exp_f32_e32 v228, v228\n\t"
      "v_mfma_f32_16x16x32_bf16 v[184:187], a[48:51], a[116:119], v[184:187]\n\t"
      "v_exp_f32_e32 v229, v229\n\t"
      "v_mfma_f32_16x16x32_bf16 v[184:187], a[40:43], a[112:115], v[184:187]\n\t"
      "v_mul_f32_e64 v230, s84, |v106|\n\t"
      "v_mul_f32_e64 v231, s84, |v107|\n\t"
      "v_mfma_f32_16x16x32_bf16 v[184:187], a[32:35], a[140:143], v[184:187]\n\t"
      "buffer_load_dwordx4 %[vw], s[56:59], s67 offen offset:1024 lds\n\t"
      "v_mul_f32_e32 v108, s86, v108\n\t"
      "v_mfma_f32_16x16x32_bf16 v[160:163], a[72:75], a[160:163], v[160:163]\n\t"
      "v_exp_f32_e32 v230, v230\n\t"
      "v_mfma_f32_16x16x32_bf16 v[160:163], a[80:83], a[164:167], v[160:163]\n\t"
      "v_exp_f32_e32 v231, v231\n\t"
      "v_mfma_f32_16x16x32_bf16 v[160:163], a[88:91], a[168:171], v[160:163]\n\t"
      "ds_read_b128 a[128:131], %[lx0] offset:7168\n\t"
      "v_mul_f32_e32 v109, s86, v109\n\t"
      "v_mfma_f32_16x16x32_bf16 v[160:163], a[96:99], a[172:175], v[160:163]\n\t"
      "s_waitcnt lgkmcnt(4)\n\t"
      "v_exp_f32_e32 v108, v108\n\t"
      "v_mfma_f32_16x16x32_bf16 v[160:163], a[104:107], a[144:147], v[160:163]\n\t"
      "v_exp_f32_e32 v109, v109\n\t"
      "v_mfma_f32_16x16x32_bf16 v[164:167], a[108:111], a[144:147], v[164:167]\n\t"
      "v_mul_f32_e32 v110, s86, v110\n\t"
      "v_mul_f32_e32 v111, s86, v111\n\t"
      "v_mfma_f32_16x16x32_bf16 v[164:167], a[100:103], a[172:175], v[164:167]\n\t"
      "v_exp_f32_e32 v110, v110\n\t"
      "v_mfma_f32_16x16x32_bf16 v[164:167], a[92:95], a[168:171], v[164:167]\n\t"
      "v_exp_f32_e32 v111, v111\n\t"
      "v_mfma_f32_16x16x32_bf16 v[164:167], a[84:87], a[164:167], v[164:167]\n\t"
      "ds_read_b128 a[132:135], %[lx0] offset:7424\n\t"
      "v_fma_f32 v228, v228, -0.5, -0.5\n\t"
      "v_mfma_f32_16x16x32_bf16 v[164:167], a[76:79], a[160:163], v[164:167]\n\t"
      "v_fma_f32 v229, v229, -0.5, -0.5\n\t"
      "v_fma_f32 v230, v230, -0.5, -0.5\n\t"
      "v_mfma_f32_16x16x32_bf16 v[172:175], a[76:79], a[164:167], v[172:175]\n\t"
      "v_fma_f32 v231, v231, -0.5, -0.5\n\t"
      "v_fma_f32 v108, v228, v108, v228\n\t"
      "v_mfma_f32_16x16x32_bf16 v[172:175], a[84:87], a[168:171], v[172:175]\n\t"
      "v_rcp_f32_e32 v108, v108\n\t"
      "v_mfma_f32_16x16x32_bf16 v[172:175], a[92:95], a[172:175], v[172:175]\n\t"
      "v_fma_f32 v109, v229, v109, v229\n\t"
      "v_fma_f32 v110, v230, v110, v230\n\t"
      "v_mfma_f32_16x16x32_bf16 v[172:175], a[100:103], a[144:147], v[172:175]\n\t"
      "s_waitcnt lgkmcnt(4)\n\t"
      "s_add_u32 m0, %[ismx], 3072\n\t"
      "v_fma_f32 v111, v231, v111, v231\n\t"
      "v_mfma_f32_16x16x32_bf16 v[172:175], a[108:111], a[148:151], v[172:175]\n\t"
      "s_add_u32 s67, s71, 24576\n\t"
      "ds_read_b128 a[136:139], %[lx0] offset:7680\n\t"
      "v_bfi_b32 v104, s75, v108, v104\n\t"
      "v_mfma_f32_16x16x32_bf16 v[168:171], a[104:107], a[148:151], v[168:171]\n\t"
      "buffer_load_dwordx4 %[vx], s[48:51], s67 offen lds\n\t"
      "v_fmac_f32_e32 v104, v228, v104\n\t"
      "v_mfma_f32_16x16x32_bf16 v[168:171], a[96:99], a[144:147], v[168:171]\n\t"
      "v_rcp_f32_e32 v109, v109\n\t"
      "v_mfma_f32_16x16x32_bf16 v[168:171], a[88:91], a[172:175], v[168:171]\n\t"
      "v_rcp_f32_e32 v110, v110\n\t"
      "v_mfma_f32_16x16x32_bf16 v[168:171], a[80:83], a[168:171], v[168:171]\n\t"
      "v_rcp_f32_e32 v111, v111\n\t"
      "v_mfma_f32_16x16x32_bf16 v[168:171], a[72:75], a[164:167], v[168:171]\n\t"
      "v_bfi_b32 v105, s75, v109, v105\n\t"
      "v_bfi_b32 v106, s75, v110, v106\n\t"
      "v_mfma_f32_16x16x32_bf16 v[176:179], a[72:75], a[168:171], v[176:179]\n\t"
      "ds_read_b128 a[140:143], %[lx0] offset:7936\n\t"
      "v_bfi_b32 v107, s75, v111, v107\n\t"
      "v_mfma_f32_16x16x32_bf16 v[176:179], a[80:83], a[172:175], v[176:179]\n\t"
      "v_fmac_f32_e32 v105, v229, v105\n\t"
      "v_fmac_f32_e32 v106, v230, v106\n\t"
      "v_mfma_f32_16x16x32_bf16 v[176:179], a[88:91], a[144:147], v[176:179]\n\t"
      "v_fmac_f32_e32 v107, v231, v107\n\t"
      "v_cvt_pk_bf16_f32 v108, v104, v105\n\t"
      "v_mfma_f32_16x16x32_bf16 v[176:179], a[96:99], a[148:151], v[176:179]\n\t"
      "s_waitcnt lgkmcnt(5)\n\t"
      "v_cvt_pk_bf16_f32 v109, v106, v107\n\t"
      "v_mul_f32_e64 v224, s84, |v112|\n\t"
      "v_mfma_f32_16x16x32_bf16 v[176:179], a[104:107], a[152:155], v[176:179]\n\t"
      "s_mov_b32 s68, 24576\n\t"
      "v_exp_f32_e32 v224, v224\n\t"
      "v_mfma_f32_16x16x32_bf16 v[180:183], a[108:111], a[152:155], v[180:183]\n\t"
      "buffer_store_dwordx2 v[108:109], %[vo], s[52:55], s68 offen offset:2048\n\t"
      "v_mfma_f32_16x16x32_bf16 v[180:183], a[100:103], a[148:151], v[180:183]\n\t"
      "v_mul_f32_e64 v225, s84, |v113|\n\t"
      "v_mul_f32_e64 v226, s84, |v114|\n\t"
      "v_mfma_f32_16x16x32_bf16 v[180:183], a[92:95], a[144:147], v[180:183]\n\t"
      "v_exp_f32_e32 v225, v225\n\t"
      "v_mfma_f32_16x16x32_bf16 v[180:183], a[84:87], a[172:175], v[180:183]\n\t"
      "v_exp_f32_e32 v226, v226\n\t"
      "v_mfma_f32_16x16x32_bf16 v[180:183], a[76:79], a[168:171], v[180:183]\n\t"
      "s_add_u32 m0, %[ismx], 39936\n\t"
      "v_mul_f32_e64 v227, s84, |v115|\n\t"
      "v_mul_f32_e32 v116, s86, v116\n\t"
      "v_mfma_f32_16x16x32_bf16 v[188:191], a[76:79], a[172:175], v[188:191]\n\t"
      "s_add_u32 s67, s76, 24576\n\t"
      "v_exp_f32_e32 v227, v227\n\t"
      "v_mfma_f32_16x16x32_bf16 v[188:191], a[84:87], a[144:147], v[188:191]\n\t"
      "buffer_load_dwordx4 %[vx], s[48:51], s67 offen lds\n\t"
      "v_mul_f32_e32 v117, s86, v117\n\t"
      "v_mfma_f32_16x16x32_bf16 v[188:191], a[92:95], a[148:151], v[188:191]\n\t"
      "v_exp_f32_e32 v116, v116\n\t"
      "v_mfma_f32_16x16x32_bf16 v[188:191], a[100:103], a[152:155], v[188:191]\n\t"
      "s_waitcnt lgkmcnt(4)\n\t"
      "v_exp_f32_e32 v117, v117\n\t"
      "v_mfma_f32_16x16x32_bf16 v[188:191], a[108:111], a[156:159], v[188:191]\n\t"
      "v_mul_f32_e32 v118, s86, v118\n\t"
      "v_mul_f32_e32 v119, s86, v119\n\t"
      "v_mfma_f32_16x16x32_bf16 v[184:187], a[104:107], a[156:159], v[184:187]\n\t"
      "v_exp_f32_e32 v118, v118\n\t"
      "v_mfma_f32_16x16x32_bf16 v[184:187], a[96:99], a[152:155], v[184:187]\n\t"
      "v_exp_f32_e32 v119, v119\n\t"
      "v_mfma_f32_16x16x32_bf16 v[184:187], a[88:91], a[148:151], v[184:187]\n\t"
      "v_fma_f32 v224, v224, -0.5, -0.5\n\t"
      "v_fma_f32 v225, v225, -0.5, -0.5\n\t"
      "v_mfma_f32_16x16x32_bf16 v[184:187], a[80:83], a[144:147], v[184:187]\n\t"
      "v_fma_f32 v226, v226, -0.5, -0.5\n\t"
      "v_fma_f32 v227, v227, -0.5, -0.5\n\t"
      "v_mfma_f32_16x16x32_bf16 v[184:187], a[72:75], a[172:175], v[184:187]\n\t"
      "v_fma_f32 v116, v224, v116, v224\n\t"
      "v_fma_f32 v117, v225, v117, v225\n\t"
      "v_mfma_f32_16x16x32_bf16 v[192:195], a[32:35], a[112:115], v[192:195]\n\t"
      "v_rcp_f32_e32 v116, v116\n\t"
      "v_mfma_f32_16x16x32_bf16 v[192:195], a[40:43], a[116:119], v[192:195]\n\t"
      "v_rcp_f32_e32 v117, v117\n\t"
      "v_mfma_f32_16x16x32_bf16 v[192:195], a[48:51], a[120:123], v[192:195]\n\t"
      "v_fma_f32 v118, v226, v118, v226\n\t"
      "v_fma_f32 v119, v227, v119, v227\n\t"
      "v_mfma_f32_16x16x32_bf16 v[192:195], a[56:59], a[124:127], v[192:195]\n\t"
      "s_waitcnt lgkmcnt(3)\n\t"
      "v_rcp_f32_e32 v118, v118\n\t"
      "v_mfma_f32_16x16x32_bf16 v[192:195], a[64:67], a[128:131], v[192:195]\n\t"
      "ds_read_b128 a[160:163], %[lx1] offset:7168\n\t"
      "v_bfi_b32 v112, s75, v116, v112\n\t"
      "v_mfma_f32_16x16x32_bf16 v[196:199], a[68:71], a[128:131], v[196:199]\n\t"
      "v_rcp_f32_e32 v119, v119\n\t"
      "v_mfma_f32_16x16x32_bf16 v[196:199], a[60:63], a[124:127], v[196:199]\n\t"
      "v_bfi_b32 v113, s75, v117, v113\n\t"
      "v_bfi_b32 v114, s75, v118, v114\n\t"
      "v_mfma_f32_16x16x32_bf16 v[196:199], a[52:55], a[120:123], v[196:199]\n\t"
      "s_add_u32 m0, %[ismx], 4096\n\t"
      "v_bfi_b32 v115, s75, v119, v115\n\t"
      "v_fmac_f32_e32 v112, v224, v112\n\t"
      "v_mfma_f32_16x16x32_bf16 v[196:199], a[44:47], a[116:119], v[196:199]\n\t"
      "s_add_u32 s67, s71, 32768\n\t"
      "v_fmac_f32_e32 v113, v225, v113\n\t"
      "v_fmac_f32_e32 v114, v226, v114\n\t"
      "v_mfma_f32_16x16x32_bf16 v[196:199], a[36:39], a[112:115], v[196:199]\n\t"
      "buffer_load_dwordx4 %[vx], s[48:51], s67 offen lds\n\t"
      "v_fmac_f32_e32 v115, v227, v115\n\t"
      "v_mfma_f32_16x16x32_bf16 v[204:207], a[36:39], a[116:119], v[204:207]\n\t"
      "ds_read_b128 a[112:115], %[lx0] offset:8192\n\t"
      "v_cvt_pk_bf16_f32 v116, v112, v113\n\t"
      "v_mfma_f32_16x16x32_bf16 v[204:207], a[44:47], a[120:123], v[204:207]\n\t"
      "s_mov_b32 s68, 28672\n\t"
      "v_cvt_pk_bf16_f32 v117, v114, v115\n\t"
      "v_mul_f32_e64 v228, s84, |v120|\n\t"
      "v_mfma_f32_16x16x32_bf16 v[204:207], a[52:55], a[124:127], v[204:207]\n\t"
      "buffer_store_dwordx2 v[116:117], %[vo], s[52:55], s68 offen offset:0\n\t"
      "v_mfma_f32_16x16x32_bf16 v[204:207], a[60:63], a[128:131], v[204:207]\n\t"
      "s_waitcnt lgkmcnt(4)\n\t"
      "v_exp_f32_e32 v228, v228\n\t"
      "v_mfma_f32_16x16x32_bf16 v[204:207], a[68:71], a[132:135], v[204:207]\n\t"
      "ds_read_b128 a[164:167], %[lx1] offset:7424\n\t"
      "v_mul_f32_e64 v229, s84, |v121|\n\t"
      "v_mfma_f32_16x16x32_bf16 v[200:203], a[64:67], a[132:135], v[200:203]\n\t"
      "v_exp_f32_e32 v229, v229\n\t"
      "v_mfma_f32_16x16x32_bf16 v[200:203], a[56:59], a[128:131], v[200:203]\n\t"
      "v_mul_f32_e64 v230, s84, |v122|\n\t"
      "v_mul_f32_e64 v231, s84, |v123|\n\t"
      "v_mfma_f32_16x16x32_bf16 v[200:203], a[48:51], a[124:127], v[200:203]\n\t"
      "v_exp_f32_e32 v230, v230\n\t"
      "v_mfma_f32_16x16x32_bf16 v[200:203], a[40:43], a[120:123], v[200:203]\n\t"
      "v_exp_f32_e32 v231, v231\n\t"
      "v_mfma_f32_16x16x32_bf16 v[200:203], a[32:35], a[116:119], v[200:203]\n\t"
      "v_mul_f32_e32 v124, s86, v124\n\t"
      "v_mul_f32_e32 v125, s86, v125\n\t"
      "v_mfma_f32_16x16x32_bf16 v[208:211], a[32:35], a[120:123], v[208:211]\n\t"
      "ds_read_b128 a[116:119], %[lx0] offset:8448\n\t"
      "v_mul_f32_e32 v126, s86, v126\n\t"
      "v_mfma_f32_16x16x32_bf16 v[208:211], a[40:43], a[124:127], v[208:211]\n\t"
      "v_exp_f32_e32 v124, v124\n\t"
      "v_mfma_f32_16x16x32_bf16 v[208:211], a[48:51], a[128:131], v[208:211]\n\t"
      "v_exp_f32_e32 v125, v125\n\t"
      "v_mfma_f32_16x16x32_bf16 v[208:211], a[56:59], a[132:135], v[208:211]\n\t"
      "s_waitcnt lgkmcnt(5)\n\t"
      "v_exp_f32_e32 v126, v126\n\t"
      "v_mfma_f32_16x16x32_bf16 v[208:211], a[64:67], a[136:139], v[208:211]\n\t"
      "s_add_u32 m0, %[ismx], 40960\n\t"
      "ds_read_b128 a[168:171], %[lx1] offset:7680\n\t"
      "v_mul_f32_e32 v127, s86, v127\n\t"
      "v_mfma_f32_16x16x32_bf16 v[212:215], a[68:71], a[136:139], v[212:215]\n\t"
      "s_add_u32 s67, s76, 32768\n\t"
      "v_exp_f32_e32 v127, v127\n\t"
      "v_mfma_f32_16x16x32_bf16 v[212:215], a[60:63], a[132:135], v[212:215]\n\t"
      "buffer_load_dwordx4 %[vx], s[48:51], s67 offen lds\n\t"
      "v_fma_f32 v228, v228, -0.5, -0.5\n\t"
      "v_mfma_f32_16x16x32_bf16 v[212:215], a[52:55], a[128:131], v[212:215]\n\t"
      "v_fma_f32 v229, v229, -0.5, -0.5\n\t"
      "v_fma_f32 v230, v230, -0.5, -0.5\n\t"
      "v_mfma_f32_16x16x32_bf16 v[212:215], a[44:47], a[124:127], v[212:215]\n\t"
      "v_fma_f32 v231, v231, -0.5, -0.5\n\t"
      "v_fma_f32 v124, v228, v124, v228\n\t"
      "v_mfma_f32_16x16x32_bf16 v[212:215], a[36:39], a[120:123], v[212:215]\n\t"
      "v_rcp_f32_e32 v124, v124\n\t"
      "v_mfma_f32_16x16x32_bf16 v[220:223], a[36:39], a[124:127], v[220:223]\n\t"
      "ds_read_b128 a[120:123], %[lx0] offset:8704\n\t"
      "v_fma_f32 v125, v229, v125, v229\n\t"
      "v_mfma_f32_16x16x32_bf16 v[220:223], a[44:47], a[128:131], v[220:223]\n\t"
      "v_rcp_f32_e32 v125, v125\n\t"
      "v_mfma_f32_16x16x32_bf16 v[220:223], a[52:55], a[132:135], v[220:223]\n\t"
      "v_fma_f32 v126, v230, v126, v230\n\t"
      "v_fma_f32 v127, v231, v127, v231\n\t"
      "v_mfma_f32_16x16x32_bf16 v[220:223], a[60:63], a[136:139], v[220:223]\n\t"
      "s_waitcnt lgkmcnt(6)\n\t"
      "v_rcp_f32_e32 v126, v126\n\t"
      "v_mfma_f32_16x16x32_bf16 v[220:223], a[68:71], a[140:143], v[220:223]\n\t"
      "ds_read_b128 a[172:175], %[lx1] offset:7936\n\t"
      "v_bfi_b32 v120, s75, v124, v120\n\t"
      "v_mfma_f32_16x16x32_bf16 v[216:219], a[64:67], a[140:143], v[216:219]\n\t"
      "v_rcp_f32_e32 v127, v127\n\t"
      "v_mfma_f32_16x16x32_bf16 v[216:219], a[56:59], a[136:139], v[216:219]\n\t"
      "v_bfi_b32 v121, s75, v125, v121\n\t"
      "v_bfi_b32 v122, s75, v126, v122\n\t"
      "v_mfma_f32_16x16x32_bf16 v[216:219], a[48:51], a[132:135], v[216:219]\n\t"
      "v_bfi_b32 v123, s75, v127, v123\n\t"
      "v_fmac_f32_e32 v120, v228, v120\n\t"
      "v_mfma_f32_16x16x32_bf16 v[216:219], a[40:43], a[128:131], v[216:219]\n\t"
      "v_fmac_f32_e32 v121, v229, v121\n\t"
      "v_fmac_f32_e32 v122, v230, v122\n\t"
      "v_mfma_f32_16x16x32_bf16 v[216:219], a[32:35], a[124:127], v[216:219]\n\t"
      "v_fmac_f32_e32 v123, v231, v123\n\t"
      "v_cvt_pk_bf16_f32 v124, v120, v121\n\t"
      "v_mfma_f32_16x16x32_bf16 a[0:3], a[32:35], a[128:131], a[0:3]\n\t"
      "ds_read_b128 a[124:127], %[lx0] offset:8960\n\t"
      "v_cvt_pk_bf16_f32 v125, v122, v123\n\t"
      "v_mfma_f32_16x16x32_bf16 a[0:3], a[40:43], a[132:135], a[0:3]\n\t"
      "s_mov_b32 s68, 28672\n\t"
      "v_mul_f32_e64 v224, s84, |v128|\n\t"
      "v_mul_f32_e64 v225, s84, |v129|\n\t"
      "v_mfma_f32_16x16x32_bf16 a[0:3], a[48:51], a[136:139], a[0:3]\n\t"
      "s_add_u32 m0, %[ismx], 5120\n\t"
      "buffer_store_dwordx2 v[124:125], %[vo], s[52:55], s68 offen offset:2048\n\t"
      "v_mfma_f32_16x16x32_bf16 a[0:3], a[56:59], a[140:143], a[0:3]\n\t"
      "s_waitcnt lgkmcnt(6)\n\t"
      "s_add_u32 s67, s71, 40960\n\t"
      "v_mul_f32_e64 v226, s84, |v130|\n\t"
      "v_mfma_f32_16x16x32_bf16 a[0:3], a[64:67], a[112:115], a[0:3]\n\t"
      "buffer_load_dwordx4 %[vx], s[48:51], s67 offen lds\n\t"
      "v_mul_f32_e64 v227, s84, |v131|\n\t"
      "v_mfma_f32_16x16x32_bf16 a[4:7], a[68:71], a[112:115], a[4:7]\n\t"
      "v_exp_f32_e32 v224, v224\n\t"
      "v_mfma_f32_16x16x32_bf16 a[4:7], a[60:63], a[140:143], a[4:7]\n\t"
      "v_exp_f32_e32 v225, v225\n\t"
      "v_mfma_f32_16x16x32_bf16 a[4:7], a[52:55], a[136:139], a[4:7]\n\t"
      "v_exp_f32_e32 v226, v226\n\t"
      "v_mfma_f32_16x16x32_bf16 a[4:7], a[44:47], a[132:135], a[4:7]\n\t"
      "v_exp_f32_e32 v227, v227\n\t"
      "v_mfma_f32_16x16x32_bf16 a[4:7], a[36:39], a[128:131], a[4:7]\n\t"
      "v_mul_f32_e32 v132, s86, v132\n\t"
      "v_mul_f32_e32 v133, s86, v133\n\t"
      "v_mfma_f32_16x16x32_bf16 a[12:15], a[36:39], a[132:135], a[12:15]\n\t"
      "v_exp_f32_e32 v132, v132\n\t"
      "v_mfma_f32_16x16x32_bf16 a[12:15], a[44:47], a[136:139], a[12:15]\n\t"
      "v_exp_f32_e32 v133, v133\n\t"
      "v_mfma_f32_16x16x32_bf16 a[12:15], a[52:55], a[140:143], a[12:15]\n\t"
      "v_mul_f32_e32 v134, s86, v134\n\t"
      "v_mul_f32_e32 v135, s86, v135\n\t"
      "v_mfma_f32_16x16x32_bf16 a[12:15], a[60:63], a[112:115], a[12:15]\n\t"
      "s_waitcnt lgkmcnt(4)\n\t"
      "v_exp_f32_e32 v134, v134\n\t"
      "v_mfma_f32_16x16x32_bf16 a[12:15], a[68:71], a[116:119], a[12:15]\n\t"
      "v_exp_f32_e32 v135, v135\n\t"
      "v_mfma_f32_16x16x32_bf16 a[8:11], a[64:67], a[116:119], a[8:11]\n\t"
      "v_fma_f32 v224, v224, -0.5, -0.5\n\t"
      "v_fma_f32 v225, v225, -0.5, -0.5\n\t"
      "v_mfma_f32_16x16x32_bf16 a[8:11], a[56:59], a[112:115], a[8:11]\n\t"
      "v_fma_f32 v226, v226, -0.5, -0.5\n\t"
      "v_fma_f32 v227, v227, -0.5, -0.5\n\t"
      "v_mfma_f32_16x16x32_bf16 a[8:11], a[48:51], a[140:143], a[8:11]\n\t"
      "v_fma_f32 v132, v224, v132, v224\n\t"
      "v_fma_f32 v133, v225, v133, v225\n\t"
      "v_mfma_f32_16x16x32_bf16 a[8:11], a[40:43], a[136:139], a[8:11]\n\t"
      "s_cmp_le_u32 s78, 30\n\t"
      "v_mfma_f32_16x16x32_bf16 a[8:11], a[32:35], a[132:135], a[8:11]\n\t"
      "s_cbranch_scc1 LSKS7480_%=\n\t"
      "v_mfma_f32_16x16x32_bf16 a[16:19], a[32:35], a[136:139], a[16:19]\n\t"
      "v_mfma_f32_16x16x32_bf16 a[16:19], a[40:43], a[140:143], a[16:19]\n\t"
      "v_mfma_f32_16x16x32_bf16 a[16:19], a[48:51], a[112:115], a[16:19]\n\t"
      "v_mfma_f32_16x16x32_bf16 a[16:19], a[56:59], a[116:119], a[16:19]\n\t"
      "s_waitcnt lgkmcnt(2)\n\t"
      "v_mfma_f32_16x16x32_bf16 a[16:19], a[64:67], a[120:123], a[16:19]\n\t"
      "v_mfma_f32_16x16x32_bf16 a[20:23], a[68:71], a[120:123], a[20:23]\n\t"
      "v_mfma_f32_16x16x32_bf16 a[20:23], a[60:63], a[116:119], a[20:23]\n\t"
      "v_mfma_f32_16x16x32_bf16 a[20:23], a[52:55], a[112:115], a[20:23]\n\t"
      "v_mfma_f32_16x16x32_bf16 a[20:23], a[44:47], a[140:143], a[20:23]\n\t"
      "s_cmp_le_u32 s78, 31\n\t"
      "v_mfma_f32_16x16x32_bf16 a[20:23], a[36:39], a[136:139], a[20:23]\n\t"
      "s_cbranch_scc1 LSKS7480_%=\n\t"
      "v_mfma_f32_16x16x32_bf16 a[28:31], a[36:39], a[140:143], a[28:31]\n\t"
      "v_mfma_f32_16x16x32_bf16 a[28:31], a[44:47], a[112:115], a[28:31]\n\t"
      "v_mfma_f32_16x16x32_bf16 a[28:31], a[52:55], a[116:119], a[28:31]\n\t"
      "v_mfma_f32_16x16x32_bf16 a[28:31], a[60:63], a[120:123], a[28:31]\n\t"
      "s_waitcnt lgkmcnt(0)\n\t"
      "v_mfma_f32_16x16x32_bf16 a[28:31], a[68:71], a[124:127], a[28:31]\n\t"
      "v_mfma_f32_16x16x32_bf16 a[24:27], a[64:67], a[124:127], a[24:27]\n\t"
      "v_mfma_f32_16x16x32_bf16 a[24:27], a[56:59], a[120:123], a[24:27]\n\t"
      "v_mfma_f32_16x16x32_bf16 a[24:27], a[48:51], a[116:119], a[24:27]\n\t"
      "v_mfma_f32_16x16x32_bf16 a[24:27], a[40:43], a[112:115], a[24:27]\n\t"
      "v_mfma_f32_16x16x32_bf16 a[24:27], a[32:35], a[140:143], a[24:27]\n\t"
      "LSKS7480_%=:\n\t"
      "v_mfma_f32_16x16x32_bf16 v[192:195], a[72:75], a[144:147], v[192:195]\n\t"
      "v_rcp_f32_e32 v132, v132\n\t"
      "v_mfma_f32_16x16x32_bf16 v[192:195], a[80:83], a[148:151], v[192:195]\n\t"
      "v_rcp_f32_e32 v133, v133\n\t"
      "v_mfma_f32_16x16x32_bf16 v[192:195], a[88:91], a[152:155], v[192:195]\n\t"
      "v_fma_f32 v134, v226, v134, v226\n\t"
      "v_fma_f32 v135, v227, v135, v227\n\t"
      "v_mfma_f32_16x16x32_bf16 v[192:195], a[96:99], a[156:159], v[192:195]\n\t"
      "v_rcp_f32_e32 v134, v134\n\t"
      "v_mfma_f32_16x16x32_bf16 v[192:195], a[104:107], a[160:163], v[192:195]\n\t"
      "s_add_u32 m0, %[ismx], 41984\n\t"
      "v_rcp_f32_e32 v135, v135\n\t"
      "v_mfma_f32_16x16x32_bf16 v[196:199], a[108:111], a[160:163], v[196:199]\n\t"
      "s_add_u32 s67, s76, 40960\n\t"
      "v_bfi_b32 v128, s75, v132, v128\n\t"
      "v_bfi_b32 v129, s75, v133, v129\n\t"
      "v_mfma_f32_16x16x32_bf16 v[196:199], a[100:103], a[156:159], v[196:199]\n\t"
      "buffer_load_dwordx4 %[vx], s[48:51], s67 offen lds\n\t"
      "v_bfi_b32 v130, s75, v134, v130\n\t"
      "v_mfma_f32_16x16x32_bf16 v[196:199], a[92:95], a[152:155], v[196:199]\n\t"
      "v_bfi_b32 v131, s75, v135, v131\n\t"
      "v_fmac_f32_e32 v128, v224, v128\n\t"
      "v_mfma_f32_16x16x32_bf16 v[196:199], a[84:87], a[148:151], v[196:199]\n\t"
      "v_fmac_f32_e32 v129, v225, v129\n\t"
      "v_fmac_f32_e32 v130, v226, v130\n\t"
      "v_mfma_f32_16x16x32_bf16 v[196:199], a[76:79], a[144:147], v[196:199]\n\t"
      "v_fmac_f32_e32 v131, v227, v131\n\t"
      "v_cvt_pk_bf16_f32 v132, v128, v129\n\t"
      "v_mfma_f32_16x16x32_bf16 v[204:207], a[76:79], a[148:151], v[204:207]\n\t"
      "ds_read_b128 a[144:147], %[lx1] offset:8192\n\t"
      "v_cvt_pk_bf16_f32 v133, v130, v131\n\t"
      "v_mfma_f32_16x16x32_bf16 v[204:207], a[84:87], a[152:155], v[204:207]\n\t"
      "s_mov_b32 s68, 32768\n\t"
      "v_mul_f32_e64 v228, s84, |v136|\n\t"
      "v_mul_f32_e64 v229, s84, |v137|\n\t"
      "v_mfma_f32_16x16x32_bf16 v[204:207], a[92:95], a[156:159], v[204:207]\n\t"
      "buffer_store_dwordx2 v[132:133], %[vo], s[52:55], s68 offen offset:0\n\t"
      "v_mfma_f32_16x16x32_bf16 v[204:207], a[100:103], a[160:163], v[204:207]\n\t"
      "v_exp_f32_e32 v228, v228\n\t"
      "v_mfma_f32_16x16x32_bf16 v[204:207], a[108:111], a[164:167], v[204:207]\n\t"
      "s_waitcnt vmcnt(10) lgkmcnt(2)\n\t"
      "s_barrier\n\t"
      "ds_read_b64_tr_b16 a[32:33], %[lw0] offset:0\n\t"
      "v_mfma_f32_16x16x32_bf16 v[200:203], a[104:107], a[164:167], v[200:203]\n\t"
      "ds_read_b64_tr_b16 a[34:35], %[lw0] offset:512\n\t"
      "v_mul_f32_e64 v230, s84, |v138|\n\t"
      "v_mfma_f32_16x16x32_bf16 v[200:203], a[96:99], a[160:163], v[200:203]\n\t"
      "ds_read_b128 a[112:115], %[lx0] offset:0\n\t"
      "v_mul_f32_e64 v231, s84, |v139|\n\t"
      "v_mfma_f32_16x16x32_bf16 v[200:203], a[88:91], a[156:159], v[200:203]\n\t"
      "ds_read_b64_tr_b16 a[40:41], %[lw0] offset:8192\n\t"
      "v_mul_f32_e32 v140, s86, v140\n\t"
      "v_mfma_f32_16x16x32_bf16 v[200:203], a[80:83], a[152:155], v[200:203]\n\t"
      "ds_read_b64_tr_b16 a[42:43], %[lw0] offset:8704\n\t"
      "v_mul_f32_e32 v141, s86, v141\n\t"
      "v_mfma_f32_16x16x32_bf16 v[200:203], a[72:75], a[148:151], v[200:203]\n\t"
      "ds_read_b128 a[116:119], %[lx0] offset:256\n\t"
      "v_mul_f32_e32 v142, s86, v142\n\t"
      "v_mfma_f32_16x16x32_bf16 v[208:211], a[72:75], a[152:155], v[208:211]\n\t"
      "ds_read_b128 a[148:151], %[lx1] offset:8448\n\t"
      "v_mul_f32_e32 v143, s86, v143\n\t"
      "v_mfma_f32_16x16x32_bf16 v[208:211], a[80:83], a[156:159], v[208:211]\n\t"
      "ds_read_b64_tr_b16 a[48:49], %[lw0] offset:16384\n\t"
      "v_fma_f32 v228, v228, -0.5, -0.5\n\t"
      "v_mfma_f32_16x16x32_bf16 v[208:211], a[88:91], a[160:163], v[208:211]\n\t"
      "ds_read_b64_tr_b16 a[50:51], %[lw0] offset:16896\n\t"
      "v_mul_f32_e64 v224, s84, |v144|\n\t"
      "v_mfma_f32_16x16x32_bf16 v[208:211], a[96:99], a[164:167], v[208:211]\n\t"
      "ds_read_b128 a[120:123], %[lx0] offset:512\n\t"
      "v_mul_f32_e64 v225, s84, |v145|\n\t"
      "v_mfma_f32_16x16x32_bf16 v[208:211], a[104:107], a[168:171], v[208:211]\n\t"
      "ds_read_b64_tr_b16 a[56:57], %[lw0] offset:24576\n\t"
      "v_mul_f32_e64 v226, s84, |v146|\n\t"
      "v_mfma_f32_16x16x32_bf16 v[212:215], a[108:111], a[168:171], v[212:215]\n\t"
      "ds_read_b64_tr_b16 a[58:59], %[lw0] offset:25088\n\t"
      "v_mul_f32_e64 v227, s84, |v147|\n\t"
      "v_mfma_f32_16x16x32_bf16 v[212:215], a[100:103], a[164:167], v[212:215]\n\t"
      "ds_read_b128 a[124:127], %[lx0] offset:768\n\t"
      "v_mul_f32_e32 v148, s86, v148\n\t"
      "v_mfma_f32_16x16x32_bf16 v[212:215], a[92:95], a[160:163], v[212:215]\n\t"
      "ds_read_b64_tr_b16 a[64:65], %[lw0] offset:32768\n\t"
      "v_mul_f32_e32 v149, s86, v149\n\t"
      "v_mfma_f32_16x16x32_bf16 v[212:215], a[84:87], a[156:159], v[212:215]\n\t"
      "ds_read_b64_tr_b16 a[66:67], %[lw0] offset:33280\n\t"
      "v_mul_f32_e32 v150, s86, v150\n\t"
      "v_mfma_f32_16x16x32_bf16 v[212:215], a[76:79], a[152:155], v[212:215]\n\t"
      "ds_read_b128 a[128:131], %[lx0] offset:1024\n\t"
      "v_mul_f32_e32 v151, s86, v151\n\t"
      "v_mfma_f32_16x16x32_bf16 v[220:223], a[76:79], a[156:159], v[220:223]\n\t"
      "ds_read_b128 a[152:155], %[lx1] offset:8704\n\t"
      "v_mfma_f32_16x16x32_bf16 v[220:223], a[84:87], a[160:163], v[220:223]\n\t"
      "ds_read_b64_tr_b16 a[68:69], %[lw0] offset:33792\n\t"
      "v_mfma_f32_16x16x32_bf16 v[220:223], a[92:95], a[164:167], v[220:223]\n\t"
      "ds_read_b64_tr_b16 a[70:71], %[lw0] offset:34304\n\t"
      "v_mfma_f32_16x16x32_bf16 v[220:223], a[100:103], a[168:171], v[220:223]\n\t"
      "s_waitcnt lgkmcnt(15)\n\t"
      "ds_read_b64_tr_b16 a[60:61], %[lw0] offset:25600\n\t"
      "v_mfma_f32_16x16x32_bf16 v[220:223], a[108:111], a[172:175], v[220:223]\n\t"
      "ds_read_b64_tr_b16 a[62:63], %[lw0] offset:26112\n\t"
      "v_mfma_f32_16x16x32_bf16 v[216:219], a[104:107], a[172:175], v[216:219]\n\t"
      "ds_read_b64_tr_b16 a[52:53], %[lw0] offset:17408\n\t"
      "v_mfma_f32_16x16x32_bf16 v[216:219], a[96:99], a[168:171], v[216:219]\n\t"
      "ds_read_b64_tr_b16 a[54:55], %[lw0] offset:17920\n\t"
      "v_mfma_f32_16x16x32_bf16 v[216:219], a[88:91], a[164:167], v[216:219]\n\t"
      "ds_read_b64_tr_b16 a[44:45], %[lw0] offset:9216\n\t"
      "v_mfma_f32_16x16x32_bf16 v[216:219], a[80:83], a[160:163], v[216:219]\n\t"
      "ds_read_b64_tr_b16 a[46:47], %[lw0] offset:9728\n\t"
      "v_mfma_f32_16x16x32_bf16 v[216:219], a[72:75], a[156:159], v[216:219]\n\t"
      "ds_read_b64_tr_b16 a[36:37], %[lw0] offset:1024\n\t"
      "v_mfma_f32_16x16x32_bf16 a[0:3], a[72:75], a[160:163], a[0:3]\n\t"
      "ds_read_b128 a[156:159], %[lx1] offset:8960\n\t"
      "v_mfma_f32_16x16x32_bf16 a[0:3], a[80:83], a[164:167], a[0:3]\n\t"
      "ds_read_b64_tr_b16 a[38:39], %[lw0] offset:1536\n\t"
      "v_mfma_f32_16x16x32_bf16 a[0:3], a[88:91], a[168:171], a[0:3]\n\t"
      "ds_read_b128 a[132:135], %[lx0] offset:1280\n\t"
      "v_mfma_f32_16x16x32_bf16 a[0:3], a[96:99], a[172:175], a[0:3]\n\t"
      "s_waitcnt lgkmcnt(15)\n\t"
      "ds_read_b128 a[136:139], %[lx0] offset:1536\n\t"
      "v_mfma_f32_16x16x32_bf16 a[0:3], a[104:107], a[144:147], a[0:3]\n\t"
      "ds_read_b128 a[140:143], %[lx0] offset:1792\n\t"
      "v_mfma_f32_16x16x32_bf16 a[4:7], a[108:111], a[144:147], a[4:7]\n\t"
      "v_exp_f32_e32 v229, v229\n\t"
      "v_mfma_f32_16x16x32_bf16 a[4:7], a[100:103], a[172:175], a[4:7]\n\t"
      "v_exp_f32_e32 v230, v230\n\t"
      "v_mfma_f32_16x16x32_bf16 a[4:7], a[92:95], a[168:171], a[4:7]\n\t"
      "v_exp_f32_e32 v231, v231\n\t"
      "v_mfma_f32_16x16x32_bf16 a[4:7], a[84:87], a[164:167], a[4:7]\n\t"
      "v_exp_f32_e32 v140, v140\n\t"
      "v_mfma_f32_16x16x32_bf16 a[4:7], a[76:79], a[160:163], a[4:7]\n\t"
      "v_exp_f32_e32 v141, v141\n\t"
      "v_mfma_f32_16x16x32_bf16 a[12:15], a[76:79], a[164:167], a[12:15]\n\t"
      "v_exp_f32_e32 v142, v142\n\t"
      "v_mfma_f32_16x16x32_bf16 a[12:15], a[84:87], a[168:171], a[12:15]\n\t"
      "v_exp_f32_e32 v143, v143\n\t"
      "v_mfma_f32_16x16x32_bf16 a[12:15], a[92:95], a[172:175], a[12:15]\n\t"
      "v_fma_f32 v229, v229, -0.5, -0.5\n\t"
      "v_fma_f32 v230, v230, -0.5, -0.5\n\t"
      "v_mfma_f32_16x16x32_bf16 a[12:15], a[100:103], a[144:147], a[12:15]\n\t"
      "s_waitcnt lgkmcnt(15)\n\t"
      "v_fma_f32 v231, v231, -0.5, -0.5\n\t"
      "v_fma_f32 v140, v228, v140, v228\n\t"
      "v_mfma_f32_16x16x32_bf16 a[12:15], a[108:111], a[148:151], a[12:15]\n\t"
      "v_rcp_f32_e32 v140, v140\n\t"
      "v_mfma_f32_16x16x32_bf16 a[8:11], a[104:107], a[148:151], a[8:11]\n\t"
      "v_fma_f32 v141, v229, v141, v229\n\t"
      "v_fma_f32 v142, v230, v142, v230\n\t"
      "v_mfma_f32_16x16x32_bf16 a[8:11], a[96:99], a[144:147], a[8:11]\n\t"
      "v_rcp_f32_e32 v141, v141\n\t"
      "v_mfma_f32_16x16x32_bf16 a[8:11], a[88:91], a[172:175], a[8:11]\n\t"
      "v_rcp_f32_e32 v142, v142\n\t"
      "v_mfma_f32_16x16x32_bf16 a[8:11], a[80:83], a[168:171], a[8:11]\n\t"
      "s_cmp_le_u32 s78, 30\n\t"
      "v_mfma_f32_16x16x32_bf16 a[8:11], a[72:75], a[164:167], a[8:11]\n\t"
      "s_cbranch_scc1 LSKS7560_%=\n\t"
      "v_mfma_f32_16x16x32_bf16 a[16:19], a[72:75], a[168:171], a[16:19]\n\t"
      "v_mfma_f32_16x16x32_bf16 a[16:19], a[80:83], a[172:175], a[16:19]\n\t"
      "v_mfma_f32_16x16x32_bf16 a[16:19], a[88:91], a[144:147], a[16:19]\n\t"
      "v_mfma_f32_16x16x32_bf16 a[16:19], a[96:99], a[148:151], a[16:19]\n\t"
      "s_waitcnt lgkmcnt(14)\n\t"
      "v_mfma_f32_16x16x32_bf16 a[16:19], a[104:107], a[152:155], a[16:19]\n\t"
      "v_mfma_f32_16x16x32_bf16 a[20:23], a[108:111], a[152:155], a[20:23]\n\t"
      "v_mfma_f32_16x16x32_bf16 a[20:23], a[100:103], a[148:151], a[20:23]\n\t"
      "v_mfma_f32_16x16x32_bf16 a[20:23], a[92:95], a[144:147], a[20:23]\n\t"
      "v_mfma_f32_16x16x32_bf16 a[20:23], a[84:87], a[172:175], a[20:23]\n\t"
      "s_cmp_le_u32 s78, 31\n\t"
      "v_mfma_f32_16x16x32_bf16 a[20:23], a[76:79], a[168:171], a[20:23]\n\t"
      "s_cbranch_scc1 LSKS7560_%=\n\t"
      "v_mfma_f32_16x16x32_bf16 a[28:31], a[76:79], a[172:175], a[28:31]\n\t"
      "v_mfma_f32_16x16x32_bf16 a[28:31], a[84:87], a[144:147], a[28:31]\n\t"
      "v_mfma_f32_16x16x32_bf16 a[28:31], a[92:95], a[148:151], a[28:31]\n\t"
      "v_mfma_f32_16x16x32_bf16 a[28:31], a[100:103], a[152:155], a[28:31]\n\t"
      "s_waitcnt lgkmcnt(4)\n\t"
      "v_mfma_f32_16x16x32_bf16 a[28:31], a[108:111], a[156:159], a[28:31]\n\t"
      "v_mfma_f32_16x16x32_bf16 a[24:27], a[104:107], a[156:159], a[24:27]\n\t"
      "v_mfma_f32_16x16x32_bf16 a[24:27], a[96:99], a[152:155], a[24:27]\n\t"
      "v_mfma_f32_16x16x32_bf16 a[24:27], a[88:91], a[148:151], a[24:27]\n\t"
      "v_mfma_f32_16x16x32_bf16 a[24:27], a[80:83], a[144:147], a[24:27]\n\t"
      "v_mfma_f32_16x16x32_bf16 a[24:27], a[72:75], a[172:175], a[24:27]\n\t"
      "LSKS7560_%=:\n\t"
      "s_mov_b32 s78, %[ijb]\n\t"
      "v_mfma_f32_16x16x32_bf16 v[0:3], a[32:35], a[112:115], 0\n\t"
      "ds_read_b64_tr_b16 a[72:73], %[lw1] offset:0\n\t"
      "v_fma_f32 v143, v231, v143, v231\n\t"
      "v_mfma_f32_16x16x32_bf16 v[0:3], a[40:43], a[116:119], v[0:3]\n\t"
      "s_waitcnt lgkmcnt(15)\n\t"
      "ds_read_b64_tr_b16 a[74:75], %[lw1] offset:512\n\t"
      "v_bfi_b32 v136, s75, v140, v136\n\t"
      "v_mfma_f32_16x16x32_bf16 v[0:3], a[48:51], a[120:123], v[0:3]\n\t"
      "s_waitcnt lgkmcnt(15)\n\t"
      "ds_read_b128 a[144:147], %[lx1] offset:0\n\t"
      "v_bfi_b32 v137, s75, v141, v137\n\t"
      "v_mfma_f32_16x16x32_bf16 v[0:3], a[56:59], a[124:127], v[0:3]\n\t"
      "s_waitcnt lgkmcnt(15)\n\t"
      "ds_read_b64_tr_b16 a[80:81], %[lw1] offset:8192\n\t"
      "v_bfi_b32 v138, s75, v142, v138\n\t"
      "v_mfma_f32_16x16x32_bf16 v[0:3], a[64:67], a[128:131], v[0:3]\n\t"
      "s_waitcnt lgkmcnt(15)\n\t"
      "ds_read_b64_tr_b16 a[82:83], %[lw1] offset:8704\n\t"
      "v_fmac_f32_e32 v136, v228, v136\n\t"
      "v_mfma_f32_16x16x32_bf16 v[4:7], a[68:71], a[128:131], 0\n\t"
      "s_waitcnt lgkmcnt(15)\n\t"
      "ds_read_b128 a[148:151], %[lx1] offset:256\n\t"
      "v_fmac_f32_e32 v137, v229, v137\n\t"
      "v_mfma_f32_16x16x32_bf16 v[4:7], a[60:63], a[124:127], v[4:7]\n\t"
      "s_waitcnt lgkmcnt(14)\n\t"
      "ds_read_b64_tr_b16 a[88:89], %[lw1] offset:16384\n\t"
      "v_fmac_f32_e32 v138, v230, v138\n\t"
      "v_mfma_f32_16x16x32_bf16 v[4:7], a[52:55], a[120:123], v[4:7]\n\t"
      "s_waitcnt lgkmcnt(13)\n\t"
      "ds_read_b64_tr_b16 a[90:91], %[lw1] offset:16896\n\t"
      "v_mfma_f32_16x16x32_bf16 v[4:7], a[44:47], a[116:119], v[4:7]\n\t"
      "s_waitcnt lgkmcnt(11)\n\t"
      "ds_read_b128 a[152:155], %[lx1] offset:512\n\t"
      "v_mfma_f32_16x16x32_bf16 v[4:7], a[36:39], a[112:115], v[4:7]\n\t"
      "ds_read_b64_tr_b16 a[96:97], %[lw1] offset:24576\n\t"
      "v_mfma_f32_16x16x32_bf16 v[12:15], a[36:39], a[116:119], 0\n\t"
      "ds_read_b64_tr_b16 a[98:99], %[lw1] offset:25088\n\t"
      "v_mfma_f32_16x16x32_bf16 v[12:15], a[44:47], a[120:123], v[12:15]\n\t"
      "ds_read_b128 a[156:159], %[lx1] offset:768\n\t"
      "v_mfma_f32_16x16x32_bf16 v[12:15], a[52:55], a[124:127], v[12:15]\n\t"
      "ds_read_b64_tr_b16 a[104:105], %[lw1] offset:32768\n\t"
      "v_mfma_f32_16x16x32_bf16 v[12:15], a[60:63], a[128:131], v[12:15]\n\t"
      "s_waitcnt lgkmcnt(15)\n\t"
      "ds_read_b64_tr_b16 a[106:107], %[lw1] offset:33280\n\t"
      "v_mfma_f32_16x16x32_bf16 v[12:15], a[68:71], a[132:135], v[12:15]\n\t"
      "ds_read_b128 a[160:163], %[lx1] offset:1024\n\t"
      "v_mfma_f32_16x16x32_bf16 v[8:11], a[64:67], a[132:135], 0\n\t"
      "ds_read_b64_tr_b16 a[108:109], %[lw1] offset:33792\n\t"
      "v_mfma_f32_16x16x32_bf16 v[8:11], a[56:59], a[128:131], v[8:11]\n\t"
      "ds_read_b64_tr_b16 a[110:111], %[lw1] offset:34304\n\t"
      "v_mfma_f32_16x16x32_bf16 v[8:11], a[48:51], a[124:127], v[8:11]\n\t"
      "ds_read_b64_tr_b16 a[100:101], %[lw1] offset:25600\n\t"
      "v_mfma_f32_16x16x32_bf16 v[8:11], a[40:43], a[120:123], v[8:11]\n\t"
      "ds_read_b64_tr_b16 a[102:103], %[lw1] offset:26112\n\t"
      "v_mfma_f32_16x16x32_bf16 v[8:11], a[32:35], a[116:119], v[8:11]\n\t"
      "ds_read_b64_tr_b16 a[92:93], %[lw1] offset:17408\n\t"
      "v_mfma_f32_16x16x32_bf16 v[16:19], a[32:35], a[120:123], 0\n\t"
      "ds_read_b64_tr_b16 a[94:95], %[lw1] offset:17920\n\t"
      "v_mfma_f32_16x16x32_bf16 v[16:19], a[40:43], a[124:127], v[16:19]\n\t"
      "ds_read_b64_tr_b16 a[84:85], %[lw1] offset:9216\n\t"
      "v_mfma_f32_16x16x32_bf16 v[16:19], a[48:51], a[128:131], v[16:19]\n\t"
      "ds_read_b64_tr_b16 a[86:87], %[lw1] offset:9728\n\t"
      "v_mfma_f32_16x16x32_bf16 v[16:19], a[56:59], a[132:135], v[16:19]\n\t"
      "s_waitcnt lgkmcnt(15)\n\t"
      "ds_read_b64_tr_b16 a[76:77], %[lw1] offset:1024\n\t"
      "v_mfma_f32_16x16x32_bf16 v[16:19], a[64:67], a[136:139], v[16:19]\n\t"
      "ds_read_b64_tr_b16 a[78:79], %[lw1] offset:1536\n\t"
      "v_mfma_f32_16x16x32_bf16 v[20:23], a[68:71], a[136:139], 0\n\t"
      "ds_read_b128 a[164:167], %[lx1] offset:1280\n\t"
      "v_mfma_f32_16x16x32_bf16 v[20:23], a[60:63], a[132:135], v[20:23]\n\t"
      "ds_read_b128 a[168:171], %[lx1] offset:1536\n\t"
      "v_mfma_f32_16x16x32_bf16 v[20:23], a[52:55], a[128:131], v[20:23]\n\t"
      "ds_read_b128 a[172:175], %[lx1] offset:1792\n\t"
      "v_mfma_f32_16x16x32_bf16 v[20:23], a[44:47], a[124:127], v[20:23]\n\t"
      "v_rcp_f32_e32 v143, v143\n\t"
      "v_mfma_f32_16x16x32_bf16 v[20:23], a[36:39], a[120:123], v[20:23]\n\t"
      "v_exp_f32_e32 v224, v224\n\t"
      "v_mfma_f32_16x16x32_bf16 v[28:31], a[36:39], a[124:127], 0\n\t"
      "v_exp_f32_e32 v225, v225\n\t"
      "v_mfma_f32_16x16x32_bf16 v[28:31], a[44:47], a[128:131], v[28:31]\n\t"
      "v_exp_f32_e32 v226, v226\n\t"
      "v_mfma_f32_16x16x32_bf16 v[28:31], a[52:55], a[132:135], v[28:31]\n\t"
      "s_add_u32 m0, %[ismx], 6144\n\t"
      "v_exp_f32_e32 v227, v227\n\t"
      "v_mfma_f32_16x16x32_bf16 v[28:31], a[60:63], a[136:139], v[28:31]\n\t"
      "s_waitcnt lgkmcnt(15)\n\t"
      "s_add_u32 s67, s71, 49152\n\t"
      "v_bfi_b32 v139, s75, v143, v139\n\t"
      "v_mfma_f32_16x16x32_bf16 v[28:31], a[68:71], a[140:143], v[28:31]\n\t"
      "buffer_load_dwordx4 %[vx], s[48:51], s67 offen lds\n\t"
      "v_fmac_f32_e32 v139, v231, v139\n\t"
      "v_mfma_f32_16x16x32_bf16 v[24:27], a[64:67], a[140:143], 0\n\t"
      "v_cvt_pk_bf16_f32 v140, v136, v137\n\t"
      "v_cvt_pk_bf16_f32 v141, v138, v139\n\t"
      "v_mfma_f32_16x16x32_bf16 v[24:27], a[56:59], a[136:139], v[24:27]\n\t"
      "s_mov_b32 s68, 32768\n\t"
      "v_exp_f32_e32 v148, v148\n\t"
      "v_mfma_f32_16x16x32_bf16 v[24:27], a[48:51], a[132:135], v[24:27]\n\t"
      "buffer_store_dwordx2 v[140:141], %[vo], s[52:55], s68 offen offset:2048\n\t"
      "v_mfma_f32_16x16x32_bf16 v[24:27], a[40:43], a[128:131], v[24:27]\n\t"
      "v_exp_f32_e32 v149, v149\n\t"
      "v_mfma_f32_16x16x32_bf16 v[24:27], a[32:35], a[124:127], v[24:27]\n\t"
      "v_exp_f32_e32 v150, v150\n\t"
      "v_mfma_f32_16x16x32_bf16 v[0:3], a[72:75], a[144:147], v[0:3]\n\t"
      "v_exp_f32_e32 v151, v151\n\t"
      "v_mfma_f32_16x16x32_bf16 v[0:3], a[80:83], a[148:151], v[0:3]\n\t"
      "v_fma_f32 v224, v224, -0.5, -0.5\n\t"
      "v_fma_f32 v225, v225, -0.5, -0.5\n\t"
      "v_mfma_f32_16x16x32_bf16 v[0:3], a[88:91], a[152:155], v[0:3]\n\t"
      "ds_read_b128 a[112:115], %[lx0] offset:2048\n\t"
      "v_fma_f32 v226, v226, -0.5, -0.5\n\t"
      "v_mfma_f32_16x16x32_bf16 v[0:3], a[96:99], a[156:159], v[0:3]\n\t"
      "s_waitcnt lgkmcnt(14)\n\t"
      "v_fma_f32 v227, v227, -0.5, -0.5\n\t"
      "v_fma_f32 v148, v224, v148, v224\n\t"
      "v_mfma_f32_16x16x32_bf16 v[0:3], a[104:107], a[160:163], v[0:3]\n\t"
      "s_waitcnt lgkmcnt(12)\n\t"
      "v_rcp_f32_e32 v148, v148\n\t"
      "v_mfma_f32_16x16x32_bf16 v[4:7], a[108:111], a[160:163], v[4:7]\n\t"
      "s_waitcnt lgkmcnt(10)\n\t"
      "v_fma_f32 v149, v225, v149, v225\n\t"
      "v_fma_f32 v150, v226, v150, v226\n\t"
      "v_mfma_f32_16x16x32_bf16 v[4:7], a[100:103], a[156:159], v[4:7]\n\t"
      "s_waitcnt lgkmcnt(8)\n\t"
      "v_rcp_f32_e32 v149, v149\n\t"
      "v_mfma_f32_16x16x32_bf16 v[4:7], a[92:95], a[152:155], v[4:7]\n\t"
      "s_waitcnt lgkmcnt(6)\n\t"
      "v_rcp_f32_e32 v150, v150\n\t"
      "v_mfma_f32_16x16x32_bf16 v[4:7], a[84:87], a[148:151], v[4:7]\n\t"
      "s_waitcnt lgkmcnt(4)\n\t"
      "ds_read_b128 a[116:119], %[lx0] offset:2304\n\t"
      "v_fma_f32 v151, v227, v151, v227\n\t"
      "v_mfma_f32_16x16x32_bf16 v[4:7], a[76:79], a[144:147], v[4:7]\n\t"
      "v_rcp_f32_e32 v151, v151\n\t"
      "v_mfma_f32_16x16x32_bf16 v[12:15], a[76:79], a[148:151], v[12:15]\n\t"
      "v_bfi_b32 v144, s75, v148, v144\n\t"
      "v_bfi_b32 v145, s75, v149, v145\n\t"
      "v_mfma_f32_16x16x32_bf16 v[12:15], a[84:87], a[152:155], v[12:15]\n\t"
      "v_bfi_b32 v146, s75, v150, v146\n\t"
      "v_bfi_b32 v147, s75, v151, v147\n\t"
      "v_mfma_f32_16x16x32_bf16 v[12:15], a[92:95], a[156:159], v[12:15]\n\t"
      "v_fmac_f32_e32 v144, v224, v144\n\t"
      "v_fmac_f32_e32 v145, v225, v145\n\t"
      "v_mfma_f32_16x16x32_bf16 v[12:15], a[100:103], a[160:163], v[12:15]\n\t"
      "s_waitcnt lgkmcnt(4)\n\t"
      "s_add_u32 m0, %[ismx], 43008\n\t"
      "v_fmac_f32_e32 v146, v226, v146\n\t"
      "v_mfma_f32_16x16x32_bf16 v[12:15], a[108:111], a[164:167], v[12:15]\n\t"
      "s_add_u32 s67, s76, 49152\n\t"
      "ds_read_b128 a[120:123], %[lx0] offset:2560\n\t"
      "v_fmac_f32_e32 v147, v227, v147\n\t"
      "v_mfma_f32_16x16x32_bf16 v[8:11], a[104:107], a[164:167], v[8:11]\n\t"
      "buffer_load_dwordx4 %[vx], s[48:51], s67 offen lds\n\t"
      "v_cvt_pk_bf16_f32 v148, v144, v145\n\t"
      "v_mfma_f32_16x16x32_bf16 v[8:11], a[96:99], a[160:163], v[8:11]\n\t"
      "s_mov_b32 s68, 36864\n\t"
      "v_cvt_pk_bf16_f32 v149, v146, v147\n\t"
      "v_mul_f32_e64 v228, s84, |v152|\n\t"
      "v_mfma_f32_16x16x32_bf16 v[8:11], a[88:91], a[156:159], v[8:11]\n\t"
      "buffer_store_dwordx2 v[148:149], %[vo], s[52:55], s68 offen offset:0\n\t"
      "v_mfma_f32_16x16x32_bf16 v[8:11], a[80:83], a[152:155], v[8:11]\n\t"
      "v_exp_f32_e32 v228, v228\n\t"
      "v_mfma_f32_16x16x32_bf16 v[8:11], a[72:75], a[148:151], v[8:11]\n\t"
      "v_mul_f32_e64 v229, s84, |v153|\n\t"
      "v_mul_f32_e64 v230, s84, |v154|\n\t"
      "v_mfma_f32_16x16x32_bf16 v[16:19], a[72:75], a[152:155], v[16:19]\n\t"
      "ds_read_b128 a[124:127], %[lx0] offset:2816\n\t"
      "v_mul_f32_e64 v231, s84, |v155|\n\t"
      "v_mfma_f32_16x16x32_bf16 v[16:19], a[80:83], a[156:159], v[16:19]\n\t"
      "v_exp_f32_e32 v229, v229\n\t"
      "v_mfma_f32_16x16x32_bf16 v[16:19], a[88:91], a[160:163], v[16:19]\n\t"
      "v_exp_f32_e32 v230, v230\n\t"
      "v_mfma_f32_16x16x32_bf16 v[16:19], a[96:99], a[164:167], v[16:19]\n\t"
      "s_waitcnt lgkmcnt(5)\n\t"
      "v_exp_f32_e32 v231, v231\n\t"
      "v_mfma_f32_16x16x32_bf16 v[16:19], a[104:107], a[168:171], v[16:19]\n\t"
      "v_mul_f32_e32 v156, s86, v156\n\t"
      "v_mul_f32_e32 v157, s86, v157\n\t"
      "v_mfma_f32_16x16x32_bf16 v[20:23], a[108:111], a[168:171], v[20:23]\n\t"
      "v_exp_f32_e32 v156, v156\n\t"
      "v_mfma_f32_16x16x32_bf16 v[20:23], a[100:103], a[164:167], v[20:23]\n\t"
      "v_exp_f32_e32 v157, v157\n\t"
      "v_mfma_f32_16x16x32_bf16 v[20:23], a[92:95], a[160:163], v[20:23]\n\t"
      "v_mul_f32_e32 v158, s86, v158\n\t"
      "v_mul_f32_e32 v159, s86, v159\n\t"
      "v_mfma_f32_16x16x32_bf16 v[20:23], a[84:87], a[156:159], v[20:23]\n\t"
      "v_exp_f32_e32 v158, v158\n\t"
      "v_mfma_f32_16x16x32_bf16 v[20:23], a[76:79], a[152:155], v[20:23]\n\t"
      "v_exp_f32_e32 v159, v159\n\t"
      "v_mfma_f32_16x16x32_bf16 v[28:31], a[76:79], a[156:159], v[28:31]\n\t"
      "v_fma_f32 v228, v228, -0.5, -0.5\n\t"
      "v_fma_f32 v229, v229, -0.5, -0.5\n\t"
      "v_mfma_f32_16x16x32_bf16 v[28:31], a[84:87], a[160:163], v[28:31]\n\t"
      "v_fma_f32 v230, v230, -0.5, -0.5\n\t"
      "v_fma_f32 v231, v231, -0.5, -0.5\n\t"
      "v_mfma_f32_16x16x32_bf16 v[28:31], a[92:95], a[164:167], v[28:31]\n\t"
      "v_fma_f32 v156, v228, v156, v228\n\t"
      "v_fma_f32 v157, v229, v157, v229\n\t"
      "v_mfma_f32_16x16x32_bf16 v[28:31], a[100:103], a[168:171], v[28:31]\n\t"
      "s_waitcnt lgkmcnt(4)\n\t"
      "v_rcp_f32_e32 v156, v156\n\t"
      "v_mfma_f32_16x16x32_bf16 v[28:31], a[108:111], a[172:175], v[28:31]\n\t"
      "s_add_u32 m0, %[ismx], 7168\n\t"
      "v_rcp_f32_e32 v157, v157\n\t"
      "v_mfma_f32_16x16x32_bf16 v[24:27], a[104:107], a[172:175], v[24:27]\n\t"
      "s_add_u32 s67, s71, 57344\n\t"
      "v_fma_f32 v158, v230, v158, v230\n\t"
      "v_fma_f32 v159, v231, v159, v231\n\t"
      "v_mfma_f32_16x16x32_bf16 v[24:27], a[96:99], a[168:171], v[24:27]\n\t"
      "buffer_load_dwordx4 %[vx], s[48:51], s67 offen lds\n\t"
      "v_bfi_b32 v152, s75, v156, v152\n\t"
      "v_mfma_f32_16x16x32_bf16 v[24:27], a[88:91], a[164:167], v[24:27]\n\t"
      "v_rcp_f32_e32 v158, v158\n\t"
      "v_mfma_f32_16x16x32_bf16 v[24:27], a[80:83], a[160:163], v[24:27]\n\t"
      "v_rcp_f32_e32 v159, v159\n\t"
      "v_mfma_f32_16x16x32_bf16 v[24:27], a[72:75], a[156:159], v[24:27]\n\t"
      "v_bfi_b32 v153, s75, v157, v153\n\t"
      "v_bfi_b32 v154, s75, v158, v154\n\t"
      "v_mfma_f32_16x16x32_bf16 v[32:35], a[32:35], a[128:131], 0\n\t"
      "v_bfi_b32 v155, s75, v159, v155\n\t"
      "v_fmac_f32_e32 v152, v228, v152\n\t"
      "v_mfma_f32_16x16x32_bf16 v[32:35], a[40:43], a[132:135], v[32:35]\n\t"
      "v_fmac_f32_e32 v153, v229, v153\n\t"
      "v_fmac_f32_e32 v154, v230, v154\n\t"
      "v_mfma_f32_16x16x32_bf16 v[32:35], a[48:51], a[136:139], v[32:35]\n\t"
      "ds_read_b128 a[144:147], %[lx1] offset:2048\n\t"
      "v_fmac_f32_e32 v155, v231, v155\n\t"
      "v_mfma_f32_16x16x32_bf16 v[32:35], a[56:59], a[140:143], v[32:35]\n\t"
      "s_waitcnt lgkmcnt(4)\n\t"
      "v_cvt_pk_bf16_f32 v156, v152, v153\n\t"
      "v_cvt_pk_bf16_f32 v157, v154, v155\n\t"
      "v_mfma_f32_16x16x32_bf16 v[32:35], a[64:67], a[112:115], v[32:35]\n\t"
      "s_mov_b32 s68, 36864\n\t"
      "v_mul_f32_e64 v224, s84, |v160|\n\t"
      "v_mul_f32_e64 v225, s84, |v161|\n\t"
      "v_mfma_f32_16x16x32_bf16 v[36:39], a[68:71], a[112:115], 0\n\t"
      "buffer_store_dwordx2 v[156:157], %[vo], s[52:55], s68 offen offset:2048\n\t"
      "v_mfma_f32_16x16x32_bf16 v[36:39], a[60:63], a[140:143], v[36:39]\n\t"
      "v_exp_f32_e32 v224, v224\n\t"
      "v_mfma_f32_16x16x32_bf16 v[36:39], a[52:55], a[136:139], v[36:39]\n\t"
      "v_exp_f32_e32 v225, v225\n\t"
      "v_mfma_f32_16x16x32_bf16 v[36:39], a[44:47], a[132:135], v[36:39]\n\t"
      "ds_read_b128 a[148:151], %[lx1] offset:2304\n\t"
      "v_mul_f32_e64 v226, s84, |v162|\n\t"
      "v_mfma_f32_16x16x32_bf16 v[36:39], a[36:39], a[128:131], v[36:39]\n\t"
      "v_exp_f32_e32 v226, v226\n\t"
      "v_mfma_f32_16x16x32_bf16 v[44:47], a[36:39], a[132:135], 0\n\t"
      "v_mul_f32_e64 v227, s84, |v163|\n\t"
      "v_mul_f32_e32 v164, s86, v164\n\t"
      "v_mfma_f32_16x16x32_bf16 v[44:47], a[44:47], a[136:139], v[44:47]\n\t"
      "v_exp_f32_e32 v227, v227\n\t"
      "v_mfma_f32_16x16x32_bf16 v[44:47], a[52:55], a[140:143], v[44:47]\n\t"
      "v_exp_f32_e32 v164, v164\n\t"
      "v_mfma_f32_16x16x32_bf16 v[44:47], a[60:63], a[112:115], v[44:47]\n\t"
      "s_waitcnt lgkmcnt(4)\n\t"
      "v_mul_f32_e32 v165, s86, v165\n\t"
      "v_mul_f32_e32 v166, s86, v166\n\t"
      "v_mfma_f32_16x16x32_bf16 v[44:47], a[68:71], a[116:119], v[44:47]\n\t"
      "ds_read_b128 a[152:155], %[lx1] offset:2560\n\t"
      "v_mul_f32_e32 v167, s86, v167\n\t"
      "v_mfma_f32_16x16x32_bf16 v[40:43], a[64:67], a[116:119], 0\n\t"
      "s_add_u32 m0, %[ismx], 44032\n\t"
      "v_exp_f32_e32 v165, v165\n\t"
      "v_mfma_f32_16x16x32_bf16 v[40:43], a[56:59], a[112:115], v[40:43]\n\t"
      "s_add_u32 s67, s76, 57344\n\t"
      "v_exp_f32_e32 v166, v166\n\t"
      "v_mfma_f32_16x16x32_bf16 v[40:43], a[48:51], a[140:143], v[40:43]\n\t"
      "buffer_load_dwordx4 %[vx], s[48:51], s67 offen lds\n\t"
      "v_fma_f32 v224, v224, -0.5, -0.5\n\t"
      "v_mfma_f32_16x16x32_bf16 v[40:43], a[40:43], a[136:139], v[40:43]\n\t"
      "v_exp_f32_e32 v167, v167\n\t"
      "v_mfma_f32_16x16x32_bf16 v[40:43], a[32:35], a[132:135], v[40:43]\n\t"
      "v_fma_f32 v225, v225, -0.5, -0.5\n\t"
      "v_fma_f32 v226, v226, -0.5, -0.5\n\t"
      "v_mfma_f32_16x16x32_bf16 v[48:51], a[32:35], a[136:139], 0\n\t"
      "ds_read_b128 a[156:159], %[lx1] offset:2816\n\t"
      "v_fma_f32 v227, v227, -0.5, -0.5\n\t"
      "v_mfma_f32_16x16x32_bf16 v[48:51], a[40:43], a[140:143], v[48:51]\n\t"
      "v_fma_f32 v164, v224, v164, v224\n\t"
      "v_fma_f32 v165, v225, v165, v225\n\t"
      "v_mfma_f32_16x16x32_bf16 v[48:51], a[48:51], a[112:115], v[48:51]\n\t"
      "v_rcp_f32_e32 v164, v164\n\t"
      "v_mfma_f32_16x16x32_bf16 v[48:51], a[56:59], a[116:119], v[48:51]\n\t"
      "s_waitcnt lgkmcnt(5)\n\t"
      "v_rcp_f32_e32 v165, v165\n\t"
      "v_mfma_f32_16x16x32_bf16 v[48:51], a[64:67], a[120:123], v[48:51]\n\t"
      "v_fma_f32 v166, v226, v166, v226\n\t"
      "v_fma_f32 v167, v227, v167, v227\n\t"
      "v_mfma_f32_16x16x32_bf16 v[52:55], a[68:71], a[120:123], 0\n\t"
      "v_rcp_f32_e32 v166, v166\n\t"
      "v_mfma_f32_16x16x32_bf16 v[52:55], a[60:63], a[116:119], v[52:55]\n\t"
      "v_rcp_f32_e32 v167, v167\n\t"
      "v_mfma_f32_16x16x32_bf16 v[52:55], a[52:55], a[112:115], v[52:55]\n\t"
      "v_bfi_b32 v160, s75, v164, v160\n\t"
      "v_bfi_b32 v161, s75, v165, v161\n\t"
      "v_mfma_f32_16x16x32_bf16 v[52:55], a[44:47], a[140:143], v[52:55]\n\t"
      "v_bfi_b32 v162, s75, v166, v162\n\t"
      "v_bfi_b32 v163, s75, v167, v163\n\t"
      "v_mfma_f32_16x16x32_bf16 v[52:55], a[36:39], a[136:139], v[52:55]\n\t"
      "v_fmac_f32_e32 v160, v224, v160\n\t"
      "v_fmac_f32_e32 v161, v225, v161\n\t"
      "v_mfma_f32_16x16x32_bf16 v[60:63], a[36:39], a[140:143], 0\n\t"
      "v_fmac_f32_e32 v162, v226, v162\n\t"
      "v_fmac_f32_e32 v163, v227, v163\n\t"
      "v_mfma_f32_16x16x32_bf16 v[60:63], a[44:47], a[112:115], v[60:63]\n\t"
      "v_cvt_pk_bf16_f32 v164, v160, v161\n\t"
      "v_cvt_pk_bf16_f32 v165, v162, v163\n\t"
      "v_mfma_f32_16x16x32_bf16 v[60:63], a[52:55], a[116:119], v[60:63]\n\t"
      "s_mov_b32 s68, 40960\n\t"
      "v_mul_f32_e64 v228, s84, |v168|\n\t"
      "v_mul_f32_e64 v229, s84, |v169|\n\t"
      "v_mfma_f32_16x16x32_bf16 v[60:63], a[60:63], a[120:123], v[60:63]\n\t"
      "s_waitcnt lgkmcnt(4)\n\t"
      "buffer_store_dwordx2 v[164:165], %[vo], s[52:55], s68 offen offset:0\n\t"
      "v_mfma_f32_16x16x32_bf16 v[60:63], a[68:71], a[124:127], v[60:63]\n\t"
      "v_exp_f32_e32 v228, v228\n\t"
      "v_mfma_f32_16x16x32_bf16 v[56:59], a[64:67], a[124:127], 0\n\t"
      "v_exp_f32_e32 v229, v229\n\t"
      "v_mfma_f32_16x16x32_bf16 v[56:59], a[56:59], a[120:123], v[56:59]\n\t"
      "v_mul_f32_e64 v230, s84, |v170|\n\t"
      "v_mul_f32_e64 v231, s84, |v171|\n\t"
      "v_mfma_f32_16x16x32_bf16 v[56:59], a[48:51], a[116:119], v[56:59]\n\t"
      "v_exp_f32_e32 v230, v230\n\t"
      "v_mfma_f32_16x16x32_bf16 v[56:59], a[40:43], a[112:115], v[56:59]\n\t"
      "v_exp_f32_e32 v231, v231\n\t"
      "v_mfma_f32_16x16x32_bf16 v[56:59], a[32:35], a[140:143], v[56:59]\n\t"
      "s_waitcnt vmcnt(9) lgkmcnt(0)\n\t"
      "s_barrier\n\t"
      "v_mul_f32_e32 v172, s86, v172\n\t"
      "v_mfma_f32_16x16x32_bf16 v[32:35], a[72:75], a[160:163], v[32:35]\n\t"
      "v_exp_f32_e32 v172, v172\n\t"
      "v_mfma_f32_16x16x32_bf16 v[32:35], a[80:83], a[164:167], v[32:35]\n\t"
      "s_add_u32 m0, %[ismx], 8192\n\t"
      "v_mul_f32_e32 v173, s86, v173\n\t"
      "v_mul_f32_e32 v174, s86, v174\n\t"
      "v_mfma_f32_16x16x32_bf16 v[32:35], a[88:91], a[168:171], v[32:35]\n\t"
      "s_add_u32 s67, s71, 65536\n\t"
      "ds_read_b128 a[128:131], %[lx0] offset:3072\n\t"
      "v_mul_f32_e32 v175, s86, v175\n\t"
      "v_mfma_f32_16x16x32_bf16 v[32:35], a[96:99], a[172:175], v[32:35]\n\t"
      "buffer_load_dwordx4 %[vx], s[48:51], s67 offen lds\n\t"
      "v_fma_f32 v228, v228, -0.5, -0.5\n\t"
      "v_mfma_f32_16x16x32_bf16 v[32:35], a[104:107], a[144:147], v[32:35]\n\t"
      "v_exp_f32_e32 v173, v173\n\t"
      "v_mfma_f32_16x16x32_bf16 v[36:39], a[108:111], a[144:147], v[36:39]\n\t"
      "v_exp_f32_e32 v174, v174\n\t"
      "v_mfma_f32_16x16x32_bf16 v[36:39], a[100:103], a[172:175], v[36:39]\n\t"
      "v_exp_f32_e32 v175, v175\n\t"
      "v_mfma_f32_16x16x32_bf16 v[36:39], a[92:95], a[168:171], v[36:39]\n\t"
      "v_fma_f32 v229, v229, -0.5, -0.5\n\t"
      "v_fma_f32 v230, v230, -0.5, -0.5\n\t"
      "v_mfma_f32_16x16x32_bf16 v[36:39], a[84:87], a[164:167], v[36:39]\n\t"
      "ds_read_b128 a[132:135], %[lx0] offset:3328\n\t"
      "v_fma_f32 v231, v231, -0.5, -0.5\n\t"
      "v_mfma_f32_16x16x32_bf16 v[36:39], a[76:79], a[160:163], v[36:39]\n\t"
      "v_fma_f32 v172, v228, v172, v228\n\t"
      "v_fma_f32 v173, v229, v173, v229\n\t"
      "v_mfma_f32_16x16x32_bf16 v[44:47], a[76:79], a[164:167], v[44:47]\n\t"
      "v_rcp_f32_e32 v172, v172\n\t"
      "v_mfma_f32_16x16x32_bf16 v[44:47], a[84:87], a[168:171], v[44:47]\n\t"
      "v_rcp_f32_e32 v173, v173\n\t"
      "v_mfma_f32_16x16x32_bf16 v[44:47], a[92:95], a[172:175], v[44:47]\n\t"
      "v_fma_f32 v174, v230, v174, v230\n\t"
      "v_fma_f32 v175, v231, v175, v231\n\t"
      "v_mfma_f32_16x16x32_bf16 v[44:47], a[100:103], a[144:147], v[44:47]\n\t"
      "s_add_u32 m0, %[ismx], 45056\n\t"
      "v_rcp_f32_e32 v174, v174\n\t"
      "v_mfma_f32_16x16x32_bf16 v[44:47], a[108:111], a[148:151], v[44:47]\n\t"
      "s_add_u32 s67, s76, 65536\n\t"
      "ds_read_b128 a[136:139], %[lx0] offset:3584\n\t"
      "v_bfi_b32 v168, s75, v172, v168\n\t"
      "v_mfma_f32_16x16x32_bf16 v[40:43], a[104:107], a[148:151], v[40:43]\n\t"
      "buffer_load_dwordx4 %[vx], s[48:51], s67 offen lds\n\t"
      "v_bfi_b32 v169, s75, v173, v169\n\t"
      "v_mfma_f32_16x16x32_bf16 v[40:43], a[96:99], a[144:147], v[40:43]\n\t"
      "v_rcp_f32_e32 v175, v175\n\t"
      "v_mfma_f32_16x16x32_bf16 v[40:43], a[88:91], a[172:175], v[40:43]\n\t"
      "s_add_u32 s71, s71, 128\n\t"
      "v_bfi_b32 v170, s75, v174, v170\n\t"
      "v_bfi_b32 v171, s75, v175, v171\n\t"
      "v_mfma_f32_16x16x32_bf16 v[40:43], a[80:83], a[168:171], v[40:43]\n\t"
      "s_add_u32 s72, s72, 131072\n\t"
      "v_fmac_f32_e32 v168, v228, v168\n\t"
      "v_fmac_f32_e32 v169, v229, v169\n\t"
      "v_mfma_f32_16x16x32_bf16 v[40:43], a[72:75], a[164:167], v[40:43]\n\t"
      "s_and_b32 s71, s71, 1023\n\t"
      "v_fmac_f32_e32 v170, v230, v170\n\t"
      "v_fmac_f32_e32 v171, v231, v171\n\t"
      "v_mfma_f32_16x16x32_bf16 v[48:51], a[72:75], a[168:171], v[48:51]\n\t"
      "s_and_b32 s72, s72, 0xfffff\n\t"
      "ds_read_b128 a[140:143], %[lx0] offset:3840\n\t"
      "v_cvt_pk_bf16_f32 v172, v168, v169\n\t"
      "v_mfma_f32_16x16x32_bf16 v[48:51], a[80:83], a[172:175], v[48:51]\n\t"
      "s_add_u32 s76, s71, s77\n\t"
      "v_cvt_pk_bf16_f32 v173, v170, v171\n\t"
      "v_mul_f32_e64 v224, s84, |v176|\n\t"
      "v_mfma_f32_16x16x32_bf16 v[48:51], a[88:91], a[144:147], v[48:51]\n\t"
      "s_mov_b32 s68, 40960\n\t"
      "v_exp_f32_e32 v224, v224\n\t"
      "v_mfma_f32_16x16x32_bf16 v[48:51], a[96:99], a[148:151], v[48:51]\n\t"
      "buffer_store_dwordx2 v[172:173], %[vo], s[52:55], s68 offen offset:2048\n\t"
      "v_mfma_f32_16x16x32_bf16 v[48:51], a[104:107], a[152:155], v[48:51]\n\t"
      "v_mul_f32_e64 v225, s84, |v177|\n\t"
      "v_mul_f32_e64 v226, s84, |v178|\n\t"
      "v_mfma_f32_16x16x32_bf16 v[52:55], a[108:111], a[152:155], v[52:55]\n\t"
      "v_exp_f32_e32 v225, v225\n\t"
      "v_mfma_f32_16x16x32_bf16 v[52:55], a[100:103], a[148:151], v[52:55]\n\t"
      "v_exp_f32_e32 v226, v226\n\t"
      "v_mfma_f32_16x16x32_bf16 v[52:55], a[92:95], a[144:147], v[52:55]\n\t"
      "s_add_u32 m0, %[ismx], 0\n\t"
      "v_mul_f32_e64 v227, s84, |v179|\n\t"
      "v_mul_f32_e32 v180, s86, v180\n\t"
      "v_mfma_f32_16x16x32_bf16 v[52:55], a[84:87], a[172:175], v[52:55]\n\t"
      "s_add_u32 s67, s71, s74\n\t"
      "v_exp_f32_e32 v227, v227\n\t"
      "v_mfma_f32_16x16x32_bf16 v[52:55], a[76:79], a[168:171], v[52:55]\n\t"
      "buffer_load_dwordx4 %[vx], s[48:51], s67 offen lds\n\t"
      "v_mul_f32_e32 v181, s86, v181\n\t"
      "v_mfma_f32_16x16x32_bf16 v[60:63], a[76:79], a[172:175], v[60:63]\n\t"
      "v_exp_f32_e32 v180, v180\n\t"
      "v_mfma_f32_16x16x32_bf16 v[60:63], a[84:87], a[144:147], v[60:63]\n\t"
      "v_exp_f32_e32 v181, v181\n\t"
      "v_mfma_f32_16x16x32_bf16 v[60:63], a[92:95], a[148:151], v[60:63]\n\t"
      "v_mul_f32_e32 v182, s86, v182\n\t"
      "v_mul_f32_e32 v183, s86, v183\n\t"
      "v_mfma_f32_16x16x32_bf16 v[60:63], a[100:103], a[152:155], v[60:63]\n\t"
      "v_exp_f32_e32 v182, v182\n\t"
      "v_mfma_f32_16x16x32_bf16 v[60:63], a[108:111], a[156:159], v[60:63]\n\t"
      "v_exp_f32_e32 v183, v183\n\t"
      "v_mfma_f32_16x16x32_bf16 v[56:59], a[104:107], a[156:159], v[56:59]\n\t"
      "v_fma_f32 v224, v224, -0.5, -0.5\n\t"
      "v_fma_f32 v225, v225, -0.5, -0.5\n\t"
      "v_mfma_f32_16x16x32_bf16 v[56:59], a[96:99], a[152:155], v[56:59]\n\t"
      "v_fma_f32 v226, v226, -0.5, -0.5\n\t"
      "v_fma_f32 v227, v227, -0.5, -0.5\n\t"
      "v_mfma_f32_16x16x32_bf16 v[56:59], a[88:91], a[148:151], v[56:59]\n\t"
      "v_fma_f32 v180, v224, v180, v224\n\t"
      "v_fma_f32 v181, v225, v181, v225\n\t"
      "v_mfma_f32_16x16x32_bf16 v[56:59], a[80:83], a[144:147], v[56:59]\n\t"
      "s_add_u32 m0, %[ismx], 36864\n\t"
      "v_rcp_f32_e32 v180, v180\n\t"
      "v_mfma_f32_16x16x32_bf16 v[56:59], a[72:75], a[172:175], v[56:59]\n\t"
      "s_add_u32 s67, s76, 0\n\t"
      "v_rcp_f32_e32 v181, v181\n\t"
      "v_mfma_f32_16x16x32_bf16 v[64:67], a[32:35], a[112:115], 0\n\t"
      "buffer_load_dwordx4 %[vx], s[48:51], s67 offen lds\n\t"
      "v_fma_f32 v182, v226, v182, v226\n\t"
      "v_mfma_f32_16x16x32_bf16 v[64:67], a[40:43], a[116:119], v[64:67]\n\t"
      "v_rcp_f32_e32 v182, v182\n\t"
      "v_mfma_f32_16x16x32_bf16 v[64:67], a[48:51], a[120:123], v[64:67]\n\t"
      "ds_read_b128 a[160:163], %[lx1] offset:3072\n\t"
      "v_fma_f32 v183, v227, v183, v227\n\t"
      "v_mfma_f32_16x16x32_bf16 v[64:67], a[56:59], a[124:127], v[64:67]\n\t"
      "s_waitcnt lgkmcnt(4)\n\t"
      "v_rcp_f32_e32 v183, v183\n\t"
      "v_mfma_f32_16x16x32_bf16 v[64:67], a[64:67], a[128:131], v[64:67]\n\t"
      "v_bfi_b32 v176, s75, v180, v176\n\t"
      "v_bfi_b32 v177, s75, v181, v177\n\t"
      "v_mfma_f32_16x16x32_bf16 v[68:71], a[68:71], a[128:131], 0\n\t"
      "v_bfi_b32 v178, s75, v182, v178\n\t"
      "v_bfi_b32 v179, s75, v183, v179\n\t"
      "v_mfma_f32_16x16x32_bf16 v[68:71], a[60:63], a[124:127], v[68:71]\n\t"
      "v_fmac_f32_e32 v176, v224, v176\n\t"
      "v_fmac_f32_e32 v177, v225, v177\n\t"
      "v_mfma_f32_16x16x32_bf16 v[68:71], a[52:55], a[120:123], v[68:71]\n\t"
      "v_fmac_f32_e32 v178, v226, v178\n\t"
      "v_fmac_f32_e32 v179, v227, v179\n\t"
      "v_mfma_f32_16x16x32_bf16 v[68:71], a[44:47], a[116:119], v[68:71]\n\t"
      "ds_read_b128 a[164:167], %[lx1] offset:3328\n\t"
      "v_cvt_pk_bf16_f32 v180, v176, v177\n\t"
      "v_mfma_f32_16x16x32_bf16 v[68:71], a[36:39], a[112:115], v[68:71]\n\t"
      "s_mov_b32 s68, 45056\n\t"
      "v_cvt_pk_bf16_f32 v181, v178, v179\n\t"
      "v_mul_f32_e64 v228, s84, |v184|\n\t"
      "v_mfma_f32_16x16x32_bf16 v[76:79], a[36:39], a[116:119], 0\n\t"
      "s_add_u32 m0, %[ismx], 1024\n\t"
      "buffer_store_dwordx2 v[180:181], %[vo], s[52:55], s68 offen offset:0\n\t"
      "v_mfma_f32_16x16x32_bf16 v[76:79], a[44:47], a[120:123], v[76:79]\n\t"
      "s_add_u32 s67, s71, 8192\n\t"
      "v_exp_f32_e32 v228, v228\n\t"
      "v_mfma_f32_16x16x32_bf16 v[76:79], a[52:55], a[124:127], v[76:79]\n\t"
      "buffer_load_dwordx4 %[vx], s[48:51], s67 offen lds\n\t"
      "v_mul_f32_e64 v229, s84, |v185|\n\t"
      "v_mfma_f32_16x16x32_bf16 v[76:79], a[60:63], a[128:131], v[76:79]\n\t"
      "s_waitcnt lgkmcnt(4)\n\t"
      "v_exp_f32_e32 v229, v229\n\t"
      "v_mfma_f32_16x16x32_bf16 v[76:79], a[68:71], a[132:135], v[76:79]\n\t"
      "ds_read_b128 a[168:171], %[lx1] offset:3584\n\t"
      "v_mul_f32_e64 v230, s84, |v186|\n\t"
      "v_mfma_f32_16x16x32_bf16 v[72:75], a[64:67], a[132:135], 0\n\t"
      "v_exp_f32_e32 v230, v230\n\t"
      "v_mfma_f32_16x16x32_bf16 v[72:75], a[56:59], a[128:131], v[72:75]\n\t"
      "v_mul_f32_e64 v231, s84, |v187|\n\t"
      "v_mul_f32_e32 v188, s86, v188\n\t"
      "v_mfma_f32_16x16x32_bf16 v[72:75], a[48:51], a[124:127], v[72:75]\n\t"
      "v_exp_f32_e32 v231, v231\n\t"
      "v_mfma_f32_16x16x32_bf16 v[72:75], a[40:43], a[120:123], v[72:75]\n\t"
      "v_exp_f32_e32 v188, v188\n\t"
      "v_mfma_f32_16x16x32_bf16 v[72:75], a[32:35], a[116:119], v[72:75]\n\t"
      "v_mul_f32_e32 v189, s86, v189\n\t"
      "v_mul_f32_e32 v190, s86, v190\n\t"
      "v_mfma_f32_16x16x32_bf16 v[80:83], a[32:35], a[120:123], 0\n\t"
      "ds_read_b128 a[172:175], %[lx1] offset:3840\n\t"
      "v_mul_f32_e32 v191, s86, v191\n\t"
      "v_mfma_f32_16x16x32_bf16 v[80:83], a[40:43], a[124:127], v[80:83]\n\t"
      "s_add_u32 m0, %[ismx], 37888\n\t"
      "v_exp_f32_e32 v189, v189\n\t"
      "v_mfma_f32_16x16x32_bf16 v[80:83], a[48:51], a[128:131], v[80:83]\n\t"
      "s_add_u32 s67, s76, 8192\n\t"
      "v_exp_f32_e32 v190, v190\n\t"
      "v_mfma_f32_16x16x32_bf16 v[80:83], a[56:59], a[132:135], v[80:83]\n\t"
      "s_waitcnt lgkmcnt(5)\n\t"
      "buffer_load_dwordx4 %[vx], s[48:51], s67 offen lds\n\t"
      "v_fma_f32 v228, v228, -0.5, -0.5\n\t"
      "v_mfma_f32_16x16x32_bf16 v[80:83], a[64:67], a[136:139], v[80:83]\n\t"
      "v_exp_f32_e32 v191, v191\n\t"
      "v_mfma_f32_16x16x32_bf16 v[84:87], a[68:71], a[136:139], 0\n\t"
      "v_fma_f32 v229, v229, -0.5, -0.5\n\t"
      "v_fma_f32 v230, v230, -0.5, -0.5\n\t"
      "v_mfma_f32_16x16x32_bf16 v[84:87], a[60:63], a[132:135], v[84:87]\n\t"
      "v_fma_f32 v231, v231, -0.5, -0.5\n\t"
      "v_fma_f32 v188, v228, v188, v228\n\t"
      "v_mfma_f32_16x16x32_bf16 v[84:87], a[52:55], a[128:131], v[84:87]\n\t"
      "v_rcp_f32_e32 v188, v188\n\t"
      "v_mfma_f32_16x16x32_bf16 v[84:87], a[44:47], a[124:127], v[84:87]\n\t"
      "v_fma_f32 v189, v229, v189, v229\n\t"
      "v_fma_f32 v190, v230, v190, v230\n\t"
      "v_mfma_f32_16x16x32_bf16 v[84:87], a[36:39], a[120:123], v[84:87]\n\t"
      "v_rcp_f32_e32 v189, v189\n\t"
      "v_mfma_f32_16x16x32_bf16 v[92:95], a[36:39], a[124:127], 0\n\t"
      "v_rcp_f32_e32 v190, v190\n\t"
      "v_mfma_f32_16x16x32_bf16 v[92:95], a[44:47], a[128:131], v[92:95]\n\t"
      "v_fma_f32 v191, v231, v191, v231\n\t"
      "v_bfi_b32 v184, s75, v188, v184\n\t"
      "v_mfma_f32_16x16x32_bf16 v[92:95], a[52:55], a[132:135], v[92:95]\n\t"
      "v_rcp_f32_e32 v191, v191\n\t"
      "v_mfma_f32_16x16x32_bf16 v[92:95], a[60:63], a[136:139], v[92:95]\n\t"
      "s_waitcnt lgkmcnt(4)\n\t"
      "s_add_u32 m0, %[ismx], 2048\n\t"
      "v_bfi_b32 v185, s75, v189, v185\n\t"
      "v_mfma_f32_16x16x32_bf16 v[92:95], a[68:71], a[140:143], v[92:95]\n\t"
      "s_add_u32 s67, s71, 16384\n\t"
      "v_bfi_b32 v186, s75, v190, v186\n\t"
      "v_bfi_b32 v187, s75, v191, v187\n\t"
      "v_mfma_f32_16x16x32_bf16 v[88:91], a[64:67], a[140:143], 0\n\t"
      "buffer_load_dwordx4 %[vx], s[48:51], s67 offen lds\n\t"
      "v_fmac_f32_e32 v184, v228, v184\n\t"
      "v_mfma_f32_16x16x32_bf16 v[88:91], a[56:59], a[136:139], v[88:91]\n\t"
      "v_fmac_f32_e32 v185, v229, v185\n\t"
      "v_fmac_f32_e32 v186, v230, v186\n\t"
      "v_mfma_f32_16x16x32_bf16 v[88:91], a[48:51], a[132:135], v[88:91]\n\t"
      "v_fmac_f32_e32 v187, v231, v187\n\t"
      "v_cvt_pk_bf16_f32 v188, v184, v185\n\t"
      "v_mfma_f32_16x16x32_bf16 v[88:91], a[40:43], a[128:131], v[88:91]\n\t"
      "s_mov_b32 s68, 45056\n\t"
      "v_cvt_pk_bf16_f32 v189, v186, v187\n\t"
      "v_mul_f32_e64 v224, s84, |v192|\n\t"
      "v_mfma_f32_16x16x32_bf16 v[88:91], a[32:35], a[124:127], v[88:91]\n\t"
      "buffer_store_dwordx2 v[188:189], %[vo], s[52:55], s68 offen offset:2048\n\t"
      "v_mfma_f32_16x16x32_bf16 v[64:67], a[72:75], a[144:147], v[64:67]\n\t"
      "v_exp_f32_e32 v224, v224\n\t"
      "v_mfma_f32_16x16x32_bf16 v[64:67], a[80:83], a[148:151], v[64:67]\n\t"
      "v_mul_f32_e64 v225, s84, |v193|\n\t"
      "v_mul_f32_e64 v226, s84, |v194|\n\t"
      "v_mfma_f32_16x16x32_bf16 v[64:67], a[88:91], a[152:155], v[64:67]\n\t"
      "ds_read_b128 a[112:115], %[lx0] offset:4096\n\t"
      "v_mul_f32_e64 v227, s84, |v195|\n\t"
      "v_mfma_f32_16x16x32_bf16 v[64:67], a[96:99], a[156:159], v[64:67]\n\t"
      "s_waitcnt lgkmcnt(4)\n\t"
      "v_exp_f32_e32 v225, v225\n\t"
      "v_mfma_f32_16x16x32_bf16 v[64:67], a[104:107], a[160:163], v[64:67]\n\t"
      "v_exp_f32_e32 v226, v226\n\t"
      "v_mfma_f32_16x16x32_bf16 v[68:71], a[108:111], a[160:163], v[68:71]\n\t"
      "s_add_u32 m0, %[ismx], 38912\n\t"
      "v_exp_f32_e32 v227, v227\n\t"
      "v_mfma_f32_16x16x32_bf16 v[68:71], a[100:103], a[156:159], v[68:71]\n\t"
      "s_add_u32 s67, s76, 16384\n\t"
      "v_mul_f32_e32 v196, s86, v196\n\t"
      "v_mul_f32_e32 v197, s86, v197\n\t"
      "v_mfma_f32_16x16x32_bf16 v[68:71], a[92:95], a[152:155], v[68:71]\n\t"
      "buffer_load_dwordx4 %[vx], s[48:51], s67 offen lds\n\t"
      "v_mul_f32_e32 v198, s86, v198\n\t"
      "v_mfma_f32_16x16x32_bf16 v[68:71], a[84:87], a[148:151], v[68:71]\n\t"
      "ds_read_b128 a[116:119], %[lx0] offset:4352\n\t"
      "v_mul_f32_e32 v199, s86, v199\n\t"
      "v_mfma_f32_16x16x32_bf16 v[68:71], a[76:79], a[144:147], v[68:71]\n\t"
      "v_exp_f32_e32 v196, v196\n\t"
      "v_mfma_f32_16x16x32_bf16 v[76:79], a[76:79], a[148:151], v[76:79]\n\t"
      "v_exp_f32_e32 v197, v197\n\t"
      "v_mfma_f32_16x16x32_bf16 v[76:79], a[84:87], a[152:155], v[76:79]\n\t"
      "v_exp_f32_e32 v198, v198\n\t"
      "v_mfma_f32_16x16x32_bf16 v[76:79], a[92:95], a[156:159], v[76:79]\n\t"
      "v_exp_f32_e32 v199, v199\n\t"
      "v_mfma_f32_16x16x32_bf16 v[76:79], a[100:103], a[160:163], v[76:79]\n\t"
      "s_waitcnt lgkmcnt(4)\n\t"
      "v_fma_f32 v224, v224, -0.5, -0.5\n\t"
      "v_fma_f32 v225, v225, -0.5, -0.5\n\t"
      "v_mfma_f32_16x16x32_bf16 v[76:79], a[108:111], a[164:167], v[76:79]\n\t"
      "ds_read_b128 a[120:123], %[lx0] offset:4608\n\t"
      "v_fma_f32 v226, v226, -0.5, -0.5\n\t"
      "v_mfma_f32_16x16x32_bf16 v[72:75], a[104:107], a[164:167], v[72:75]\n\t"
      "v_fma_f32 v227, v227, -0.5, -0.5\n\t"
      "v_fma_f32 v196, v224, v196, v224\n\t"
      "v_mfma_f32_16x16x32_bf16 v[72:75], a[96:99], a[160:163], v[72:75]\n\t"
      "s_add_u32 m0, %[ismw], 0\n\t"
      "v_rcp_f32_e32 v196, v196\n\t"
      "v_mfma_f32_16x16x32_bf16 v[72:75], a[88:91], a[156:159], v[72:75]\n\t"
      "s_add_u32 s67, s72, 0\n\t"
      "v_fma_f32 v197, v225, v197, v225\n\t"
      "v_fma_f32 v198, v226, v198, v226\n\t"
      "v_mfma_f32_16x16x32_bf16 v[72:75], a[80:83], a[152:155], v[72:75]\n\t"
      "buffer_load_dwordx4 %[vw], s[56:59], s67 offen lds\n\t"
      "v_fma_f32 v199, v227, v199, v227\n\t"
      "v_mfma_f32_16x16x32_bf16 v[72:75], a[72:75], a[148:151], v[72:75]\n\t"
      "v_rcp_f32_e32 v197, v197\n\t"
      "v_mfma_f32_16x16x32_bf16 v[80:83], a[72:75], a[152:155], v[80:83]\n\t"
      "ds_read_b128 a[124:127], %[lx0] offset:4864\n\t"
      "v_bfi_b32 v192, s75, v196, v192\n\t"
      "v_mfma_f32_16x16x32_bf16 v[80:83], a[80:83], a[156:159], v[80:83]\n\t"
      "v_rcp_f32_e32 v198, v198\n\t"
      "v_mfma_f32_16x16x32_bf16 v[80:83], a[88:91], a[160:163], v[80:83]\n\t"
      "v_rcp_f32_e32 v199, v199\n\t"
      "v_mfma_f32_16x16x32_bf16 v[80:83], a[96:99], a[164:167], v[80:83]\n\t"
      "s_waitcnt lgkmcnt(5)\n\t"
      "v_bfi_b32 v193, s75, v197, v193\n\t"
      "v_bfi_b32 v194, s75, v198, v194\n\t"
      "v_mfma_f32_16x16x32_bf16 v[80:83], a[104:107], a[168:171], v[80:83]\n\t"
      "v_bfi_b32 v195, s75, v199, v195\n\t"
      "v_fmac_f32_e32 v192, v224, v192\n\t"
      "v_mfma_f32_16x16x32_bf16 v[84:87], a[108:111], a[168:171], v[84:87]\n\t"
      "v_fmac_f32_e32 v193, v225, v193\n\t"
      "v_fmac_f32_e32 v194, v226, v194\n\t"
      "v_mfma_f32_16x16x32_bf16 v[84:87], a[100:103], a[164:167], v[84:87]\n\t"
      "v_fmac_f32_e32 v195, v227, v195\n\t"
      "v_cvt_pk_bf16_f32 v196, v192, v193\n\t"
      "v_mfma_f32_16x16x32_bf16 v[84:87], a[92:95], a[160:163], v[84:87]\n\t"
      "s_mov_b32 s68, 49152\n\t"
      "v_cvt_pk_bf16_f32 v197, v194, v195\n\t"
      "v_mul_f32_e64 v228, s84, |v200|\n\t"
      "v_mfma_f32_16x16x32_bf16 v[84:87], a[84:87], a[156:159], v[84:87]\n\t"
      "buffer_store_dwordx2 v[196:197], %[vo], s[52:55], s68 offen offset:0\n\t"
      "v_mfma_f32_16x16x32_bf16 v[84:87], a[76:79], a[152:155], v[84:87]\n\t"
      "v_exp_f32_e32 v228, v228\n\t"
      "v_mfma_f32_16x16x32_bf16 v[92:95], a[76:79], a[156:159], v[92:95]\n\t"
      "buffer_load_dwordx4 %[vw], s[56:59], s67 offen offset:1024 lds\n\t"
      "v_mul_f32_e64 v229, s84, |v201|\n\t"
      "v_mfma_f32_16x16x32_bf16 v[92:95], a[84:87], a[160:163], v[92:95]\n\t"
      "v_exp_f32_e32 v229, v229\n\t"
      "v_mfma_f32_16x16x32_bf16 v[92:95], a[92:95], a[164:167], v[92:95]\n\t"
      "v_mul_f32_e64 v230, s84, |v202|\n\t"
      "v_mul_f32_e64 v231, s84, |v203|\n\t"
      "v_mfma_f32_16x16x32_bf16 v[92:95], a[100:103], a[168:171], v[92:95]\n\t"
      "s_waitcnt lgkmcnt(4)\n\t"
      "v_exp_f32_e32 v230, v230\n\t"
      "v_mfma_f32_16x16x32_bf16 v[92:95], a[108:111], a[172:175], v[92:95]\n\t"
      "v_exp_f32_e32 v231, v231\n\t"
      "v_mfma_f32_16x16x32_bf16 v[88:91], a[104:107], a[172:175], v[88:91]\n\t"
      "v_mul_f32_e32 v204, s86, v204\n\t"
      "v_mul_f32_e32 v205, s86, v205\n\t"
      "v_mfma_f32_16x16x32_bf16 v[88:91], a[96:99], a[168:171], v[88:91]\n\t"
      "v_exp_f32_e32 v204, v204\n\t"
      "v_mfma_f32_16x16x32_bf16 v[88:91], a[88:91], a[164:167], v[88:91]\n\t"
      "v_exp_f32_e32 v205, v205\n\t"
      "v_mfma_f32_16x16x32_bf16 v[88:91], a[80:83], a[160:163], v[88:91]\n\t"
      "v_mul_f32_e32 v206, s86, v206\n\t"
      "v_mul_f32_e32 v207, s86, v207\n\t"
      "v_mfma_f32_16x16x32_bf16 v[88:91], a[72:75], a[156:159], v[88:91]\n\t"
      "s_add_u32 m0, %[ismw], 8192\n\t"
      "v_exp_f32_e32 v206, v206\n\t"
      "v_mfma_f32_16x16x32_bf16 v[96:99], a[32:35], a[128:131], 0\n\t"
      "s_add_u32 s67, s72, 1048576\n\t"
      "v_exp_f32_e32 v207, v207\n\t"
      "v_mfma_f32_16x16x32_bf16 v[96:99], a[40:43], a[132:135], v[96:99]\n\t"
      "buffer_load_dwordx4 %[vw], s[56:59], s67 offen lds\n\t"
      "v_fma_f32 v228, v228, -0.5, -0.5\n\t"
      "v_mfma_f32_16x16x32_bf16 v[96:99], a[48:51], a[136:139], v[96:99]\n\t"
      "ds_read_b128 a[144:147], %[lx1] offset:4096\n\t"
      "v_fma_f32 v229, v229, -0.5, -0.5\n\t"
      "v_mfma_f32_16x16x32_bf16 v[96:99], a[56:59], a[140:143], v[96:99]\n\t"
      "s_waitcnt lgkmcnt(4)\n\t"
      "v_fma_f32 v230, v230, -0.5, -0.5\n\t"
      "v_fma_f32 v231, v231, -0.5, -0.5\n\t"
      "v_mfma_f32_16x16x32_bf16 v[96:99], a[64:67], a[112:115], v[96:99]\n\t"
      "v_fma_f32 v204, v228, v204, v228\n\t"
      "v_fma_f32 v205, v229, v205, v229\n\t"
      "v_mfma_f32_16x16x32_bf16 v[100:103], a[68:71], a[112:115], 0\n\t"
      "v_rcp_f32_e32 v204, v204\n\t"
      "v_mfma_f32_16x16x32_bf16 v[100:103], a[60:63], a[140:143], v[100:103]\n\t"
      "v_rcp_f32_e32 v205, v205\n\t"
      "v_mfma_f32_16x16x32_bf16 v[100:103], a[52:55], a[136:139], v[100:103]\n\t"
      "v_fma_f32 v206, v230, v206, v230\n\t"
      "v_fma_f32 v207, v231, v207, v231\n\t"
      "v_mfma_f32_16x16x32_bf16 v[100:103], a[44:47], a[132:135], v[100:103]\n\t"
      "ds_read_b128 a[148:151], %[lx1] offset:4352\n\t"
      "v_bfi_b32 v200, s75, v204, v200\n\t"
      "v_mfma_f32_16x16x32_bf16 v[100:103], a[36:39], a[128:131], v[100:103]\n\t"
      "v_rcp_f32_e32 v206, v206\n\t"
      "v_mfma_f32_16x16x32_bf16 v[108:111], a[36:39], a[132:135], 0\n\t"
      "v_rcp_f32_e32 v207, v207\n\t"
      "v_mfma_f32_16x16x32_bf16 v[108:111], a[44:47], a[136:139], v[108:111]\n\t"
      "v_bfi_b32 v201, s75, v205, v201\n\t"
      "v_bfi_b32 v202, s75, v206, v202\n\t"
      "v_mfma_f32_16x16x32_bf16 v[108:111], a[52:55], a[140:143], v[108:111]\n\t"
      "v_bfi_b32 v203, s75, v207, v203\n\t"
      "v_fmac_f32_e32 v200, v228, v200\n\t"
      "v_mfma_f32_16x16x32_bf16 v[108:111], a[60:63], a[112:115], v[108:111]\n\t"
      "s_waitcnt lgkmcnt(4)\n\t"
      "buffer_load_dwordx4 %[vw], s[56:59], s67 offen offset:1024 lds\n\t"
      "v_fmac_f32_e32 v201, v229, v201\n\t"
      "v_mfma_f32_16x16x32_bf16 v[108:111], a[68:71], a[116:119], v[108:111]\n\t"
      "ds_read_b128 a[152:155], %[lx1] offset:4608\n\t"
      "v_fmac_f32_e32 v202, v230, v202\n\t"
      "v_mfma_f32_16x16x32_bf16 v[104:107], a[64:67], a[116:119], 0\n\t"
      "v_fmac_f32_e32 v203, v231, v203\n\t"
      "v_cvt_pk_bf16_f32 v204, v200, v201\n\t"
      "v_mfma_f32_16x16x32_bf16 v[104:107], a[56:59], a[112:115], v[104:107]\n\t"
      "s_mov_b32 s68, 49152\n\t"
      "v_cvt_pk_bf16_f32 v205, v202, v203\n\t"
      "v_mul_f32_e64 v224, s84, |v208|\n\t"
      "v_mfma_f32_16x16x32_bf16 v[104:107], a[48:51], a[140:143], v[104:107]\n\t"
      "buffer_store_dwordx2 v[204:205], %[vo], s[52:55], s68 offen offset:2048\n\t"
      "v_mfma_f32_16x16x32_bf16 v[104:107], a[40:43], a[136:139], v[104:107]\n\t"
      "v_exp_f32_e32 v224, v224\n\t"
      "v_mfma_f32_16x16x32_bf16 v[104:107], a[32:35], a[132:135], v[104:107]\n\t"
      "v_mul_f32_e64 v225, s84, |v209|\n\t"
      "v_mul_f32_e64 v226, s84, |v210|\n\t"
      "v_mfma_f32_16x16x32_bf16 v[112:115], a[32:35], a[136:139], 0\n\t"
      "ds_read_b128 a[156:159], %[lx1] offset:4864\n\t"
      "v_mul_f32_e64 v227, s84, |v211|\n\t"
      "v_mfma_f32_16x16x32_bf16 v[112:115], a[40:43], a[140:143], v[112:115]\n\t"
      "v_exp_f32_e32 v225, v225\n\t"
      "v_mfma_f32_16x16x32_bf16 v[112:115], a[48:51], a[112:115], v[112:115]\n\t"
      "v_exp_f32_e32 v226, v226\n\t"
      "v_mfma_f32_16x16x32_bf16 v[112:115], a[56:59], a[116:119], v[112:115]\n\t"
      "s_waitcnt lgkmcnt(5)\n\t"
      "s_add_u32 m0, %[ismw], 16384\n\t"
      "v_mul_f32_e32 v212, s86, v212\n\t"
      "v_mfma_f32_16x16x32_bf16 v[112:115], a[64:67], a[120:123], v[112:115]\n\t"
      "s_add_u32 s67, s72, 2097152\n\t"
      "v_exp_f32_e32 v227, v227\n\t"
      "v_mfma_f32_16x16x32_bf16 v[116:119], a[68:71], a[120:123], 0\n\t"
      "buffer_load_dwordx4 %[vw], s[56:59], s67 offen lds\n\t"
      "v_mul_f32_e32 v213, s86, v213\n\t"
      "v_mfma_f32_16x16x32_bf16 v[116:119], a[60:63], a[116:119], v[116:119]\n\t"
      "v_exp_f32_e32 v212, v212\n\t"
      "v_mfma_f32_16x16x32_bf16 v[116:119], a[52:55], a[112:115], v[116:119]\n\t"
      "v_exp_f32_e32 v213, v213\n\t"
      "v_mfma_f32_16x16x32_bf16 v[116:119], a[44:47], a[140:143], v[116:119]\n\t"
      "v_mul_f32_e32 v214, s86, v214\n\t"
      "v_mul_f32_e32 v215, s86, v215\n\t"
      "v_mfma_f32_16x16x32_bf16 v[116:119], a[36:39], a[136:139], v[116:119]\n\t"
      "v_exp_f32_e32 v214, v214\n\t"
      "v_mfma_f32_16x16x32_bf16 v[124:127], a[36:39], a[140:143], 0\n\t"
      "v_exp_f32_e32 v215, v215\n\t"
      "v_mfma_f32_16x16x32_bf16 v[124:127], a[44:47], a[112:115], v[124:127]\n\t"
      "v_fma_f32 v224, v224, -0.5, -0.5\n\t"
      "v_fma_f32 v225, v225, -0.5, -0.5\n\t"
      "v_mfma_f32_16x16x32_bf16 v[124:127], a[52:55], a[116:119], v[124:127]\n\t"
      "v_fma_f32 v226, v226, -0.5, -0.5\n\t"
      "v_fma_f32 v227, v227, -0.5, -0.5\n\t"
      "v_mfma_f32_16x16x32_bf16 v[124:127], a[60:63], a[120:123], v[124:127]\n\t"
      "s_waitcnt lgkmcnt(4)\n\t"
      "v_fma_f32 v212, v224, v212, v224\n\t"
      "v_fma_f32 v213, v225, v213, v225\n\t"
      "v_mfma_f32_16x16x32_bf16 v[124:127], a[68:71], a[124:127], v[124:127]\n\t"
      "v_rcp_f32_e32 v212, v212\n\t"
      "v_mfma_f32_16x16x32_bf16 v[120:123], a[64:67], a[124:127], 0\n\t"
      "v_rcp_f32_e32 v213, v213\n\t"
      "v_mfma_f32_16x16x32_bf16 v[120:123], a[56:59], a[120:123], v[120:123]\n\t"
      "buffer_load_dwordx4 %[vw], s[56:59], s67 offen offset:1024 lds\n\t"
      "v_fma_f32 v214, v226, v214, v226\n\t"
      "v_mfma_f32_16x16x32_bf16 v[120:123], a[48:51], a[116:119], v[120:123]\n\t"
      "v_rcp_f32_e32 v214, v214\n\t"
      "v_mfma_f32_16x16x32_bf16 v[120:123], a[40:43], a[112:115], v[120:123]\n\t"
      "v_fma_f32 v215, v227, v215, v227\n\t"
      "v_bfi_b32 v208, s75, v212, v208\n\t"
      "v_mfma_f32_16x16x32_bf16 v[120:123], a[32:35], a[140:143], v[120:123]\n\t"
      "v_rcp_f32_e32 v215, v215\n\t"
      "v_mfma_f32_16x16x32_bf16 v[96:99], a[72:75], a[160:163], v[96:99]\n\t"
      "v_bfi_b32 v209, s75, v213, v209\n\t"
      "v_bfi_b32 v210, s75, v214, v210\n\t"
      "v_mfma_f32_16x16x32_bf16 v[96:99], a[80:83], a[164:167], v[96:99]\n\t"
      "v_bfi_b32 v211, s75, v215, v211\n\t"
      "v_fmac_f32_e32 v208, v224, v208\n\t"
      "v_mfma_f32_16x16x32_bf16 v[96:99], a[88:91], a[168:171], v[96:99]\n\t"
      "ds_read_b128 a[128:131], %[lx0] offset:5120\n\t"
      "v_fmac_f32_e32 v209, v225, v209\n\t"
      "v_mfma_f32_16x16x32_bf16 v[96:99], a[96:99], a[172:175], v[96:99]\n\t"
      "s_waitcnt lgkmcnt(4)\n\t"
      "v_fmac_f32_e32 v210, v226, v210\n\t"
      "v_fmac_f32_e32 v211, v227, v211\n\t"
      "v_mfma_f32_16x16x32_bf16 v[96:99], a[104:107], a[144:147], v[96:99]\n\t"
      "v_cvt_pk_bf16_f32 v212, v208, v209\n\t"
      "v_cvt_pk_bf16_f32 v213, v210, v211\n\t"
      "v_mfma_f32_16x16x32_bf16 v[100:103], a[108:111], a[144:147], v[100:103]\n\t"
      "s_mov_b32 s68, 53248\n\t"
      "v_mul_f32_e64 v228, s84, |v216|\n\t"
      "v_mul_f32_e64 v229, s84, |v217|\n\t"
      "v_mfma_f32_16x16x32_bf16 v[100:103], a[100:103], a[172:175], v[100:103]\n\t"
      "buffer_store_dwordx2 v[212:213], %[vo], s[52:55], s68 offen offset:0\n\t"
      "v_mfma_f32_16x16x32_bf16 v[100:103], a[92:95], a[168:171], v[100:103]\n\t"
      "s_add_u32 m0, %[ismw], 24576\n\t"
      "v_exp_f32_e32 v228, v228\n\t"
      "v_mfma_f32_16x16x32_bf16 v[100:103], a[84:87], a[164:167], v[100:103]\n\t"
      "s_add_u32 s67, s72, 3145728\n\t"
      "ds_read_b128 a[132:135], %[lx0] offset:5376\n\t"
      "v_mul_f32_e64 v230, s84, |v218|\n\t"
      "v_mfma_f32_16x16x32_bf16 v[100:103], a[76:79], a[160:163], v[100:103]\n\t"
      "buffer_load_dwordx4 %[vw], s[56:59], s67 offen lds\n\t"
      "v_mul_f32_e64 v231, s84, |v219|\n\t"
      "v_mfma_f32_16x16x32_bf16 v[108:111], a[76:79], a[164:167], v[108:111]\n\t"
      "v_exp_f32_e32 v229, v229\n\t"
      "v_mfma_f32_16x16x32_bf16 v[108:111], a[84:87], a[168:171], v[108:111]\n\t"
      "v_exp_f32_e32 v230, v230\n\t"
      "v_mfma_f32_16x16x32_bf16 v[108:111], a[92:95], a[172:175], v[108:111]\n\t"
      "v_exp_f32_e32 v231, v231\n\t"
      "v_mfma_f32_16x16x32_bf16 v[108:111], a[100:103], a[144:147], v[108:111]\n\t"
      "s_waitcnt lgkmcnt(4)\n\t"
      "v_mul_f32_e32 v220, s86, v220\n\t"
      "v_mul_f32_e32 v221, s86, v221\n\t"
      "v_mfma_f32_16x16x32_bf16 v[108:111], a[108:111], a[148:151], v[108:111]\n\t"
      "ds_read_b128 a[136:139], %[lx0] offset:5632\n\t"
      "v_mul_f32_e32 v222, s86, v222\n\t"
      "v_mfma_f32_16x16x32_bf16 v[104:107], a[104:107], a[148:151], v[104:107]\n\t"
      "v_exp_f32_e32 v220, v220\n\t"
      "v_mfma_f32_16x16x32_bf16 v[104:107], a[96:99], a[144:147], v[104:107]\n\t"
      "v_exp_f32_e32 v221, v221\n\t"
      "v_mfma_f32_16x16x32_bf16 v[104:107], a[88:91], a[172:175], v[104:107]\n\t"
      "v_exp_f32_e32 v222, v222\n\t"
      "v_mfma_f32_16x16x32_bf16 v[104:107], a[80:83], a[168:171], v[104:107]\n\t"
      "v_mul_f32_e32 v223, s86, v223\n\t"
      "v_fma_f32 v228, v228, -0.5, -0.5\n\t"
      "v_mfma_f32_16x16x32_bf16 v[104:107], a[72:75], a[164:167], v[104:107]\n\t"
      "v_exp_f32_e32 v223, v223\n\t"
      "v_mfma_f32_16x16x32_bf16 v[112:115], a[72:75], a[168:171], v[112:115]\n\t"
      "s_mov_b32 s69, 6\n\t"
      "ds_read_b128 a[140:143], %[lx0] offset:5888\n\t"
      "v_fma_f32 v229, v229, -0.5, -0.5\n\t"
      "v_mfma_f32_16x16x32_bf16 v[112:115], a[80:83], a[172:175], v[112:115]\n\t"
      "buffer_load_dwordx4 %[vw], s[56:59], s67 offen offset:1024 lds\n\t"
      "v_fma_f32 v230, v230, -0.5, -0.5\n\t"
      "v_mfma_f32_16x16x32_bf16 v[112:115], a[88:91], a[144:147], v[112:115]\n\t"
      "v_fma_f32 v231, v231, -0.5, -0.5\n\t"
      "v_fma_f32 v220, v228, v220, v228\n\t"
      "v_mfma_f32_16x16x32_bf16 v[112:115], a[96:99], a[148:151], v[112:115]\n\t"
      "s_waitcnt lgkmcnt(5)\n\t"
      "v_rcp_f32_e32 v220, v220\n\t"
      "v_mfma_f32_16x16x32_bf16 v[112:115], a[104:107], a[152:155], v[112:115]\n\t"
      "v_fma_f32 v221, v229, v221, v229\n\t"
      "v_fma_f32 v222, v230, v222, v230\n\t"
      "v_mfma_f32_16x16x32_bf16 v[116:119], a[108:111], a[152:155], v[116:119]\n\t"
      "v_rcp_f32_e32 v221, v221\n\t"
      "v_mfma_f32_16x16x32_bf16 v[116:119], a[100:103], a[148:151], v[116:119]\n\t"
      "v_rcp_f32_e32 v222, v222\n\t"
      "v_mfma_f32_16x16x32_bf16 v[116:119], a[92:95], a[144:147], v[116:119]\n\t"
      "v_fma_f32 v223, v231, v223, v231\n\t"
      "v_bfi_b32 v216, s75, v220, v216\n\t"
      "v_mfma_f32_16x16x32_bf16 v[116:119], a[84:87], a[172:175], v[116:119]\n\t"
      "v_rcp_f32_e32 v223, v223\n\t"
      "v_mfma_f32_16x16x32_bf16 v[116:119], a[76:79], a[168:171], v[116:119]\n\t"
      "s_add_u32 m0, %[ismw], 32768\n\t"
      "v_bfi_b32 v217, s75, v221, v217\n\t"
      "v_bfi_b32 v218, s75, v222, v218\n\t"
      "v_mfma_f32_16x16x32_bf16 v[124:127], a[76:79], a[172:175], v[124:127]\n\t"
      "s_add_u32 s67, s72, 4194304\n\t"
      "v_bfi_b32 v219, s75, v223, v219\n\t"
      "v_fmac_f32_e32 v216, v228, v216\n\t"
      "v_mfma_f32_16x16x32_bf16 v[124:127], a[84:87], a[144:147], v[124:127]\n\t"
      "buffer_load_dwordx4 %[vw], s[56:59], s67 offen lds\n\t"
      "v_fmac_f32_e32 v217, v229, v217\n\t"
      "v_mfma_f32_16x16x32_bf16 v[124:127], a[92:95], a[148:151], v[124:127]\n\t"
      "v_fmac_f32_e32 v218, v230, v218\n\t"
      "v_fmac_f32_e32 v219, v231, v219\n\t"
      "v_mfma_f32_16x16x32_bf16 v[124:127], a[100:103], a[152:155], v[124:127]\n\t"
      "s_waitcnt lgkmcnt(4)\n\t"
      "v_cvt_pk_bf16_f32 v220, v216, v217\n\t"
      "v_cvt_pk_bf16_f32 v221, v218, v219\n\t"
      "v_mfma_f32_16x16x32_bf16 v[124:127], a[108:111], a[156:159], v[124:127]\n\t"
      "s_mov_b32 s68, 53248\n\t"
      "v_accvgpr_read_b32 v232, a0\n\t"
      "v_accvgpr_read_b32 v233, a1\n\t"
      "v_mfma_f32_16x16x32_bf16 v[120:123], a[104:107], a[156:159], v[120:123]\n\t"
      "buffer_store_dwordx2 v[220:221], %[vo], s[52:55], s68 offen offset:2048\n\t"
      "v_mfma_f32_16x16x32_bf16 v[120:123], a[96:99], a[152:155], v[120:123]\n\t"
      "v_accvgpr_read_b32 v234, a2\n\t"
      "v_accvgpr_read_b32 v235, a3\n\t"
      "v_mfma_f32_16x16x32_bf16 v[120:123], a[88:91], a[148:151], v[120:123]\n\t"
      "v_accvgpr_read_b32 v236, a4\n\t"
      "v_accvgpr_read_b32 v237, a5\n\t"
      "v_mfma_f32_16x16x32_bf16 v[120:123], a[80:83], a[144:147], v[120:123]\n\t"
      "v_accvgpr_read_b32 v238, a6\n\t"
      "v_accvgpr_read_b32 v239, a7\n\t"
      "v_mfma_f32_16x16x32_bf16 v[120:123], a[72:75], a[172:175], v[120:123]\n\t"
      "v_mul_f32_e64 v224, s84, |v232|\n\t"
      "v_mul_f32_e64 v225, s84, |v233|\n\t"
      "v_mfma_f32_16x16x32_bf16 v[128:131], a[32:35], a[112:115], 0\n\t"
      "v_exp_f32_e32 v224, v224\n\t"
      "v_mfma_f32_16x16x32_bf16 v[128:131], a[40:43], a[116:119], v[128:131]\n\t"
      "v_exp_f32_e32 v225, v225\n\t"
      "v_mfma_f32_16x16x32_bf16 v[128:131], a[48:51], a[120:123], v[128:131]\n\t"
      "ds_read_b128 a[160:163], %[lx1] offset:5120\n\t"
      "v_mul_f32_e64 v226, s84, |v234|\n\t"
      "v_mfma_f32_16x16x32_bf16 v[128:131], a[56:59], a[124:127], v[128:131]\n\t"
      "s_waitcnt lgkmcnt(4)\n\t"
      "buffer_load_dwordx4 %[vw], s[56:59], s67 offen offset:1024 lds\n\t"
      "v_mul_f32_e64 v227, s84, |v235|\n\t"
      "v_mfma_f32_16x16x32_bf16 v[128:131], a[64:67], a[128:131], v[128:131]\n\t"
      "v_exp_f32_e32 v226, v226\n\t"
      "v_mfma_f32_16x16x32_bf16 v[132:135], a[68:71], a[128:131], 0\n\t"
      "v_exp_f32_e32 v227, v227\n\t"
      "v_mfma_f32_16x16x32_bf16 v[132:135], a[60:63], a[124:127], v[132:135]\n\t"
      "v_mul_f32_e32 v236, s86, v236\n\t"
      "v_mul_f32_e32 v237, s86, v237\n\t"
      "v_mfma_f32_16x16x32_bf16 v[132:135], a[52:55], a[120:123], v[132:135]\n\t"
      "v_exp_f32_e32 v236, v236\n\t"
      "v_mfma_f32_16x16x32_bf16 v[132:135], a[44:47], a[116:119], v[132:135]\n\t"
      "ds_read_b128 a[164:167], %[lx1] offset:5376\n\t"
      "v_mul_f32_e32 v238, s86, v238\n\t"
      "v_mfma_f32_16x16x32_bf16 v[132:135], a[36:39], a[112:115], v[132:135]\n\t"
      "v_exp_f32_e32 v237, v237\n\t"
      "v_mfma_f32_16x16x32_bf16 v[140:143], a[36:39], a[116:119], 0\n\t"
      "v_exp_f32_e32 v238, v238\n\t"
      "v_mfma_f32_16x16x32_bf16 v[140:143], a[44:47], a[120:123], v[140:143]\n\t"
      "v_mul_f32_e32 v239, s86, v239\n\t"
      "v_fma_f32 v224, v224, -0.5, -0.5\n\t"
      "v_mfma_f32_16x16x32_bf16 v[140:143], a[52:55], a[124:127], v[140:143]\n\t"
      "v_exp_f32_e32 v239, v239\n\t"
      "v_mfma_f32_16x16x32_bf16 v[140:143], a[60:63], a[128:131], v[140:143]\n\t"
      "s_waitcnt lgkmcnt(4)\n\t"
      "s_add_u32 m0, %[ismw], 40960\n\t"
      "v_fma_f32 v225, v225, -0.5, -0.5\n\t"
      "v_mfma_f32_16x16x32_bf16 v[140:143], a[68:71], a[132:135], v[140:143]\n\t"
      "s_add_u32 s67, s72, 65536\n\t"
      "ds_read_b128 a[168:171], %[lx1] offset:5632\n\t"
      "v_fma_f32 v226, v226, -0.5, -0.5\n\t"
      "v_mfma_f32_16x16x32_bf16 v[136:139], a[64:67], a[132:135], 0\n\t"
      "buffer_load_dwordx4 %[vw], s[56:59], s67 offen lds\n\t"
      "v_fma_f32 v227, v227, -0.5, -0.5\n\t"
      "v_mfma_f32_16x16x32_bf16 v[136:139], a[56:59], a[128:131], v[136:139]\n\t"
      "v_fma_f32 v236, v224, v236, v224\n\t"
      "v_fma_f32 v237, v225, v237, v225\n\t"
      "v_mfma_f32_16x16x32_bf16 v[136:139], a[48:51], a[124:127], v[136:139]\n\t"
      "v_rcp_f32_e32 v236, v236\n\t"
      "v_mfma_f32_16x16x32_bf16 v[136:139], a[40:43], a[120:123], v[136:139]\n\t"
      "v_rcp_f32_e32 v237, v237\n\t"
      "v_mfma_f32_16x16x32_bf16 v[136:139], a[32:35], a[116:119], v[136:139]\n\t"
      "v_fma_f32 v238, v226, v238, v226\n\t"
      "v_fma_f32 v239, v227, v239, v227\n\t"
      "v_mfma_f32_16x16x32_bf16 v[144:147], a[32:35], a[120:123], 0\n\t"
      "ds_read_b128 a[172:175], %[lx1] offset:5888\n\t"
      "v_bfi_b32 v232, s75, v236, v232\n\t"
      "v_mfma_f32_16x16x32_bf16 v[144:147], a[40:43], a[124:127], v[144:147]\n\t"
      "v_rcp_f32_e32 v238, v238\n\t"
      "v_mfma_f32_16x16x32_bf16 v[144:147], a[48:51], a[128:131], v[144:147]\n\t"
      "v_rcp_f32_e32 v239, v239\n\t"
      "v_mfma_f32_16x16x32_bf16 v[144:147], a[56:59], a[132:135], v[144:147]\n\t"
      "s_waitcnt lgkmcnt(5)\n\t"
      "v_bfi_b32 v233, s75, v237, v233\n\t"
      "v_bfi_b32 v234, s75, v238, v234\n\t"
      "v_mfma_f32_16x16x32_bf16 v[144:147], a[64:67], a[136:139], v[144:147]\n\t"
      "v_bfi_b32 v235, s75, v239, v235\n\t"
      "v_fmac_f32_e32 v232, v224, v232\n\t"
      "v_mfma_f32_16x16x32_bf16 v[148:151], a[68:71], a[136:139], 0\n\t"
      "v_fmac_f32_e32 v233, v225, v233\n\t"
      "v_fmac_f32_e32 v234, v226, v234\n\t"
      "v_mfma_f32_16x16x32_bf16 v[148:151], a[60:63], a[132:135], v[148:151]\n\t"
      "buffer_load_dwordx4 %[vw], s[56:59], s67 offen offset:1024 lds\n\t"
      "v_fmac_f32_e32 v235, v227, v235\n\t"
      "v_mfma_f32_16x16x32_bf16 v[148:151], a[52:55], a[128:131], v[148:151]\n\t"
      "v_cvt_pk_bf16_f32 v236, v232, v233\n\t"
      "v_cvt_pk_bf16_f32 v237, v234, v235\n\t"
      "v_mfma_f32_16x16x32_bf16 v[148:151], a[44:47], a[124:127], v[148:151]\n\t"
      "s_mov_b32 s68, 57344\n\t"
      "v_accvgpr_read_b32 v240, a8\n\t"
      "v_accvgpr_read_b32 v241, a9\n\t"
      "v_mfma_f32_16x16x32_bf16 v[148:151], a[36:39], a[120:123], v[148:151]\n\t"
      "buffer_store_dwordx2 v[236:237], %[vo], s[52:55], s68 offen offset:0\n\t"
      "v_mfma_f32_16x16x32_bf16 v[156:159], a[36:39], a[124:127], 0\n\t"
      "v_accvgpr_read_b32 v242, a10\n\t"
      "v_accvgpr_read_b32 v243, a11\n\t"
      "v_mfma_f32_16x16x32_bf16 v[156:159], a[44:47], a[128:131], v[156:159]\n\t"
      "v_accvgpr_read_b32 v244, a12\n\t"
      "v_accvgpr_read_b32 v245, a13\n\t"
      "v_mfma_f32_16x16x32_bf16 v[156:159], a[52:55], a[132:135], v[156:159]\n\t"
      "v_accvgpr_read_b32 v246, a14\n\t"
      "v_accvgpr_read_b32 v247, a15\n\t"
      "v_mfma_f32_16x16x32_bf16 v[156:159], a[60:63], a[136:139], v[156:159]\n\t"
      "s_waitcnt lgkmcnt(4)\n\t"
      "v_mul_f32_e64 v228, s84, |v240|\n\t"
      "v_mul_f32_e64 v229, s84, |v241|\n\t"
      "v_mfma_f32_16x16x32_bf16 v[156:159], a[68:71], a[140:143], v[156:159]\n\t"
      "v_exp_f32_e32 v228, v228\n\t"
      "v_mfma_f32_16x16x32_bf16 v[152:155], a[64:67], a[140:143], 0\n\t"
      "v_exp_f32_e32 v229, v229\n\t"
      "v_mfma_f32_16x16x32_bf16 v[152:155], a[56:59], a[136:139], v[152:155]\n\t"
      "s_add_u32 m0, %[ismw], 49152\n\t"
      "v_mul_f32_e64 v230, s84, |v242|\n\t"
      "v_mul_f32_e64 v231, s84, |v243|\n\t"
      "v_mfma_f32_16x16x32_bf16 v[152:155], a[48:51], a[132:135], v[152:155]\n\t"
      "s_add_u32 s67, s72, 1114112\n\t"
      "v_exp_f32_e32 v230, v230\n\t"
      "v_mfma_f32_16x16x32_bf16 v[152:155], a[40:43], a[128:131], v[152:155]\n\t"
      "buffer_load_dwordx4 %[vw], s[56:59], s67 offen lds\n\t"
      "v_mul_f32_e32 v244, s86, v244\n\t"
      "v_mfma_f32_16x16x32_bf16 v[152:155], a[32:35], a[124:127], v[152:155]\n\t"
      "s_waitcnt vmcnt(27) lgkmcnt(0)\n\t"
      "s_barrier\n\t"
      "v_mul_f32_e32 v245, s86, v245\n\t"
      "v_mfma_f32_16x16x32_bf16 v[128:131], a[72:75], a[144:147], v[128:131]\n\t"
      "v_exp_f32_e32 v231, v231\n\t"
      "v_mfma_f32_16x16x32_bf16 v[128:131], a[80:83], a[148:151], v[128:131]\n\t"
      "v_exp_f32_e32 v244, v244\n\t"
      "v_mfma_f32_16x16x32_bf16 v[128:131], a[88:91], a[152:155], v[128:131]\n\t"
      "ds_read_b128 a[112:115], %[lx0] offset:6144\n\t"
      "v_mul_f32_e32 v246, s86, v246\n\t"
      "v_mfma_f32_16x16x32_bf16 v[128:131], a[96:99], a[156:159], v[128:131]\n\t"
      "v_exp_f32_e32 v245, v245\n\t"
      "v_mfma_f32_16x16x32_bf16 v[128:131], a[104:107], a[160:163], v[128:131]\n\t"
      "v_exp_f32_e32 v246, v246\n\t"
      "v_mfma_f32_16x16x32_bf16 v[132:135], a[108:111], a[160:163], v[132:135]\n\t"
      "v_mul_f32_e32 v247, s86, v247\n\t"
      "v_fma_f32 v228, v228, -0.5, -0.5\n\t"
      "v_mfma_f32_16x16x32_bf16 v[132:135], a[100:103], a[156:159], v[132:135]\n\t"
      "v_exp_f32_e32 v247, v247\n\t"
      "v_mfma_f32_16x16x32_bf16 v[132:135], a[92:95], a[152:155], v[132:135]\n\t"
      "v_fma_f32 v229, v229, -0.5, -0.5\n\t"
      "v_fma_f32 v230, v230, -0.5, -0.5\n\t"
      "v_mfma_f32_16x16x32_bf16 v[132:135], a[84:87], a[148:151], v[132:135]\n\t"
      "ds_read_b128 a[116:119], %[lx0] offset:6400\n\t"
      "v_fma_f32 v231, v231, -0.5, -0.5\n\t"
      "v_mfma_f32_16x16x32_bf16 v[132:135], a[76:79], a[144:147], v[132:135]\n\t"
      "buffer_load_dwordx4 %[vw], s[56:59], s67 offen offset:1024 lds\n\t"
      "v_fma_f32 v244, v228, v244, v228\n\t"
      "v_mfma_f32_16x16x32_bf16 v[140:143], a[76:79], a[148:151], v[140:143]\n\t"
      "v_rcp_f32_e32 v244, v244\n\t"
      "v_mfma_f32_16x16x32_bf16 v[140:143], a[84:87], a[152:155], v[140:143]\n\t"
      "v_fma_f32 v245, v229, v245, v229\n\t"
      "v_fma_f32 v246, v230, v246, v230\n\t"
      "v_mfma_f32_16x16x32_bf16 v[140:143], a[92:95], a[156:159], v[140:143]\n\t"
      "v_rcp_f32_e32 v245, v245\n\t"
      "v_mfma_f32_16x16x32_bf16 v[140:143], a[100:103], a[160:163], v[140:143]\n\t"
      "v_rcp_f32_e32 v246, v246\n\t"
      "v_mfma_f32_16x16x32_bf16 v[140:143], a[108:111], a[164:167], v[140:143]\n\t"
      "ds_read_b128 a[120:123], %[lx0] offset:6656\n\t"
      "v_fma_f32 v247, v231, v247, v231\n\t"
      "v_mfma_f32_16x16x32_bf16 v[136:139], a[104:107], a[164:167], v[136:139]\n\t"
      "v_rcp_f32_e32 v247, v247\n\t"
      "v_mfma_f32_16x16x32_bf16 v[136:139], a[96:99], a[160:163], v[136:139]\n\t"
      "v_bfi_b32 v240, s75, v244, v240\n\t"
      "v_bfi_b32 v241, s75, v245, v241\n\t"
      "v_mfma_f32_16x16x32_bf16 v[136:139], a[88:91], a[156:159], v[136:139]\n\t"
      "v_bfi_b32 v242, s75, v246, v242\n\t"
      "v_bfi_b32 v243, s75, v247, v243\n\t"
      "v_mfma_f32_16x16x32_bf16 v[136:139], a[80:83], a[152:155], v[136:139]\n\t"
      "v_fmac_f32_e32 v240, v228, v240\n\t"
      "v_fmac_f32_e32 v241, v229, v241\n\t"
      "v_mfma_f32_16x16x32_bf16 v[136:139], a[72:75], a[148:151], v[136:139]\n\t"
      "s_add_u32 m0, %[ismw], 57344\n\t"
      "v_fmac_f32_e32 v242, v230, v242\n\t"
      "v_fmac_f32_e32 v243, v231, v243\n\t"
      "v_mfma_f32_16x16x32_bf16 v[144:147], a[72:75], a[152:155], v[144:147]\n\t"
      "s_add_u32 s67, s72, 2162688\n\t"
      "ds_read_b128 a[124:127], %[lx0] offset:6912\n\t"
      "v_cvt_pk_bf16_f32 v244, v240, v241\n\t"
      "v_mfma_f32_16x16x32_bf16 v[144:147], a[80:83], a[156:159], v[144:147]\n\t"
      "buffer_load_dwordx4 %[vw], s[56:59], s67 offen lds\n\t"
      "v_cvt_pk_bf16_f32 v245, v242, v243\n\t"
      "v_mfma_f32_16x16x32_bf16 v[144:147], a[88:91], a[160:163], v[144:147]\n\t"
      "s_mov_b32 s68, 57344\n\t"
      "v_accvgpr_read_b32 v232, a16\n\t"
      "v_accvgpr_read_b32 v233, a17\n\t"
      "v_mfma_f32_16x16x32_bf16 v[144:147], a[96:99], a[164:167], v[144:147]\n\t"
      "buffer_store_dwordx2 v[244:245], %[vo], s[52:55], s68 offen offset:2048\n\t"
      "v_mfma_f32_16x16x32_bf16 v[144:147], a[104:107], a[168:171], v[144:147]\n\t"
      "v_accvgpr_read_b32 v234, a18\n\t"
      "v_accvgpr_read_b32 v235, a19\n\t"
      "v_mfma_f32_16x16x32_bf16 v[148:151], a[108:111], a[168:171], v[148:151]\n\t"
      "v_accvgpr_read_b32 v236, a20\n\t"
      "v_accvgpr_read_b32 v237, a21\n\t"
      "v_mfma_f32_16x16x32_bf16 v[148:151], a[100:103], a[164:167], v[148:151]\n\t"
      "v_accvgpr_read_b32 v238, a22\n\t"
      "v_accvgpr_read_b32 v239, a23\n\t"
      "v_mfma_f32_16x16x32_bf16 v[148:151], a[92:95], a[160:163], v[148:151]\n\t"
      "v_mul_f32_e64 v224, s84, |v232|\n\t"
      "v_mul_f32_e64 v225, s84, |v233|\n\t"
      "v_mfma_f32_16x16x32_bf16 v[148:151], a[84:87], a[156:159], v[148:151]\n\t"
      "v_exp_f32_e32 v224, v224\n\t"
      "v_mfma_f32_16x16x32_bf16 v[148:151], a[76:79], a[152:155], v[148:151]\n\t"
      "v_exp_f32_e32 v225, v225\n\t"
      "v_mfma_f32_16x16x32_bf16 v[156:159], a[76:79], a[156:159], v[156:159]\n\t"
      "v_mul_f32_e64 v226, s84, |v234|\n\t"
      "v_mul_f32_e64 v227, s84, |v235|\n\t"
      "v_mfma_f32_16x16x32_bf16 v[156:159], a[84:87], a[160:163], v[156:159]\n\t"
      "v_exp_f32_e32 v226, v226\n\t"
      "v_mfma_f32_16x16x32_bf16 v[156:159], a[92:95], a[164:167], v[156:159]\n\t"
      "buffer_load_dwordx4 %[vw], s[56:59], s67 offen offset:1024 lds\n\t"
      "v_mul_f32_e32 v236, s86, v236\n\t"
      "v_mfma_f32_16x16x32_bf16 v[156:159], a[100:103], a[168:171], v[156:159]\n\t"
      "v_exp_f32_e32 v227, v227\n\t"
      "v_mfma_f32_16x16x32_bf16 v[156:159], a[108:111], a[172:175], v[156:159]\n\t"
      "v_exp_f32_e32 v236, v236\n\t"
      "v_mfma_f32_16x16x32_bf16 v[152:155], a[104:107], a[172:175], v[152:155]\n\t"
      "v_mul_f32_e32 v237, s86, v237\n\t"
      "v_mul_f32_e32 v238, s86, v238\n\t"
      "v_mfma_f32_16x16x32_bf16 v[152:155], a[96:99], a[168:171], v[152:155]\n\t"
      "v_exp_f32_e32 v237, v237\n\t"
      "v_mfma_f32_16x16x32_bf16 v[152:155], a[88:91], a[164:167], v[152:155]\n\t"
      "v_exp_f32_e32 v238, v238\n\t"
      "v_mfma_f32_16x16x32_bf16 v[152:155], a[80:83], a[160:163], v[152:155]\n\t"
      "v_mul_f32_e32 v239, s86, v239\n\t"
      "v_fma_f32 v224, v224, -0.5, -0.5\n\t"
      "v_mfma_f32_16x16x32_bf16 v[152:155], a[72:75], a[156:159], v[152:155]\n\t"
      "v_exp_f32_e32 v239, v239\n\t"
      "v_mfma_f32_16x16x32_bf16 v[160:163], a[32:35], a[128:131], 0\n\t"
      "v_fma_f32 v225, v225, -0.5, -0.5\n\t"
      "v_fma_f32 v226, v226, -0.5, -0.5\n\t"
      "v_mfma_f32_16x16x32_bf16 v[160:163], a[40:43], a[132:135], v[160:163]\n\t"
      "v_fma_f32 v227, v227, -0.5, -0.5\n\t"
      "v_fma_f32 v236, v224, v236, v224\n\t"
      "v_mfma_f32_16x16x32_bf16 v[160:163], a[48:51], a[136:139], v[160:163]\n\t"
      "s_add_u32 m0, %[ismw], 65536\n\t"
      "ds_read_b128 a[144:147], %[lx1] offset:6144\n\t"
      "v_fma_f32 v237, v225, v237, v225\n\t"
      "v_mfma_f32_16x16x32_bf16 v[160:163], a[56:59], a[140:143], v[160:163]\n\t"
      "s_waitcnt lgkmcnt(4)\n\t"
      "s_add_u32 s67, s72, 3211264\n\t"
      "v_fma_f32 v238, v226, v238, v226\n\t"
      "v_mfma_f32_16x16x32_bf16 v[160:163], a[64:67], a[112:115], v[160:163]\n\t"
      "buffer_load_dwordx4 %[vw], s[56:59], s67 offen lds\n\t"
      "v_fma_f32 v239, v227, v239, v227\n\t"
      "v_mfma_f32_16x16x32_bf16 v[164:167], a[68:71], a[112:115], 0\n\t"
      "v_rcp_f32_e32 v236, v236\n\t"
      "v_mfma_f32_16x16x32_bf16 v[164:167], a[60:63], a[140:143], v[164:167]\n\t"
      "v_rcp_f32_e32 v237, v237\n\t"
      "v_mfma_f32_16x16x32_bf16 v[164:167], a[52:55], a[136:139], v[164:167]\n\t"
      "v_rcp_f32_e32 v238, v238\n\t"
      "v_mfma_f32_16x16x32_bf16 v[164:167], a[44:47], a[132:135], v[164:167]\n\t"
      "ds_read_b128 a[148:151], %[lx1] offset:6400\n\t"
      "v_bfi_b32 v232, s75, v236, v232\n\t"
      "v_mfma_f32_16x16x32_bf16 v[164:167], a[36:39], a[128:131], v[164:167]\n\t"
      "v_rcp_f32_e32 v239, v239\n\t"
      "v_mfma_f32_16x16x32_bf16 v[172:175], a[36:39], a[132:135], 0\n\t"
      "v_bfi_b32 v233, s75, v237, v233\n\t"
      "v_bfi_b32 v234, s75, v238, v234\n\t"
      "v_mfma_f32_16x16x32_bf16 v[172:175], a[44:47], a[136:139], v[172:175]\n\t"
      "v_bfi_b32 v235, s75, v239, v235\n\t"
      "v_fmac_f32_e32 v232, v224, v232\n\t"
      "v_mfma_f32_16x16x32_bf16 v[172:175], a[52:55], a[140:143], v[172:175]\n\t"
      "v_fmac_f32_e32 v233, v225, v233\n\t"
      "v_fmac_f32_e32 v234, v226, v234\n\t"
      "v_mfma_f32_16x16x32_bf16 v[172:175], a[60:63], a[112:115], v[172:175]\n\t"
      "s_waitcnt lgkmcnt(4)\n\t"
      "v_fmac_f32_e32 v235, v227, v235\n\t"
      "v_cvt_pk_bf16_f32 v236, v232, v233\n\t"
      "v_mfma_f32_16x16x32_bf16 v[172:175], a[68:71], a[116:119], v[172:175]\n\t"
      "ds_read_b128 a[152:155], %[lx1] offset:6656\n\t"
      "v_cvt_pk_bf16_f32 v237, v234, v235\n\t"
      "v_mfma_f32_16x16x32_bf16 v[168:171], a[64:67], a[116:119], 0\n\t"
      "s_cmp_gt_u32 s79, 30\n\ts_cselect_b32 s68, 61440, s80\n\t"
      "v_accvgpr_read_b32 v240, a24\n\t"
      "v_mfma_f32_16x16x32_bf16 v[168:171], a[56:59], a[112:115], v[168:171]\n\t"
      "buffer_load_dwordx4 %[vw], s[56:59], s67 offen offset:1024 lds\n\t"
      "v_accvgpr_read_b32 v241, a25\n\t"
      "v_mfma_f32_16x16x32_bf16 v[168:171], a[48:51], a[140:143], v[168:171]\n\t"
      "buffer_store_dwordx2 v[236:237], %[vo], s[52:55], s68 offen offset:0\n\t"
      "v_mfma_f32_16x16x32_bf16 v[168:171], a[40:43], a[136:139], v[168:171]\n\t"
      "v_accvgpr_read_b32 v242, a26\n\t"
      "v_accvgpr_read_b32 v243, a27\n\t"
      "v_mfma_f32_16x16x32_bf16 v[168:171], a[32:35], a[132:135], v[168:171]\n\t"
      "v_accvgpr_read_b32 v244, a28\n\t"
      "v_accvgpr_read_b32 v245, a29\n\t"
      "v_mfma_f32_16x16x32_bf16 v[176:179], a[32:35], a[136:139], 0\n\t"
      "ds_read_b128 a[156:159], %[lx1] offset:6912\n\t"
      "v_accvgpr_read_b32 v246, a30\n\t"
      "v_mfma_f32_16x16x32_bf16 v[176:179], a[40:43], a[140:143], v[176:179]\n\t"
      "v_accvgpr_read_b32 v247, a31\n\t"
      "v_mul_f32_e64 v228, s84, |v240|\n\t"
      "v_mfma_f32_16x16x32_bf16 v[176:179], a[48:51], a[112:115], v[176:179]\n\t"
      "v_exp_f32_e32 v228, v228\n\t"
      "v_mfma_f32_16x16x32_bf16 v[176:179], a[56:59], a[116:119], v[176:179]\n\t"
      "s_waitcnt lgkmcnt(5)\n\t"
      "v_mul_f32_e64 v229, s84, |v241|\n\t"
      "v_mul_f32_e64 v230, s84, |v242|\n\t"
      "v_mfma_f32_16x16x32_bf16 v[176:179], a[64:67], a[120:123], v[176:179]\n\t"
      "v_exp_f32_e32 v229, v229\n\t"
      "v_mfma_f32_16x16x32_bf16 v[180:183], a[68:71], a[120:123], 0\n\t"
      "s_add_u32 m0, %[ismw], 73728\n\t"
      "v_exp_f32_e32 v230, v230\n\t"
      "v_mfma_f32_16x16x32_bf16 v[180:183], a[60:63], a[116:119], v[180:183]\n\t"
      "s_add_u32 s67, s72, 4259840\n\t"
      "v_mul_f32_e64 v231, s84, |v243|\n\t"
      "v_mul_f32_e32 v244, s86, v244\n\t"
      "v_mfma_f32_16x16x32_bf16 v[180:183], a[52:55], a[112:115], v[180:183]\n\t"
      "buffer_load_dwordx4 %[vw], s[56:59], s67 offen lds\n\t"
      "v_mul_f32_e32 v245, s86, v245\n\t"
      "v_mfma_f32_16x16x32_bf16 v[180:183], a[44:47], a[140:143], v[180:183]\n\t"
      "v_exp_f32_e32 v231, v231\n\t"
      "v_mfma_f32_16x16x32_bf16 v[180:183], a[36:39], a[136:139], v[180:183]\n\t"
      "v_exp_f32_e32 v244, v244\n\t"
      "v_mfma_f32_16x16x32_bf16 v[188:191], a[36:39], a[140:143], 0\n\t"
      "v_exp_f32_e32 v245, v245\n\t"
      "v_mfma_f32_16x16x32_bf16 v[188:191], a[44:47], a[112:115], v[188:191]\n\t"
      "v_mul_f32_e32 v246, s86, v246\n\t"
      "v_mul_f32_e32 v247, s86, v247\n\t"
      "v_mfma_f32_16x16x32_bf16 v[188:191], a[52:55], a[116:119], v[188:191]\n\t"
      "v_exp_f32_e32 v246, v246\n\t"
      "v_mfma_f32_16x16x32_bf16 v[188:191], a[60:63], a[120:123], v[188:191]\n\t"
      "s_waitcnt lgkmcnt(4)\n\t"
      "v_exp_f32_e32 v247, v247\n\t"
      "v_mfma_f32_16x16x32_bf16 v[188:191], a[68:71], a[124:127], v[188:191]\n\t"
      "v_fma_f32 v228, v228, -0.5, -0.5\n\t"
      "v_fma_f32 v229, v229, -0.5, -0.5\n\t"
      "v_mfma_f32_16x16x32_bf16 v[184:187], a[64:67], a[124:127], 0\n\t"
      "v_fma_f32 v230, v230, -0.5, -0.5\n\t"
      "v_fma_f32 v231, v231, -0.5, -0.5\n\t"
      "v_mfma_f32_16x16x32_bf16 v[184:187], a[56:59], a[120:123], v[184:187]\n\t"
      "v_fma_f32 v244, v228, v244, v228\n\t"
      "v_fma_f32 v245, v229, v245, v229\n\t"
      "v_mfma_f32_16x16x32_bf16 v[184:187], a[48:51], a[116:119], v[184:187]\n\t"
      "v_rcp_f32_e32 v244, v244\n\t"
      "v_mfma_f32_16x16x32_bf16 v[184:187], a[40:43], a[112:115], v[184:187]\n\t"
      "v_rcp_f32_e32 v245, v245\n\t"
      "v_mfma_f32_16x16x32_bf16 v[184:187], a[32:35], a[140:143], v[184:187]\n\t"
      "buffer_load_dwordx4 %[vw], s[56:59], s67 offen offset:1024 lds\n\t"
      "v_fma_f32 v246, v230, v246, v230\n\t"
      "v_mfma_f32_16x16x32_bf16 v[160:163], a[72:75], a[160:163], v[160:163]\n\t"
      "v_rcp_f32_e32 v246, v246\n\t"
      "v_mfma_f32_16x16x32_bf16 v[160:163], a[80:83], a[164:167], v[160:163]\n\t"
      "v_fma_f32 v247, v231, v247, v231\n\t"
      "v_bfi_b32 v240, s75, v244, v240\n\t"
      "v_mfma_f32_16x16x32_bf16 v[160:163], a[88:91], a[168:171], v[160:163]\n\t"
      "ds_read_b128 a[128:131], %[lx0] offset:7168\n\t"
      "v_bfi_b32 v241, s75, v245, v241\n\t"
      "v_mfma_f32_16x16x32_bf16 v[160:163], a[96:99], a[172:175], v[160:163]\n\t"
      "s_waitcnt lgkmcnt(4)\n\t"
      "v_rcp_f32_e32 v247, v247\n\t"
      "v_mfma_f32_16x16x32_bf16 v[160:163], a[104:107], a[144:147], v[160:163]\n\t"
      "v_bfi_b32 v242, s75, v246, v242\n\t"
      "v_bfi_b32 v243, s75, v247, v243\n\t"
      "v_mfma_f32_16x16x32_bf16 v[164:167], a[108:111], a[144:147], v[164:167]\n\t"
      "v_fmac_f32_e32 v240, v228, v240\n\t"
      "v_fmac_f32_e32 v241, v229, v241\n\t"
      "v_mfma_f32_16x16x32_bf16 v[164:167], a[100:103], a[172:175], v[164:167]\n\t"
      "v_fmac_f32_e32 v242, v230, v242\n\t"
      "v_fmac_f32_e32 v243, v231, v243\n\t"
      "v_mfma_f32_16x16x32_bf16 v[164:167], a[92:95], a[168:171], v[164:167]\n\t"
      "v_cvt_pk_bf16_f32 v244, v240, v241\n\t"
      "v_cvt_pk_bf16_f32 v245, v242, v243\n\t"
      "v_mfma_f32_16x16x32_bf16 v[164:167], a[84:87], a[164:167], v[164:167]\n\t"
      "ds_read_b128 a[132:135], %[lx0] offset:7424\n\t"
      "v_mov_b32 v200, 0\n\t"
      "v_mfma_f32_16x16x32_bf16 v[164:167], a[76:79], a[160:163], v[164:167]\n\t"
      "s_cmp_gt_u32 s79, 31\n\ts_cselect_b32 s68, 61440, s80\n\t"
      "v_mov_b32 v201, 0\n\t"
      "v_mfma_f32_16x16x32_bf16 v[172:175], a[76:79], a[164:167], v[172:175]\n\t"
      "buffer_store_dwordx2 v[244:245], %[vo], s[52:55], s68 offen offset:2048\n\t"
      "v_mfma_f32_16x16x32_bf16 v[172:175], a[84:87], a[168:171], v[172:175]\n\t"
      "v_mov_b32 v202, 0\n\t"
      "v_mov_b32 v203, 0\n\t"
      "v_mfma_f32_16x16x32_bf16 v[172:175], a[92:95], a[172:175], v[172:175]\n\t"
      "v_mov_b32 v204, 0\n\t"
      "v_mov_b32 v205, 0\n\t"
      "v_mfma_f32_16x16x32_bf16 v[172:175], a[100:103], a[144:147], v[172:175]\n\t"
      "s_waitcnt lgkmcnt(4)\n\t"
      "s_add_u32 m0, %[ismx], 3072\n\t"
      "v_mov_b32 v206, 0\n\t"
      "v_mfma_f32_16x16x32_bf16 v[172:175], a[108:111], a[148:151], v[172:175]\n\t"
      "s_add_u32 s67, s71, 24576\n\t"
      "ds_read_b128 a[136:139], %[lx0] offset:7680\n\t"
      "v_mov_b32 v207, 0\n\t"
      "v_mfma_f32_16x16x32_bf16 v[168:171], a[104:107], a[148:151], v[168:171]\n\t"
      "buffer_load_dwordx4 %[vx], s[48:51], s67 offen lds\n\t"
      "v_mov_b32 v208, 0\n\t"
      "v_mfma_f32_16x16x32_bf16 v[168:171], a[96:99], a[144:147], v[168:171]\n\t"
      "v_mov_b32 v209, 0\n\t"
      "v_mov_b32 v210, 0\n\t"
      "v_mfma_f32_16x16x32_bf16 v[168:171], a[88:91], a[172:175], v[168:171]\n\t"
      "v_mov_b32 v211, 0\n\t"
      "v_mov_b32 v212, 0\n\t"
      "v_mfma_f32_16x16x32_bf16 v[168:171], a[80:83], a[168:171], v[168:171]\n\t"
      "v_mov_b32 v213, 0\n\t"
      "v_mov_b32 v214, 0\n\t"
      "v_mfma_f32_16x16x32_bf16 v[168:171], a[72:75], a[164:167], v[168:171]\n\t"
      "v_mov_b32 v215, 0\n\t"
      "v_mov_b32 v216, 0\n\t"
      "v_mfma_f32_16x16x32_bf16 v[176:179], a[72:75], a[168:171], v[176:179]\n\t"
      "ds_read_b128 a[140:143], %[lx0] offset:7936\n\t"
      "v_mov_b32 v217, 0\n\t"
      "v_mfma_f32_16x16x32_bf16 v[176:179], a[80:83], a[172:175], v[176:179]\n\t"
      "v_mov_b32 v218, 0\n\t"
      "v_mov_b32 v219, 0\n\t"
      "v_mfma_f32_16x16x32_bf16 v[176:179], a[88:91], a[144:147], v[176:179]\n\t"
      "v_mov_b32 v220, 0\n\t"
      "v_mov_b32 v221, 0\n\t"
      "v_mfma_f32_16x16x32_bf16 v[176:179], a[96:99], a[148:151], v[176:179]\n\t"
      "s_waitcnt lgkmcnt(5)\n\t"
      "v_mov_b32 v222, 0\n\t"
      "v_mov_b32 v223, 0\n\t"
      "v_mfma_f32_16x16x32_bf16 v[176:179], a[104:107], a[152:155], v[176:179]\n\t"
      "v_accvgpr_write_b32 a0, 0\n\t"
      "v_accvgpr_write_b32 a1, 0\n\t"
      "v_mfma_f32_16x16x32_bf16 v[180:183], a[108:111], a[152:155], v[180:183]\n\t"
      "v_accvgpr_write_b32 a2, 0\n\t"
      "v_accvgpr_write_b32 a3, 0\n\t"
      "v_mfma_f32_16x16x32_bf16 v[180:183], a[100:103], a[148:151], v[180:183]\n\t"
      "v_accvgpr_write_b32 a4, 0\n\t"
      "v_accvgpr_write_b32 a5, 0\n\t"
      "v_mfma_f32_16x16x32_bf16 v[180:183], a[92:95], a[144:147], v[180:183]\n\t"
      "v_accvgpr_write_b32 a6, 0\n\t"
      "v_accvgpr_write_b32 a7, 0\n\t"
      "v_mfma_f32_16x16x32_bf16 v[180:183], a[84:87], a[172:175], v[180:183]\n\t"
      "v_accvgpr_write_b32 a8, 0\n\t"
      "v_accvgpr_write_b32 a9, 0\n\t"
      "v_mfma_f32_16x16x32_bf16 v[180:183], a[76:79], a[168:171], v[180:183]\n\t"
      "s_add_u32 m0, %[ismx], 39936\n\t"
      "v_accvgpr_write_b32 a10, 0\n\t"
      "v_accvgpr_write_b32 a11, 0\n\t"
      "v_mfma_f32_16x16x32_bf16 v[188:191], a[76:79], a[172:175], v[188:191]\n\t"
      "s_add_u32 s67, s76, 24576\n\t"
      "v_accvgpr_write_b32 a12, 0\n\t"
      "v_accvgpr_write_b32 a13, 0\n\t"
      "v_mfma_f32_16x16x32_bf16 v[188:191], a[84:87], a[144:147], v[188:191]\n\t"
      "buffer_load_dwordx4 %[vx], s[48:51], s67 offen lds\n\t"
      "v_accvgpr_write_b32 a14, 0\n\t"
      "v_mfma_f32_16x16x32_bf16 v[188:191], a[92:95], a[148:151], v[188:191]\n\t"
      "v_accvgpr_write_b32 a15, 0\n\t"
      "v_accvgpr_write_b32 a16, 0\n\t"
      "v_mfma_f32_16x16x32_bf16 v[188:191], a[100:103], a[152:155], v[188:191]\n\t"
      "s_waitcnt lgkmcnt(4)\n\t"
      "v_accvgpr_write_b32 a17, 0\n\t"
      "v_accvgpr_write_b32 a18, 0\n\t"
      "v_mfma_f32_16x16x32_bf16 v[188:191], a[108:111], a[156:159], v[188:191]\n\t"
      "v_accvgpr_write_b32 a19, 0\n\t"
      "v_accvgpr_write_b32 a20, 0\n\t"
      "v_mfma_f32_16x16x32_bf16 v[184:187], a[104:107], a[156:159], v[184:187]\n\t"
      "v_accvgpr_write_b32 a21, 0\n\t"
      "v_accvgpr_write_b32 a22, 0\n\t"
      "v_mfma_f32_16x16x32_bf16 v[184:187], a[96:99], a[152:155], v[184:187]\n\t"
      "v_accvgpr_write_b32 a23, 0\n\t"
      "v_accvgpr_write_b32 a24, 0\n\t"
      "v_mfma_f32_16x16x32_bf16 v[184:187], a[88:91], a[148:151], v[184:187]\n\t"
      "v_accvgpr_write_b32 a25, 0\n\t"
      "v_accvgpr_write_b32 a26, 0\n\t"
      "v_mfma_f32_16x16x32_bf16 v[184:187], a[80:83], a[144:147], v[184:187]\n\t"
      "v_accvgpr_write_b32 a27, 0\n\t"
      "v_accvgpr_write_b32 a28, 0\n\t"
      "v_mfma_f32_16x16x32_bf16 v[184:187], a[72:75], a[172:175], v[184:187]\n\t"
      "v_accvgpr_write_b32 a29, 0\n\t"
      "v_accvgpr_write_b32 a30, 0\n\t"
      "v_mfma_f32_16x16x32_bf16 v[192:195], a[32:35], a[112:115], 0\n\t"
      "v_accvgpr_write_b32 a31, 0\n\t"
      "v_mfma_f32_16x16x32_bf16 v[192:195], a[40:43], a[116:119], v[192:195]\n\t"
      "v_mfma_f32_16x16x32_bf16 v[192:195], a[48:51], a[120:123], v[192:195]\n\t"
      "v_mfma_f32_16x16x32_bf16 v[192:195], a[56:59], a[124:127], v[192:195]\n\t"
      "s_waitcnt lgkmcnt(3)\n\t"
      "v_mfma_f32_16x16x32_bf16 v[192:195], a[64:67], a[128:131], v[192:195]\n\t"
      "ds_read_b128 a[160:163], %[lx1] offset:7168\n\t"
      "v_mfma_f32_16x16x32_bf16 v[196:199], a[68:71], a[128:131], 0\n\t"
      "v_mfma_f32_16x16x32_bf16 v[196:199], a[60:63], a[124:127], v[196:199]\n\t"
      "v_mfma_f32_16x16x32_bf16 v[196:199], a[52:55], a[120:123], v[196:199]\n\t"
      "s_add_u32 m0, %[ismx], 4096\n\t"
      "v_mfma_f32_16x16x32_bf16 v[196:199], a[44:47], a[116:119], v[196:199]\n\t"
      "s_add_u32 s67, s71, 32768\n\t"
      "v_mfma_f32_16x16x32_bf16 v[196:199], a[36:39], a[112:115], v[196:199]\n\t"
      "buffer_load_dwordx4 %[vx], s[48:51], s67 offen lds\n\t"
      "s_branch LJOIN2_%=\n\t"
      "LSL_%=:\n\t"
      "v_mfma_f32_16x16x32_bf16 v[0:3], a[32:35], a[112:115], v[0:3]\n\t"
      "ds_read_b64_tr_b16 a[72:73], %[lw1] offset:0\n\t"
      "v_mfma_f32_16x16x32_bf16 v[0:3], a[40:43], a[116:119], v[0:3]\n\t"
      "s_waitcnt lgkmcnt(15)\n\t"
      "ds_read_b64_tr_b16 a[74:75], %[lw1] offset:512\n\t"
      "v_mfma_f32_16x16x32_bf16 v[0:3], a[48:51], a[120:123], v[0:3]\n\t"
      "s_waitcnt lgkmcnt(15)\n\t"
      "ds_read_b128 a[144:147], %[lx1] offset:0\n\t"
      "v_mfma_f32_16x16x32_bf16 v[0:3], a[56:59], a[124:127], v[0:3]\n\t"
      "s_waitcnt lgkmcnt(15)\n\t"
      "ds_read_b64_tr_b16 a[80:81], %[lw1] offset:8192\n\t"
      "v_mfma_f32_16x16x32_bf16 v[0:3], a[64:67], a[128:131], v[0:3]\n\t"
      "s_waitcnt lgkmcnt(15)\n\t"
      "ds_read_b64_tr_b16 a[82:83], %[lw1] offset:8704\n\t"
      "v_mfma_f32_16x16x32_bf16 v[4:7], a[68:71], a[128:131], v[4:7]\n\t"
      "s_waitcnt lgkmcnt(15)\n\t"
      "ds_read_b128 a[148:151], %[lx1] offset:256\n\t"
      "v_mfma_f32_16x16x32_bf16 v[4:7], a[60:63], a[124:127], v[4:7]\n\t"
      "s_waitcnt lgkmcnt(14)\n\t"
      "ds_read_b64_tr_b16 a[88:89], %[lw1] offset:16384\n\t"
      "v_mfma_f32_16x16x32_bf16 v[4:7], a[52:55], a[120:123], v[4:7]\n\t"
      "s_waitcnt lgkmcnt(13)\n\t"
      "ds_read_b64_tr_b16 a[90:91], %[lw1] offset:16896\n\t"
      "v_mfma_f32_16x16x32_bf16 v[4:7], a[44:47], a[116:119], v[4:7]\n\t"
      "s_waitcnt lgkmcnt(11)\n\t"
      "ds_read_b128 a[152:155], %[lx1] offset:512\n\t"
      "v_mfma_f32_16x16x32_bf16 v[4:7], a[36:39], a[112:115], v[4:7]\n\t"
      "ds_read_b64_tr_b16 a[96:97], %[lw1] offset:24576\n\t"
      "v_mfma_f32_16x16x32_bf16 v[12:15], a[36:39], a[116:119], v[12:15]\n\t"
      "ds_read_b64_tr_b16 a[98:99], %[lw1] offset:25088\n\t"
      "v_mfma_f32_16x16x32_bf16 v[12:15], a[44:47], a[120:123], v[12:15]\n\t"
      "ds_read_b128 a[156:159], %[lx1] offset:768\n\t"
      "v_mfma_f32_16x16x32_bf16 v[12:15], a[52:55], a[124:127], v[12:15]\n\t"
      "ds_read_b64_tr_b16 a[104:105], %[lw1] offset:32768\n\t"
      "v_mfma_f32_16x16x32_bf16 v[12:15], a[60:63], a[128:131], v[12:15]\n\t"
      "s_waitcnt lgkmcnt(15)\n\t"
      "ds_read_b64_tr_b16 a[106:107], %[lw1] offset:33280\n\t"
      "v_mfma_f32_16x16x32_bf16 v[12:15], a[68:71], a[132:135], v[12:15]\n\t"
      "ds_read_b128 a[160:163], %[lx1] offset:1024\n\t"
      "v_mfma_f32_16x16x32_bf16 v[8:11], a[64:67], a[132:135], v[8:11]\n\t"
      "ds_read_b64_tr_b16 a[108:109], %[lw1] offset:33792\n\t"
      "v_mfma_f32_16x16x32_bf16 v[8:11], a[56:59], a[128:131], v[8:11]\n\t"
      "ds_read_b64_tr_b16 a[110:111], %[lw1] offset:34304\n\t"
      "v_mfma_f32_16x16x32_bf16 v[8:11], a[48:51], a[124:127], v[8:11]\n\t"
      "ds_read_b64_tr_b16 a[100:101], %[lw1] offset:25600\n\t"
      "v_mfma_f32_16x16x32_bf16 v[8:11], a[40:43], a[120:123], v[8:11]\n\t"
      "ds_read_b64_tr_b16 a[102:103], %[lw1] offset:26112\n\t"
      "v_mfma_f32_16x16x32_bf16 v[8:11], a[32:35], a[116:119], v[8:11]\n\t"
      "ds_read_b64_tr_b16 a[92:93], %[lw1] offset:17408\n\t"
      "v_mfma_f32_16x16x32_bf16 v[16:19], a[32:35], a[120:123], v[16:19]\n\t"
      "ds_read_b64_tr_b16 a[94:95], %[lw1] offset:17920\n\t"
      "v_mfma_f32_16x16x32_bf16 v[16:19], a[40:43], a[124:127], v[16:19]\n\t"
      "ds_read_b64_tr_b16 a[84:85], %[lw1] offset:9216\n\t"
      "v_mfma_f32_16x16x32_bf16 v[16:19], a[48:51], a[128:131], v[16:19]\n\t"
      "ds_read_b64_tr_b16 a[86:87], %[lw1] offset:9728\n\t"
      "v_mfma_f32_16x16x32_bf16 v[16:19], a[56:59], a[132:135], v[16:19]\n\t"
      "s_waitcnt lgkmcnt(15)\n\t"
      "ds_read_b64_tr_b16 a[76:77], %[lw1] offset:1024\n\t"
      "v_mfma_f32_16x16x32_bf16 v[16:19], a[64:67], a[136:139], v[16:19]\n\t"
      "ds_read_b64_tr_b16 a[78:79], %[lw1] offset:1536\n\t"
      "v_mfma_f32_16x16x32_bf16 v[20:23], a[68:71], a[136:139], v[20:23]\n\t"
      "ds_read_b128 a[164:167], %[lx1] offset:1280\n\t"
      "v_mfma_f32_16x16x32_bf16 v[20:23], a[60:63], a[132:135], v[20:23]\n\t"
      "ds_read_b128 a[168:171], %[lx1] offset:1536\n\t"
      "v_mfma_f32_16x16x32_bf16 v[20:23], a[52:55], a[128:131], v[20:23]\n\t"
      "ds_read_b128 a[172:175], %[lx1] offset:1792\n\t"
      "v_mfma_f32_16x16x32_bf16 v[20:23], a[44:47], a[124:127], v[20:23]\n\t"
      "v_mfma_f32_16x16x32_bf16 v[20:23], a[36:39], a[120:123], v[20:23]\n\t"
      "v_mfma_f32_16x16x32_bf16 v[28:31], a[36:39], a[124:127], v[28:31]\n\t"
      "v_mfma_f32_16x16x32_bf16 v[28:31], a[44:47], a[128:131], v[28:31]\n\t"
      "v_mfma_f32_16x16x32_bf16 v[28:31], a[52:55], a[132:135], v[28:31]\n\t"
      "s_add_u32 m0, %[ismx], 6144\n\t"
      "v_mfma_f32_16x16x32_bf16 v[28:31], a[60:63], a[136:139], v[28:31]\n\t"
      "s_waitcnt lgkmcnt(15)\n\t"
      "s_add_u32 s67, s71, 49152\n\t"
      "v_mfma_f32_16x16x32_bf16 v[28:31], a[68:71], a[140:143], v[28:31]\n\t"
      "buffer_load_dwordx4 %[vx], s[48:51], s67 offen lds\n\t"
      "v_mfma_f32_16x16x32_bf16 v[24:27], a[64:67], a[140:143], v[24:27]\n\t"
      "v_mfma_f32_16x16x32_bf16 v[24:27], a[56:59], a[136:139], v[24:27]\n\t"
      "v_mfma_f32_16x16x32_bf16 v[24:27], a[48:51], a[132:135], v[24:27]\n\t"
      "v_mfma_f32_16x16x32_bf16 v[24:27], a[40:43], a[128:131], v[24:27]\n\t"
      "v_mfma_f32_16x16x32_bf16 v[24:27], a[32:35], a[124:127], v[24:27]\n\t"
      "v_mfma_f32_16x16x32_bf16 v[0:3], a[72:75], a[144:147], v[0:3]\n\t"
      "v_mfma_f32_16x16x32_bf16 v[0:3], a[80:83], a[148:151], v[0:3]\n\t"
      "v_mfma_f32_16x16x32_bf16 v[0:3], a[88:91], a[152:155], v[0:3]\n\t"
      "ds_read_b128 a[112:115], %[lx0] offset:2048\n\t"
      "v_mfma_f32_16x16x32_bf16 v[0:3], a[96:99], a[156:159], v[0:3]\n\t"
      "s_waitcnt lgkmcnt(14)\n\t"
      "v_mfma_f32_16x16x32_bf16 v[0:3], a[104:107], a[160:163], v[0:3]\n\t"
      "s_waitcnt lgkmcnt(12)\n\t"
      "v_mfma_f32_16x16x32_bf16 v[4:7], a[108:111], a[160:163], v[4:7]\n\t"
      "s_waitcnt lgkmcnt(10)\n\t"
      "v_mfma_f32_16x16x32_bf16 v[4:7], a[100:103], a[156:159], v[4:7]\n\t"
      "s_waitcnt lgkmcnt(8)\n\t"
      "s_add_u32 s52, s52, s60\n\ts_addc_u32 s53, s53, 0\n\t"
      "v_mfma_f32_16x16x32_bf16 v[4:7], a[92:95], a[152:155], v[4:7]\n\t"
      "s_waitcnt lgkmcnt(6)\n\t"
      "s_sub_u32 s54, s54, s60\n\t"
      "v_add_u32 %[vo], %[vod], %[vo]\n\t"
      "v_mfma_f32_16x16x32_bf16 v[4:7], a[84:87], a[148:151], v[4:7]\n\t"
      "s_waitcnt lgkmcnt(4)\n\t"
      "s_mov_b32 s79, s78\n\t"
      "ds_read_b128 a[116:119], %[lx0] offset:2304\n\t"
      "v_mfma_f32_16x16x32_bf16 v[4:7], a[76:79], a[144:147], v[4:7]\n\t"
      "v_mfma_f32_16x16x32_bf16 v[12:15], a[76:79], a[148:151], v[12:15]\n\t"
      "v_mfma_f32_16x16x32_bf16 v[12:15], a[84:87], a[152:155], v[12:15]\n\t"
      "v_mfma_f32_16x16x32_bf16 v[12:15], a[92:95], a[156:159], v[12:15]\n\t"
      "v_mfma_f32_16x16x32_bf16 v[12:15], a[100:103], a[160:163], v[12:15]\n\t"
      "s_waitcnt lgkmcnt(4)\n\t"
      "s_add_u32 m0, %[ismx], 43008\n\t"
      "v_mfma_f32_16x16x32_bf16 v[12:15], a[108:111], a[164:167], v[12:15]\n\t"
      "s_add_u32 s67, s76, 49152\n\t"
      "ds_read_b128 a[120:123], %[lx0] offset:2560\n\t"
      "v_mfma_f32_16x16x32_bf16 v[8:11], a[104:107], a[164:167], v[8:11]\n\t"
      "buffer_load_dwordx4 %[vx], s[48:51], s67 offen lds\n\t"
      "v_mfma_f32_16x16x32_bf16 v[8:11], a[96:99], a[160:163], v[8:11]\n\t"
      "v_mfma_f32_16x16x32_bf16 v[8:11], a[88:91], a[156:159], v[8:11]\n\t"
      "v_mfma_f32_16x16x32_bf16 v[8:11], a[80:83], a[152:155], v[8:11]\n\t"
      "v_mul_f32_e64 v224, s84, |v0|\n\t"
      "v_mul_f32_e64 v225, s84, |v1|\n\t"
      "v_mfma_f32_16x16x32_bf16 v[8:11], a[72:75], a[148:151], v[8:11]\n\t"
      "v_exp_f32_e32 v224, v224\n\t"
      "v_mfma_f32_16x16x32_bf16 v[16:19], a[72:75], a[152:155], v[16:19]\n\t"
      "ds_read_b128 a[124:127], %[lx0] offset:2816\n\t"
      "v_mul_f32_e64 v226, s84, |v2|\n\t"
      "v_mfma_f32_16x16x32_bf16 v[16:19], a[80:83], a[156:159], v[16:19]\n\t"
      "v_exp_f32_e32 v225, v225\n\t"
      "v_mfma_f32_16x16x32_bf16 v[16:19], a[88:91], a[160:163], v[16:19]\n\t"
      "v_exp_f32_e32 v226, v226\n\t"
      "v_mfma_f32_16x16x32_bf16 v[16:19], a[96:99], a[164:167], v[16:19]\n\t"
      "s_waitcnt lgkmcnt(5)\n\t"
      "v_mul_f32_e64 v227, s84, |v3|\n\t"
      "v_mul_f32_e32 v4, s86, v4\n\t"
      "v_mfma_f32_16x16x32_bf16 v[16:19], a[104:107], a[168:171], v[16:19]\n\t"
      "v_exp_f32_e32 v227, v227\n\t"
      "v_mfma_f32_16x16x32_bf16 v[20:23], a[108:111], a[168:171], v[20:23]\n\t"
      "v_exp_f32_e32 v4, v4\n\t"
      "v_mfma_f32_16x16x32_bf16 v[20:23], a[100:103], a[164:167], v[20:23]\n\t"
      "v_mul_f32_e32 v5, s86, v5\n\t"
      "v_mul_f32_e32 v6, s86, v6\n\t"
      "v_mfma_f32_16x16x32_bf16 v[20:23], a[92:95], a[160:163], v[20:23]\n\t"
      "v_exp_f32_e32 v5, v5\n\t"
      "v_mfma_f32_16x16x32_bf16 v[20:23], a[84:87], a[156:159], v[20:23]\n\t"
      "v_exp_f32_e32 v6, v6\n\t"
      "v_mfma_f32_16x16x32_bf16 v[20:23], a[76:79], a[152:155], v[20:23]\n\t"
      "v_mul_f32_e32 v7, s86, v7\n\t"
      "v_fma_f32 v224, v224, -0.5, -0.5\n\t"
      "v_mfma_f32_16x16x32_bf16 v[28:31], a[76:79], a[156:159], v[28:31]\n\t"
      "v_exp_f32_e32 v7, v7\n\t"
      "v_mfma_f32_16x16x32_bf16 v[28:31], a[84:87], a[160:163], v[28:31]\n\t"
      "v_fma_f32 v225, v225, -0.5, -0.5\n\t"
      "v_fma_f32 v226, v226, -0.5, -0.5\n\t"
      "v_mfma_f32_16x16x32_bf16 v[28:31], a[92:95], a[164:167], v[28:31]\n\t"
      "v_fma_f32 v227, v227, -0.5, -0.5\n\t"
      "v_fma_f32 v4, v224, v4, v224\n\t"
      "v_mfma_f32_16x16x32_bf16 v[28:31], a[100:103], a[168:171], v[28:31]\n\t"
      "s_waitcnt lgkmcnt(4)\n\t"
      "v_rcp_f32_e32 v4, v4\n\t"
      "v_mfma_f32_16x16x32_bf16 v[28:31], a[108:111], a[172:175], v[28:31]\n\t"
      "s_add_u32 m0, %[ismx], 7168\n\t"
      "v_fma_f32 v5, v225, v5, v225\n\t"
      "v_fma_f32 v6, v226, v6, v226\n\t"
      "v_mfma_f32_16x16x32_bf16 v[24:27], a[104:107], a[172:175], v[24:27]\n\t"
      "s_add_u32 s67, s71, 57344\n\t"
      "v_rcp_f32_e32 v5, v5\n\t"
      "v_mfma_f32_16x16x32_bf16 v[24:27], a[96:99], a[168:171], v[24:27]\n\t"
      "buffer_load_dwordx4 %[vx], s[48:51], s67 offen lds\n\t"
      "v_fma_f32 v7, v227, v7, v227\n\t"
      "v_mfma_f32_16x16x32_bf16 v[24:27], a[88:91], a[164:167], v[24:27]\n\t"
      "v_rcp_f32_e32 v6, v6\n\t"
      "v_mfma_f32_16x16x32_bf16 v[24:27], a[80:83], a[160:163], v[24:27]\n\t"
      "v_rcp_f32_e32 v7, v7\n\t"
      "v_mfma_f32_16x16x32_bf16 v[24:27], a[72:75], a[156:159], v[24:27]\n\t"
      "v_bfi_b32 v0, s75, v4, v0\n\t"
      "v_bfi_b32 v1, s75, v5, v1\n\t"
      "v_mfma_f32_16x16x32_bf16 v[32:35], a[32:35], a[128:131], v[32:35]\n\t"
      "v_bfi_b32 v2, s75, v6, v2\n\t"
      "v_bfi_b32 v3, s75, v7, v3\n\t"
      "v_mfma_f32_16x16x32_bf16 v[32:35], a[40:43], a[132:135], v[32:35]\n\t"
      "v_fmac_f32_e32 v0, v224, v0\n\t"
      "v_fmac_f32_e32 v1, v225, v1\n\t"
      "v_mfma_f32_16x16x32_bf16 v[32:35], a[48:51], a[136:139], v[32:35]\n\t"
      "ds_read_b128 a[144:147], %[lx1] offset:2048\n\t"
      "v_fmac_f32_e32 v2, v226, v2\n\t"
      "v_mfma_f32_16x16x32_bf16 v[32:35], a[56:59], a[140:143], v[32:35]\n\t"
      "s_waitcnt lgkmcnt(4)\n\t"
      "v_fmac_f32_e32 v3, v227, v3\n\t"
      "v_cvt_pk_bf16_f32 v4, v0, v1\n\t"
      "v_mfma_f32_16x16x32_bf16 v[32:35], a[64:67], a[112:115], v[32:35]\n\t"
      "s_mov_b32 s68, 0\n\t"
      "v_cvt_pk_bf16_f32 v5, v2, v3\n\t"
      "v_mul_f32_e64 v228, s84, |v8|\n\t"
      "v_mfma_f32_16x16x32_bf16 v[36:39], a[68:71], a[112:115], v[36:39]\n\t"
      "buffer_store_dwordx2 v[4:5], %[vo], s[52:55], s68 offen offset:0 nt\n\t"
      "v_mfma_f32_16x16x32_bf16 v[36:39], a[60:63], a[140:143], v[36:39]\n\t"
      "v_exp_f32_e32 v228, v228\n\t"
      "v_mfma_f32_16x16x32_bf16 v[36:39], a[52:55], a[136:139], v[36:39]\n\t"
      "v_mul_f32_e64 v229, s84, |v9|\n\t"
      "v_mul_f32_e64 v230, s84, |v10|\n\t"
      "v_mfma_f32_16x16x32_bf16 v[36:39], a[44:47], a[132:135], v[36:39]\n\t"
      "ds_read_b128 a[148:151], %[lx1] offset:2304\n\t"
      "v_mul_f32_e64 v231, s84, |v11|\n\t"
      "v_mfma_f32_16x16x32_bf16 v[36:39], a[36:39], a[128:131], v[36:39]\n\t"
      "v_exp_f32_e32 v229, v229\n\t"
      "v_mfma_f32_16x16x32_bf16 v[44:47], a[36:39], a[132:135], v[44:47]\n\t"
      "v_exp_f32_e32 v230, v230\n\t"
      "v_mfma_f32_16x16x32_bf16 v[44:47], a[44:47], a[136:139], v[44:47]\n\t"
      "v_exp_f32_e32 v231, v231\n\t"
      "v_mfma_f32_16x16x32_bf16 v[44:47], a[52:55], a[140:143], v[44:47]\n\t"
      "v_mul_f32_e32 v12, s86, v12\n\t"
      "v_mul_f32_e32 v13, s86, v13\n\t"
      "v_mfma_f32_16x16x32_bf16 v[44:47], a[60:63], a[112:115], v[44:47]\n\t"
      "s_waitcnt lgkmcnt(4)\n\t"
      "v_exp_f32_e32 v12, v12\n\t"
      "v_mfma_f32_16x16x32_bf16 v[44:47], a[68:71], a[116:119], v[44:47]\n\t"
      "ds_read_b128 a[152:155], %[lx1] offset:2560\n\t"
      "v_mul_f32_e32 v14, s86, v14\n\t"
      "v_mfma_f32_16x16x32_bf16 v[40:43], a[64:67], a[116:119], v[40:43]\n\t"
      "s_add_u32 m0, %[ismx], 44032\n\t"
      "v_exp_f32_e32 v13, v13\n\t"
      "v_mfma_f32_16x16x32_bf16 v[40:43], a[56:59], a[112:115], v[40:43]\n\t"
      "s_add_u32 s67, s76, 57344\n\t"
      "v_exp_f32_e32 v14, v14\n\t"
      "v_mfma_f32_16x16x32_bf16 v[40:43], a[48:51], a[140:143], v[40:43]\n\t"
      "buffer_load_dwordx4 %[vx], s[48:51], s67 offen lds\n\t"
      "v_mul_f32_e32 v15, s86, v15\n\t"
      "v_mfma_f32_16x16x32_bf16 v[40:43], a[40:43], a[136:139], v[40:43]\n\t"
      "v_exp_f32_e32 v15, v15\n\t"
      "v_mfma_f32_16x16x32_bf16 v[40:43], a[32:35], a[132:135], v[40:43]\n\t"
      "v_fma_f32 v228, v228, -0.5, -0.5\n\t"
      "v_fma_f32 v229, v229, -0.5, -0.5\n\t"
      "v_mfma_f32_16x16x32_bf16 v[48:51], a[32:35], a[136:139], v[48:51]\n\t"
      "ds_read_b128 a[156:159], %[lx1] offset:2816\n\t"
      "v_fma_f32 v230, v230, -0.5, -0.5\n\t"
      "v_mfma_f32_16x16x32_bf16 v[48:51], a[40:43], a[140:143], v[48:51]\n\t"
      "v_fma_f32 v231, v231, -0.5, -0.5\n\t"
      "v_fma_f32 v12, v228, v12, v228\n\t"
      "v_mfma_f32_16x16x32_bf16 v[48:51], a[48:51], a[112:115], v[48:51]\n\t"
      "v_rcp_f32_e32 v12, v12\n\t"
      "v_mfma_f32_16x16x32_bf16 v[48:51], a[56:59], a[116:119], v[48:51]\n\t"
      "s_waitcnt lgkmcnt(5)\n\t"
      "v_fma_f32 v13, v229, v13, v229\n\t"
      "v_fma_f32 v14, v230, v14, v230\n\t"
      "v_mfma_f32_16x16x32_bf16 v[48:51], a[64:67], a[120:123], v[48:51]\n\t"
      "v_rcp_f32_e32 v13, v13\n\t"
      "v_mfma_f32_16x16x32_bf16 v[52:55], a[68:71], a[120:123], v[52:55]\n\t"
      "v_rcp_f32_e32 v14, v14\n\t"
      "v_mfma_f32_16x16x32_bf16 v[52:55], a[60:63], a[116:119], v[52:55]\n\t"
      "v_fma_f32 v15, v231, v15, v231\n\t"
      "v_bfi_b32 v8, s75, v12, v8\n\t"
      "v_mfma_f32_16x16x32_bf16 v[52:55], a[52:55], a[112:115], v[52:55]\n\t"
      "v_rcp_f32_e32 v15, v15\n\t"
      "v_mfma_f32_16x16x32_bf16 v[52:55], a[44:47], a[140:143], v[52:55]\n\t"
      "v_bfi_b32 v9, s75, v13, v9\n\t"
      "v_bfi_b32 v10, s75, v14, v10\n\t"
      "v_mfma_f32_16x16x32_bf16 v[52:55], a[36:39], a[136:139], v[52:55]\n\t"
      "v_bfi_b32 v11, s75, v15, v11\n\t"
      "v_fmac_f32_e32 v8, v228, v8\n\t"
      "v_mfma_f32_16x16x32_bf16 v[60:63], a[36:39], a[140:143], v[60:63]\n\t"
      "v_fmac_f32_e32 v9, v229, v9\n\t"
      "v_fmac_f32_e32 v10, v230, v10\n\t"
      "v_mfma_f32_16x16x32_bf16 v[60:63], a[44:47], a[112:115], v[60:63]\n\t"
      "v_fmac_f32_e32 v11, v231, v11\n\t"
      "v_cvt_pk_bf16_f32 v12, v8, v9\n\t"
      "v_mfma_f32_16x16x32_bf16 v[60:63], a[52:55], a[116:119], v[60:63]\n\t"
      "s_mov_b32 s68, 0\n\t"
      "v_cvt_pk_bf16_f32 v13, v10, v11\n\t"
      "v_mul_f32_e64 v224, s84, |v16|\n\t"
      "v_mfma_f32_16x16x32_bf16 v[60:63], a[60:63], a[120:123], v[60:63]\n\t"
      "s_waitcnt lgkmcnt(4)\n\t"
      "buffer_store_dwordx2 v[12:13], %[vo], s[52:55], s68 offen offset:2048 nt\n\t"
      "v_mfma_f32_16x16x32_bf16 v[60:63], a[68:71], a[124:127], v[60:63]\n\t"
      "v_exp_f32_e32 v224, v224\n\t"
      "v_mfma_f32_16x16x32_bf16 v[56:59], a[64:67], a[124:127], v[56:59]\n\t"
      "v_mul_f32_e64 v225, s84, |v17|\n\t"
      "v_mul_f32_e64 v226, s84, |v18|\n\t"
      "v_mfma_f32_16x16x32_bf16 v[56:59], a[56:59], a[120:123], v[56:59]\n\t"
      "v_exp_f32_e32 v225, v225\n\t"
      "v_mfma_f32_16x16x32_bf16 v[56:59], a[48:51], a[116:119], v[56:59]\n\t"
      "v_exp_f32_e32 v226, v226\n\t"
      "v_mfma_f32_16x16x32_bf16 v[56:59], a[40:43], a[112:115], v[56:59]\n\t"
      "v_mul_f32_e64 v227, s84, |v19|\n\t"
      "v_mul_f32_e32 v20, s86, v20\n\t"
      "v_mfma_f32_16x16x32_bf16 v[56:59], a[32:35], a[140:143], v[56:59]\n\t"
      "s_waitcnt vmcnt(6) lgkmcnt(0)\n\t"
      "s_barrier\n\t"
      "v_mul_f32_e32 v21, s86, v21\n\t"
      "v_mfma_f32_16x16x32_bf16 v[32:35], a[72:75], a[160:163], v[32:35]\n\t"
      "v_exp_f32_e32 v227, v227\n\t"
      "v_mfma_f32_16x16x32_bf16 v[32:35], a[80:83], a[164:167], v[32:35]\n\t"
      "s_add_u32 m0, %[ismx], 8192\n\t"
      "v_exp_f32_e32 v20, v20\n\t"
      "v_mfma_f32_16x16x32_bf16 v[32:35], a[88:91], a[168:171], v[32:35]\n\t"
      "s_add_u32 s67, s71, 65536\n\t"
      "ds_read_b128 a[128:131], %[lx0] offset:3072\n\t"
      "v_mul_f32_e32 v22, s86, v22\n\t"
      "v_mfma_f32_16x16x32_bf16 v[32:35], a[96:99], a[172:175], v[32:35]\n\t"
      "buffer_load_dwordx4 %[vx], s[48:51], s67 offen lds\n\t"
      "v_mul_f32_e32 v23, s86, v23\n\t"
      "v_mfma_f32_16x16x32_bf16 v[32:35], a[104:107], a[144:147], v[32:35]\n\t"
      "v_exp_f32_e32 v21, v21\n\t"
      "v_mfma_f32_16x16x32_bf16 v[36:39], a[108:111], a[144:147], v[36:39]\n\t"
      "v_exp_f32_e32 v22, v22\n\t"
      "v_mfma_f32_16x16x32_bf16 v[36:39], a[100:103], a[172:175], v[36:39]\n\t"
      "v_exp_f32_e32 v23, v23\n\t"
      "v_mfma_f32_16x16x32_bf16 v[36:39], a[92:95], a[168:171], v[36:39]\n\t"
      "v_fma_f32 v224, v224, -0.5, -0.5\n\t"
      "v_fma_f32 v225, v225, -0.5, -0.5\n\t"
      "v_mfma_f32_16x16x32_bf16 v[36:39], a[84:87], a[164:167], v[36:39]\n\t"
      "ds_read_b128 a[132:135], %[lx0] offset:3328\n\t"
      "v_fma_f32 v226, v226, -0.5, -0.5\n\t"
      "v_mfma_f32_16x16x32_bf16 v[36:39], a[76:79], a[160:163], v[36:39]\n\t"
      "v_fma_f32 v227, v227, -0.5, -0.5\n\t"
      "v_fma_f32 v20, v224, v20, v224\n\t"
      "v_mfma_f32_16x16x32_bf16 v[44:47], a[76:79], a[164:167], v[44:47]\n\t"
      "v_rcp_f32_e32 v20, v20\n\t"
      "v_mfma_f32_16x16x32_bf16 v[44:47], a[84:87], a[168:171], v[44:47]\n\t"
      "v_fma_f32 v21, v225, v21, v225\n\t"
      "v_fma_f32 v22, v226, v22, v226\n\t"
      "v_mfma_f32_16x16x32_bf16 v[44:47], a[92:95], a[172:175], v[44:47]\n\t"
      "v_rcp_f32_e32 v21, v21\n\t"
      "v_mfma_f32_16x16x32_bf16 v[44:47], a[100:103], a[144:147], v[44:47]\n\t"
      "s_add_u32 m0, %[ismx], 45056\n\t"
      "v_rcp_f32_e32 v22, v22\n\t"
      "v_mfma_f32_16x16x32_bf16 v[44:47], a[108:111], a[148:151], v[44:47]\n\t"
      "s_add_u32 s67, s76, 65536\n\t"
      "ds_read_b128 a[136:139], %[lx0] offset:3584\n\t"
      "v_fma_f32 v23, v227, v23, v227\n\t"
      "v_mfma_f32_16x16x32_bf16 v[40:43], a[104:107], a[148:151], v[40:43]\n\t"
      "buffer_load_dwordx4 %[vx], s[48:51], s67 offen lds\n\t"
      "v_bfi_b32 v16, s75, v20, v16\n\t"
      "v_mfma_f32_16x16x32_bf16 v[40:43], a[96:99], a[144:147], v[40:43]\n\t"
      "v_rcp_f32_e32 v23, v23\n\t"
      "v_mfma_f32_16x16x32_bf16 v[40:43], a[88:91], a[172:175], v[40:43]\n\t"
      "v_bfi_b32 v17, s75, v21, v17\n\t"
      "v_bfi_b32 v18, s75, v22, v18\n\t"
      "v_mfma_f32_16x16x32_bf16 v[40:43], a[80:83], a[168:171], v[40:43]\n\t"
      "v_bfi_b32 v19, s75, v23, v19\n\t"
      "v_fmac_f32_e32 v16, v224, v16\n\t"
      "v_mfma_f32_16x16x32_bf16 v[40:43], a[72:75], a[164:167], v[40:43]\n\t"
      "v_fmac_f32_e32 v17, v225, v17\n\t"
      "v_fmac_f32_e32 v18, v226, v18\n\t"
      "v_mfma_f32_16x16x32_bf16 v[48:51], a[72:75], a[168:171], v[48:51]\n\t"
      "ds_read_b128 a[140:143], %[lx0] offset:3840\n\t"
      "v_fmac_f32_e32 v19, v227, v19\n\t"
      "v_mfma_f32_16x16x32_bf16 v[48:51], a[80:83], a[172:175], v[48:51]\n\t"
      "v_cvt_pk_bf16_f32 v20, v16, v17\n\t"
      "v_cvt_pk_bf16_f32 v21, v18, v19\n\t"
      "v_mfma_f32_16x16x32_bf16 v[48:51], a[88:91], a[144:147], v[48:51]\n\t"
      "s_mov_b32 s68, 4096\n\t"
      "v_mul_f32_e64 v228, s84, |v24|\n\t"
      "v_mul_f32_e64 v229, s84, |v25|\n\t"
      "v_mfma_f32_16x16x32_bf16 v[48:51], a[96:99], a[148:151], v[48:51]\n\t"
      "buffer_store_dwordx2 v[20:21], %[vo], s[52:55], s68 offen offset:0 nt\n\t"
      "v_mfma_f32_16x16x32_bf16 v[48:51], a[104:107], a[152:155], v[48:51]\n\t"
      "v_exp_f32_e32 v228, v228\n\t"
      "v_mfma_f32_16x16x32_bf16 v[52:55], a[108:111], a[152:155], v[52:55]\n\t"
      "v_exp_f32_e32 v229, v229\n\t"
      "v_mfma_f32_16x16x32_bf16 v[52:55], a[100:103], a[148:151], v[52:55]\n\t"
      "v_mul_f32_e64 v230, s84, |v26|\n\t"
      "v_mul_f32_e64 v231, s84, |v27|\n\t"
      "v_mfma_f32_16x16x32_bf16 v[52:55], a[92:95], a[144:147], v[52:55]\n\t"
      "v_exp_f32_e32 v230, v230\n\t"
      "v_mfma_f32_16x16x32_bf16 v[52:55], a[84:87], a[172:175], v[52:55]\n\t"
      "v_exp_f32_e32 v231, v231\n\t"
      "v_mfma_f32_16x16x32_bf16 v[52:55], a[76:79], a[168:171], v[52:55]\n\t"
      "v_mul_f32_e32 v28, s86, v28\n\t"
      "v_mul_f32_e32 v29, s86, v29\n\t"
      "v_mfma_f32_16x16x32_bf16 v[60:63], a[76:79], a[172:175], v[60:63]\n\t"
      "v_exp_f32_e32 v28, v28\n\t"
      "v_mfma_f32_16x16x32_bf16 v[60:63], a[84:87], a[144:147], v[60:63]\n\t"
      "v_exp_f32_e32 v29, v29\n\t"
      "v_mfma_f32_16x16x32_bf16 v[60:63], a[92:95], a[148:151], v[60:63]\n\t"
      "v_mul_f32_e32 v30, s86, v30\n\t"
      "v_mul_f32_e32 v31, s86, v31\n\t"
      "v_mfma_f32_16x16x32_bf16 v[60:63], a[100:103], a[152:155], v[60:63]\n\t"
      "v_exp_f32_e32 v30, v30\n\t"
      "v_mfma_f32_16x16x32_bf16 v[60:63], a[108:111], a[156:159], v[60:63]\n\t"
      "v_exp_f32_e32 v31, v31\n\t"
      "v_mfma_f32_16x16x32_bf16 v[56:59], a[104:107], a[156:159], v[56:59]\n\t"
      "v_fma_f32 v228, v228, -0.5, -0.5\n\t"
      "v_fma_f32 v229, v229, -0.5, -0.5\n\t"
      "v_mfma_f32_16x16x32_bf16 v[56:59], a[96:99], a[152:155], v[56:59]\n\t"
      "v_fma_f32 v230, v230, -0.5, -0.5\n\t"
      "v_fma_f32 v231, v231, -0.5, -0.5\n\t"
      "v_mfma_f32_16x16x32_bf16 v[56:59], a[88:91], a[148:151], v[56:59]\n\t"
      "v_fma_f32 v28, v228, v28, v228\n\t"
      "v_fma_f32 v29, v229, v29, v229\n\t"
      "v_mfma_f32_16x16x32_bf16 v[56:59], a[80:83], a[144:147], v[56:59]\n\t"
      "v_rcp_f32_e32 v28, v28\n\t"
      "v_mfma_f32_16x16x32_bf16 v[56:59], a[72:75], a[172:175], v[56:59]\n\t"
      "v_rcp_f32_e32 v29, v29\n\t"
      "v_mfma_f32_16x16x32_bf16 v[64:67], a[32:35], a[112:115], v[64:67]\n\t"
      "v_fma_f32 v30, v230, v30, v230\n\t"
      "v_fma_f32 v31, v231, v31, v231\n\t"
      "v_mfma_f32_16x16x32_bf16 v[64:67], a[40:43], a[116:119], v[64:67]\n\t"
      "v_rcp_f32_e32 v30, v30\n\t"
      "v_mfma_f32_16x16x32_bf16 v[64:67], a[48:51], a[120:123], v[64:67]\n\t"
      "ds_read_b128 a[160:163], %[lx1] offset:3072\n\t"
      "v_bfi_b32 v24, s75, v28, v24\n\t"
      "v_mfma_f32_16x16x32_bf16 v[64:67], a[56:59], a[124:127], v[64:67]\n\t"
      "s_waitcnt lgkmcnt(4)\n\t"
      "v_rcp_f32_e32 v31, v31\n\t"
      "v_mfma_f32_16x16x32_bf16 v[64:67], a[64:67], a[128:131], v[64:67]\n\t"
      "v_bfi_b32 v25, s75, v29, v25\n\t"
      "v_bfi_b32 v26, s75, v30, v26\n\t"
      "v_mfma_f32_16x16x32_bf16 v[68:71], a[68:71], a[128:131], v[68:71]\n\t"
      "v_bfi_b32 v27, s75, v31, v27\n\t"
      "v_fmac_f32_e32 v24, v228, v24\n\t"
      "v_mfma_f32_16x16x32_bf16 v[68:71], a[60:63], a[124:127], v[68:71]\n\t"
      "v_fmac_f32_e32 v25, v229, v25\n\t"
      "v_fmac_f32_e32 v26, v230, v26\n\t"
      "v_mfma_f32_16x16x32_bf16 v[68:71], a[52:55], a[120:123], v[68:71]\n\t"
      "v_fmac_f32_e32 v27, v231, v27\n\t"
      "v_cvt_pk_bf16_f32 v28, v24, v25\n\t"
      "v_mfma_f32_16x16x32_bf16 v[68:71], a[44:47], a[116:119], v[68:71]\n\t"
      "ds_read_b128 a[164:167], %[lx1] offset:3328\n\t"
      "v_cvt_pk_bf16_f32 v29, v26, v27\n\t"
      "v_mfma_f32_16x16x32_bf16 v[68:71], a[36:39], a[112:115], v[68:71]\n\t"
      "s_mov_b32 s68, 4096\n\t"
      "v_mul_f32_e64 v224, s84, |v32|\n\t"
      "v_mul_f32_e64 v225, s84, |v33|\n\t"
      "v_mfma_f32_16x16x32_bf16 v[76:79], a[36:39], a[116:119], v[76:79]\n\t"
      "buffer_store_dwordx2 v[28:29], %[vo], s[52:55], s68 offen offset:2048 nt\n\t"
      "v_mfma_f32_16x16x32_bf16 v[76:79], a[44:47], a[120:123], v[76:79]\n\t"
      "v_exp_f32_e32 v224, v224\n\t"
      "v_mfma_f32_16x16x32_bf16 v[76:79], a[52:55], a[124:127], v[76:79]\n\t"
      "v_exp_f32_e32 v225, v225\n\t"
      "v_mfma_f32_16x16x32_bf16 v[76:79], a[60:63], a[128:131], v[76:79]\n\t"
      "s_waitcnt lgkmcnt(4)\n\t"
      "v_mul_f32_e64 v226, s84, |v34|\n\t"
      "v_mul_f32_e64 v227, s84, |v35|\n\t"
      "v_mfma_f32_16x16x32_bf16 v[76:79], a[68:71], a[132:135], v[76:79]\n\t"
      "ds_read_b128 a[168:171], %[lx1] offset:3584\n\t"
      "v_mul_f32_e32 v36, s86, v36\n\t"
      "v_mfma_f32_16x16x32_bf16 v[72:75], a[64:67], a[132:135], v[72:75]\n\t"
      "v_exp_f32_e32 v226, v226\n\t"
      "v_mfma_f32_16x16x32_bf16 v[72:75], a[56:59], a[128:131], v[72:75]\n\t"
      "v_exp_f32_e32 v227, v227\n\t"
      "v_mfma_f32_16x16x32_bf16 v[72:75], a[48:51], a[124:127], v[72:75]\n\t"
      "v_exp_f32_e32 v36, v36\n\t"
      "v_mfma_f32_16x16x32_bf16 v[72:75], a[40:43], a[120:123], v[72:75]\n\t"
      "v_mul_f32_e32 v37, s86, v37\n\t"
      "v_mul_f32_e32 v38, s86, v38\n\t"
      "v_mfma_f32_16x16x32_bf16 v[72:75], a[32:35], a[116:119], v[72:75]\n\t"
      "v_exp_f32_e32 v37, v37\n\t"
      "v_mfma_f32_16x16x32_bf16 v[80:83], a[32:35], a[120:123], v[80:83]\n\t"
      "ds_read_b128 a[172:175], %[lx1] offset:3840\n\t"
      "v_mul_f32_e32 v39, s86, v39\n\t"
      "v_mfma_f32_16x16x32_bf16 v[80:83], a[40:43], a[124:127], v[80:83]\n\t"
      "v_exp_f32_e32 v38, v38\n\t"
      "v_mfma_f32_16x16x32_bf16 v[80:83], a[48:51], a[128:131], v[80:83]\n\t"
      "v_exp_f32_e32 v39, v39\n\t"
      "v_mfma_f32_16x16x32_bf16 v[80:83], a[56:59], a[132:135], v[80:83]\n\t"
      "s_waitcnt lgkmcnt(5)\n\t"
      "v_fma_f32 v224, v224, -0.5, -0.5\n\t"
      "v_fma_f32 v225, v225, -0.5, -0.5\n\t"
      "v_mfma_f32_16x16x32_bf16 v[80:83], a[64:67], a[136:139], v[80:83]\n\t"
      "v_fma_f32 v226, v226, -0.5, -0.5\n\t"
      "v_fma_f32 v227, v227, -0.5, -0.5\n\t"
      "v_mfma_f32_16x16x32_bf16 v[84:87], a[68:71], a[136:139], v[84:87]\n\t"
      "v_fma_f32 v36, v224, v36, v224\n\t"
      "v_fma_f32 v37, v225, v37, v225\n\t"
      "v_mfma_f32_16x16x32_bf16 v[84:87], a[60:63], a[132:135], v[84:87]\n\t"
      "v_rcp_f32_e32 v36, v36\n\t"
      "v_mfma_f32_16x16x32_bf16 v[84:87], a[52:55], a[128:131], v[84:87]\n\t"
      "v_rcp_f32_e32 v37, v37\n\t"
      "v_mfma_f32_16x16x32_bf16 v[84:87], a[44:47], a[124:127], v[84:87]\n\t"
      "v_fma_f32 v38, v226, v38, v226\n\t"
      "v_fma_f32 v39, v227, v39, v227\n\t"
      "v_mfma_f32_16x16x32_bf16 v[84:87], a[36:39], a[120:123], v[84:87]\n\t"
      "v_rcp_f32_e32 v38, v38\n\t"
      "v_mfma_f32_16x16x32_bf16 v[92:95], a[36:39], a[124:127], v[92:95]\n\t"
      "v_rcp_f32_e32 v39, v39\n\t"
      "v_mfma_f32_16x16x32_bf16 v[92:95], a[44:47], a[128:131], v[92:95]\n\t"
      "v_bfi_b32 v32, s75, v36, v32\n\t"
      "v_bfi_b32 v33, s75, v37, v33\n\t"
      "v_mfma_f32_16x16x32_bf16 v[92:95], a[52:55], a[132:135], v[92:95]\n\t"
      "v_bfi_b32 v34, s75, v38, v34\n\t"
      "v_bfi_b32 v35, s75, v39, v35\n\t"
      "v_mfma_f32_16x16x32_bf16 v[92:95], a[60:63], a[136:139], v[92:95]\n\t"
      "s_waitcnt lgkmcnt(4)\n\t"
      "v_fmac_f32_e32 v32, v224, v32\n\t"
      "v_fmac_f32_e32 v33, v225, v33\n\t"
      "v_mfma_f32_16x16x32_bf16 v[92:95], a[68:71], a[140:143], v[92:95]\n\t"
      "v_fmac_f32_e32 v34, v226, v34\n\t"
      "v_fmac_f32_e32 v35, v227, v35\n\t"
      "v_mfma_f32_16x16x32_bf16 v[88:91], a[64:67], a[140:143], v[88:91]\n\t"
      "v_cvt_pk_bf16_f32 v36, v32, v33\n\t"
      "v_cvt_pk_bf16_f32 v37, v34, v35\n\t"
      "v_mfma_f32_16x16x32_bf16 v[88:91], a[56:59], a[136:139], v[88:91]\n\t"
      "s_mov_b32 s68, 8192\n\t"
      "v_mul_f32_e64 v228, s84, |v40|\n\t"
      "v_mul_f32_e64 v229, s84, |v41|\n\t"
      "v_mfma_f32_16x16x32_bf16 v[88:91], a[48:51], a[132:135], v[88:91]\n\t"
      "buffer_store_dwordx2 v[36:37], %[vo], s[52:55], s68 offen offset:0 nt\n\t"
      "v_mfma_f32_16x16x32_bf16 v[88:91], a[40:43], a[128:131], v[88:91]\n\t"
      "v_exp_f32_e32 v228, v228\n\t"
      "v_mfma_f32_16x16x32_bf16 v[88:91], a[32:35], a[124:127], v[88:91]\n\t"
      "v_exp_f32_e32 v229, v229\n\t"
      "v_mfma_f32_16x16x32_bf16 v[64:67], a[72:75], a[144:147], v[64:67]\n\t"
      "v_mul_f32_e64 v230, s84, |v42|\n\t"
      "v_mul_f32_e64 v231, s84, |v43|\n\t"
      "v_mfma_f32_16x16x32_bf16 v[64:67], a[80:83], a[148:151], v[64:67]\n\t"
      "v_exp_f32_e32 v230, v230\n\t"
      "v_mfma_f32_16x16x32_bf16 v[64:67], a[88:91], a[152:155], v[64:67]\n\t"
      "ds_read_b128 a[112:115], %[lx0] offset:4096\n\t"
      "v_mul_f32_e32 v44, s86, v44\n\t"
      "v_mfma_f32_16x16x32_bf16 v[64:67], a[96:99], a[156:159], v[64:67]\n\t"
      "s_waitcnt lgkmcnt(4)\n\t"
      "v_exp_f32_e32 v231, v231\n\t"
      "v_mfma_f32_16x16x32_bf16 v[64:67], a[104:107], a[160:163], v[64:67]\n\t"
      "v_exp_f32_e32 v44, v44\n\t"
      "v_mfma_f32_16x16x32_bf16 v[68:71], a[108:111], a[160:163], v[68:71]\n\t"
      "v_mul_f32_e32 v45, s86, v45\n\t"
      "v_mul_f32_e32 v46, s86, v46\n\t"
      "v_mfma_f32_16x16x32_bf16 v[68:71], a[100:103], a[156:159], v[68:71]\n\t"
      "v_exp_f32_e32 v45, v45\n\t"
      "v_mfma_f32_16x16x32_bf16 v[68:71], a[92:95], a[152:155], v[68:71]\n\t"
      "v_exp_f32_e32 v46, v46\n\t"
      "v_mfma_f32_16x16x32_bf16 v[68:71], a[84:87], a[148:151], v[68:71]\n\t"
      "ds_read_b128 a[116:119], %[lx0] offset:4352\n\t"
      "v_mul_f32_e32 v47, s86, v47\n\t"
      "v_mfma_f32_16x16x32_bf16 v[68:71], a[76:79], a[144:147], v[68:71]\n\t"
      "v_exp_f32_e32 v47, v47\n\t"
      "v_mfma_f32_16x16x32_bf16 v[76:79], a[76:79], a[148:151], v[76:79]\n\t"
      "v_fma_f32 v228, v228, -0.5, -0.5\n\t"
      "v_fma_f32 v229, v229, -0.5, -0.5\n\t"
      "v_mfma_f32_16x16x32_bf16 v[76:79], a[84:87], a[152:155], v[76:79]\n\t"
      "v_fma_f32 v230, v230, -0.5, -0.5\n\t"
      "v_fma_f32 v231, v231, -0.5, -0.5\n\t"
      "v_mfma_f32_16x16x32_bf16 v[76:79], a[92:95], a[156:159], v[76:79]\n\t"
      "v_fma_f32 v44, v228, v44, v228\n\t"
      "v_fma_f32 v45, v229, v45, v229\n\t"
      "v_mfma_f32_16x16x32_bf16 v[76:79], a[100:103], a[160:163], v[76:79]\n\t"
      "s_waitcnt lgkmcnt(4)\n\t"
      "v_rcp_f32_e32 v44, v44\n\t"
      "v_mfma_f32_16x16x32_bf16 v[76:79], a[108:111], a[164:167], v[76:79]\n\t"
      "ds_read_b128 a[120:123], %[lx0] offset:4608\n\t"
      "v_fma_f32 v46, v230, v46, v230\n\t"
      "v_mfma_f32_16x16x32_bf16 v[72:75], a[104:107], a[164:167], v[72:75]\n\t"
      "v_rcp_f32_e32 v45, v45\n\t"
      "v_mfma_f32_16x16x32_bf16 v[72:75], a[96:99], a[160:163], v[72:75]\n\t"
      "v_rcp_f32_e32 v46, v46\n\t"
      "v_mfma_f32_16x16x32_bf16 v[72:75], a[88:91], a[156:159], v[72:75]\n\t"
      "v_fma_f32 v47, v231, v47, v231\n\t"
      "v_bfi_b32 v40, s75, v44, v40\n\t"
      "v_mfma_f32_16x16x32_bf16 v[72:75], a[80:83], a[152:155], v[72:75]\n\t"
      "v_rcp_f32_e32 v47, v47\n\t"
      "v_mfma_f32_16x16x32_bf16 v[72:75], a[72:75], a[148:151], v[72:75]\n\t"
      "v_bfi_b32 v41, s75, v45, v41\n\t"
      "v_bfi_b32 v42, s75, v46, v42\n\t"
      "v_mfma_f32_16x16x32_bf16 v[80:83], a[72:75], a[152:155], v[80:83]\n\t"
      "ds_read_b128 a[124:127], %[lx0] offset:4864\n\t"
      "v_bfi_b32 v43, s75, v47, v43\n\t"
      "v_mfma_f32_16x16x32_bf16 v[80:83], a[80:83], a[156:159], v[80:83]\n\t"
      "v_fmac_f32_e32 v40, v228, v40\n\t"
      "v_fmac_f32_e32 v41, v229, v41\n\t"
      "v_mfma_f32_16x16x32_bf16 v[80:83], a[88:91], a[160:163], v[80:83]\n\t"
      "v_fmac_f32_e32 v42, v230, v42\n\t"
      "v_fmac_f32_e32 v43, v231, v43\n\t"
      "v_mfma_f32_16x16x32_bf16 v[80:83], a[96:99], a[164:167], v[80:83]\n\t"
      "s_waitcnt lgkmcnt(5)\n\t"
      "v_cvt_pk_bf16_f32 v44, v40, v41\n\t"
      "v_cvt_pk_bf16_f32 v45, v42, v43\n\t"
      "v_mfma_f32_16x16x32_bf16 v[80:83], a[104:107], a[168:171], v[80:83]\n\t"
      "s_mov_b32 s68, 8192\n\t"
      "v_mul_f32_e64 v224, s84, |v48|\n\t"
      "v_mul_f32_e64 v225, s84, |v49|\n\t"
      "v_mfma_f32_16x16x32_bf16 v[84:87], a[108:111], a[168:171], v[84:87]\n\t"
      "buffer_store_dwordx2 v[44:45], %[vo], s[52:55], s68 offen offset:2048 nt\n\t"
      "v_mfma_f32_16x16x32_bf16 v[84:87], a[100:103], a[164:167], v[84:87]\n\t"
      "v_exp_f32_e32 v224, v224\n\t"
      "v_mfma_f32_16x16x32_bf16 v[84:87], a[92:95], a[160:163], v[84:87]\n\t"
      "v_exp_f32_e32 v225, v225\n\t"
      "v_mfma_f32_16x16x32_bf16 v[84:87], a[84:87], a[156:159], v[84:87]\n\t"
      "v_mul_f32_e64 v226, s84, |v50|\n\t"
      "v_mul_f32_e64 v227, s84, |v51|\n\t"
      "v_mfma_f32_16x16x32_bf16 v[84:87], a[76:79], a[152:155], v[84:87]\n\t"
      "v_exp_f32_e32 v226, v226\n\t"
      "v_mfma_f32_16x16x32_bf16 v[92:95], a[76:79], a[156:159], v[92:95]\n\t"
      "v_exp_f32_e32 v227, v227\n\t"
      "v_mfma_f32_16x16x32_bf16 v[92:95], a[84:87], a[160:163], v[92:95]\n\t"
      "v_mul_f32_e32 v52, s86, v52\n\t"
      "v_mul_f32_e32 v53, s86, v53\n\t"
      "v_mfma_f32_16x16x32_bf16 v[92:95], a[92:95], a[164:167], v[92:95]\n\t"
      "v_exp_f32_e32 v52, v52\n\t"
      "v_mfma_f32_16x16x32_bf16 v[92:95], a[100:103], a[168:171], v[92:95]\n\t"
      "s_waitcnt lgkmcnt(4)\n\t"
      "v_exp_f32_e32 v53, v53\n\t"
      "v_mfma_f32_16x16x32_bf16 v[92:95], a[108:111], a[172:175], v[92:95]\n\t"
      "v_mul_f32_e32 v54, s86, v54\n\t"
      "v_mul_f32_e32 v55, s86, v55\n\t"
      "v_mfma_f32_16x16x32_bf16 v[88:91], a[104:107], a[172:175], v[88:91]\n\t"
      "v_exp_f32_e32 v54, v54\n\t"
      "v_mfma_f32_16x16x32_bf16 v[88:91], a[96:99], a[168:171], v[88:91]\n\t"
      "v_exp_f32_e32 v55, v55\n\t"
      "v_mfma_f32_16x16x32_bf16 v[88:91], a[88:91], a[164:167], v[88:91]\n\t"
      "v_fma_f32 v224, v224, -0.5, -0.5\n\t"
      "v_fma_f32 v225, v225, -0.5, -0.5\n\t"
      "v_mfma_f32_16x16x32_bf16 v[88:91], a[80:83], a[160:163], v[88:91]\n\t"
      "v_fma_f32 v226, v226, -0.5, -0.5\n\t"
      "v_fma_f32 v227, v227, -0.5, -0.5\n\t"
      "v_mfma_f32_16x16x32_bf16 v[88:91], a[72:75], a[156:159], v[88:91]\n\t"
      "v_fma_f32 v52, v224, v52, v224\n\t"
      "v_fma_f32 v53, v225, v53, v225\n\t"
      "v_mfma_f32_16x16x32_bf16 v[96:99], a[32:35], a[128:131], v[96:99]\n\t"
      "v_rcp_f32_e32 v52, v52\n\t"
      "v_mfma_f32_16x16x32_bf16 v[96:99], a[40:43], a[132:135], v[96:99]\n\t"
      "v_rcp_f32_e32 v53, v53\n\t"
      "v_mfma_f32_16x16x32_bf16 v[96:99], a[48:51], a[136:139], v[96:99]\n\t"
      "ds_read_b128 a[144:147], %[lx1] offset:4096\n\t"
      "v_fma_f32 v54, v226, v54, v226\n\t"
      "v_mfma_f32_16x16x32_bf16 v[96:99], a[56:59], a[140:143], v[96:99]\n\t"
      "s_waitcnt lgkmcnt(4)\n\t"
      "v_rcp_f32_e32 v54, v54\n\t"
      "v_mfma_f32_16x16x32_bf16 v[96:99], a[64:67], a[112:115], v[96:99]\n\t"
      "v_fma_f32 v55, v227, v55, v227\n\t"
      "v_bfi_b32 v48, s75, v52, v48\n\t"
      "v_mfma_f32_16x16x32_bf16 v[100:103], a[68:71], a[112:115], v[100:103]\n\t"
      "v_rcp_f32_e32 v55, v55\n\t"
      "v_mfma_f32_16x16x32_bf16 v[100:103], a[60:63], a[140:143], v[100:103]\n\t"
      "v_bfi_b32 v49, s75, v53, v49\n\t"
      "v_bfi_b32 v50, s75, v54, v50\n\t"
      "v_mfma_f32_16x16x32_bf16 v[100:103], a[52:55], a[136:139], v[100:103]\n\t"
      "v_bfi_b32 v51, s75, v55, v51\n\t"
      "v_fmac_f32_e32 v48, v224, v48\n\t"
      "v_mfma_f32_16x16x32_bf16 v[100:103], a[44:47], a[132:135], v[100:103]\n\t"
      "ds_read_b128 a[148:151], %[lx1] offset:4352\n\t"
      "v_fmac_f32_e32 v49, v225, v49\n\t"
      "v_mfma_f32_16x16x32_bf16 v[100:103], a[36:39], a[128:131], v[100:103]\n\t"
      "v_fmac_f32_e32 v50, v226, v50\n\t"
      "v_fmac_f32_e32 v51, v227, v51\n\t"
      "v_mfma_f32_16x16x32_bf16 v[108:111], a[36:39], a[132:135], v[108:111]\n\t"
      "v_cvt_pk_bf16_f32 v52, v48, v49\n\t"
      "v_cvt_pk_bf16_f32 v53, v50, v51\n\t"
      "v_mfma_f32_16x16x32_bf16 v[108:111], a[44:47], a[136:139], v[108:111]\n\t"
      "s_mov_b32 s68, 12288\n\t"
      "v_mul_f32_e64 v228, s84, |v56|\n\t"
      "v_mul_f32_e64 v229, s84, |v57|\n\t"
      "v_mfma_f32_16x16x32_bf16 v[108:111], a[52:55], a[140:143], v[108:111]\n\t"
      "buffer_store_dwordx2 v[52:53], %[vo], s[52:55], s68 offen offset:0 nt\n\t"
      "v_mfma_f32_16x16x32_bf16 v[108:111], a[60:63], a[112:115], v[108:111]\n\t"
      "s_waitcnt lgkmcnt(4)\n\t"
      "v_exp_f32_e32 v228, v228\n\t"
      "v_mfma_f32_16x16x32_bf16 v[108:111], a[68:71], a[116:119], v[108:111]\n\t"
      "ds_read_b128 a[152:155], %[lx1] offset:4608\n\t"
      "v_mul_f32_e64 v230, s84, |v58|\n\t"
      "v_mfma_f32_16x16x32_bf16 v[104:107], a[64:67], a[116:119], v[104:107]\n\t"
      "v_exp_f32_e32 v229, v229\n\t"
      "v_mfma_f32_16x16x32_bf16 v[104:107], a[56:59], a[112:115], v[104:107]\n\t"
      "v_exp_f32_e32 v230, v230\n\t"
      "v_mfma_f32_16x16x32_bf16 v[104:107], a[48:51], a[140:143], v[104:107]\n\t"
      "v_mul_f32_e64 v231, s84, |v59|\n\t"
      "v_mul_f32_e32 v60, s86, v60\n\t"
      "v_mfma_f32_16x16x32_bf16 v[104:107], a[40:43], a[136:139], v[104:107]\n\t"
      "v_exp_f32_e32 v231, v231\n\t"
      "v_mfma_f32_16x16x32_bf16 v[104:107], a[32:35], a[132:135], v[104:107]\n\t"
      "v_exp_f32_e32 v60, v60\n\t"
      "v_mfma_f32_16x16x32_bf16 v[112:115], a[32:35], a[136:139], v[112:115]\n\t"
      "ds_read_b128 a[156:159], %[lx1] offset:4864\n\t"
      "v_mul_f32_e32 v61, s86, v61\n\t"
      "v_mfma_f32_16x16x32_bf16 v[112:115], a[40:43], a[140:143], v[112:115]\n\t"
      "v_exp_f32_e32 v61, v61\n\t"
      "v_mfma_f32_16x16x32_bf16 v[112:115], a[48:51], a[112:115], v[112:115]\n\t"
      "v_mul_f32_e32 v62, s86, v62\n\t"
      "v_mul_f32_e32 v63, s86, v63\n\t"
      "v_mfma_f32_16x16x32_bf16 v[112:115], a[56:59], a[116:119], v[112:115]\n\t"
      "s_waitcnt lgkmcnt(5)\n\t"
      "v_exp_f32_e32 v62, v62\n\t"
      "v_mfma_f32_16x16x32_bf16 v[112:115], a[64:67], a[120:123], v[112:115]\n\t"
      "v_exp_f32_e32 v63, v63\n\t"
      "v_mfma_f32_16x16x32_bf16 v[116:119], a[68:71], a[120:123], v[116:119]\n\t"
      "v_fma_f32 v228, v228, -0.5, -0.5\n\t"
      "v_fma_f32 v229, v229, -0.5, -0.5\n\t"
      "v_mfma_f32_16x16x32_bf16 v[116:119], a[60:63], a[116:119], v[116:119]\n\t"
      "v_fma_f32 v230, v230, -0.5, -0.5\n\t"
      "v_fma_f32 v231, v231, -0.5, -0.5\n\t"
      "v_mfma_f32_16x16x32_bf16 v[116:119], a[52:55], a[112:115], v[116:119]\n\t"
      "v_fma_f32 v60, v228, v60, v228\n\t"
      "v_fma_f32 v61, v229, v61, v229\n\t"
      "v_mfma_f32_16x16x32_bf16 v[116:119], a[44:47], a[140:143], v[116:119]\n\t"
      "v_rcp_f32_e32 v60, v60\n\t"
      "v_mfma_f32_16x16x32_bf16 v[116:119], a[36:39], a[136:139], v[116:119]\n\t"
      "v_rcp_f32_e32 v61, v61\n\t"
      "v_mfma_f32_16x16x32_bf16 v[124:127], a[36:39], a[140:143], v[124:127]\n\t"
      "v_fma_f32 v62, v230, v62, v230\n\t"
      "v_fma_f32 v63, v231, v63, v231\n\t"
      "v_mfma_f32_16x16x32_bf16 v[124:127], a[44:47], a[112:115], v[124:127]\n\t"
      "v_rcp_f32_e32 v62, v62\n\t"
      "v_mfma_f32_16x16x32_bf16 v[124:127], a[52:55], a[116:119], v[124:127]\n\t"
      "v_rcp_f32_e32 v63, v63\n\t"
      "v_mfma_f32_16x16x32_bf16 v[124:127], a[60:63], a[120:123], v[124:127]\n\t"
      "s_waitcnt lgkmcnt(4)\n\t"
      "v_bfi_b32 v56, s75, v60, v56\n\t"
      "v_bfi_b32 v57, s75, v61, v57\n\t"
      "v_mfma_f32_16x16x32_bf16 v[124:127], a[68:71], a[124:127], v[124:127]\n\t"
      "v_bfi_b32 v58, s75, v62, v58\n\t"
      "v_bfi_b32 v59, s75, v63, v59\n\t"
      "v_mfma_f32_16x16x32_bf16 v[120:123], a[64:67], a[124:127], v[120:123]\n\t"
      "v_fmac_f32_e32 v56, v228, v56\n\t"
      "v_fmac_f32_e32 v57, v229, v57\n\t"
      "v_mfma_f32_16x16x32_bf16 v[120:123], a[56:59], a[120:123], v[120:123]\n\t"
      "v_fmac_f32_e32 v58, v230, v58\n\t"
      "v_fmac_f32_e32 v59, v231, v59\n\t"
      "v_mfma_f32_16x16x32_bf16 v[120:123], a[48:51], a[116:119], v[120:123]\n\t"
      "v_cvt_pk_bf16_f32 v60, v56, v57\n\t"
      "v_cvt_pk_bf16_f32 v61, v58, v59\n\t"
      "v_mfma_f32_16x16x32_bf16 v[120:123], a[40:43], a[112:115], v[120:123]\n\t"
      "s_mov_b32 s68, 12288\n\t"
      "v_mul_f32_e64 v224, s84, |v64|\n\t"
      "v_mul_f32_e64 v225, s84, |v65|\n\t"
      "v_mfma_f32_16x16x32_bf16 v[120:123], a[32:35], a[140:143], v[120:123]\n\t"
      "buffer_store_dwordx2 v[60:61], %[vo], s[52:55], s68 offen offset:2048 nt\n\t"
      "v_mfma_f32_16x16x32_bf16 v[96:99], a[72:75], a[160:163], v[96:99]\n\t"
      "v_exp_f32_e32 v224, v224\n\t"
      "v_mfma_f32_16x16x32_bf16 v[96:99], a[80:83], a[164:167], v[96:99]\n\t"
      "v_exp_f32_e32 v225, v225\n\t"
      "v_mfma_f32_16x16x32_bf16 v[96:99], a[88:91], a[168:171], v[96:99]\n\t"
      "ds_read_b128 a[128:131], %[lx0] offset:5120\n\t"
      "v_mul_f32_e64 v226, s84, |v66|\n\t"
      "v_mfma_f32_16x16x32_bf16 v[96:99], a[96:99], a[172:175], v[96:99]\n\t"
      "s_waitcnt lgkmcnt(4)\n\t"
      "v_exp_f32_e32 v226, v226\n\t"
      "v_mfma_f32_16x16x32_bf16 v[96:99], a[104:107], a[144:147], v[96:99]\n\t"
      "v_mul_f32_e64 v227, s84, |v67|\n\t"
      "v_mul_f32_e32 v68, s86, v68\n\t"
      "v_mfma_f32_16x16x32_bf16 v[100:103], a[108:111], a[144:147], v[100:103]\n\t"
      "v_exp_f32_e32 v227, v227\n\t"
      "v_mfma_f32_16x16x32_bf16 v[100:103], a[100:103], a[172:175], v[100:103]\n\t"
      "v_exp_f32_e32 v68, v68\n\t"
      "v_mfma_f32_16x16x32_bf16 v[100:103], a[92:95], a[168:171], v[100:103]\n\t"
      "v_mul_f32_e32 v69, s86, v69\n\t"
      "v_mul_f32_e32 v70, s86, v70\n\t"
      "v_mfma_f32_16x16x32_bf16 v[100:103], a[84:87], a[164:167], v[100:103]\n\t"
      "ds_read_b128 a[132:135], %[lx0] offset:5376\n\t"
      "v_mul_f32_e32 v71, s86, v71\n\t"
      "v_mfma_f32_16x16x32_bf16 v[100:103], a[76:79], a[160:163], v[100:103]\n\t"
      "v_exp_f32_e32 v69, v69\n\t"
      "v_mfma_f32_16x16x32_bf16 v[108:111], a[76:79], a[164:167], v[108:111]\n\t"
      "v_exp_f32_e32 v70, v70\n\t"
      "v_mfma_f32_16x16x32_bf16 v[108:111], a[84:87], a[168:171], v[108:111]\n\t"
      "v_exp_f32_e32 v71, v71\n\t"
      "v_mfma_f32_16x16x32_bf16 v[108:111], a[92:95], a[172:175], v[108:111]\n\t"
      "v_fma_f32 v224, v224, -0.5, -0.5\n\t"
      "v_fma_f32 v225, v225, -0.5, -0.5\n\t"
      "v_mfma_f32_16x16x32_bf16 v[108:111], a[100:103], a[144:147], v[108:111]\n\t"
      "s_waitcnt lgkmcnt(4)\n\t"
      "v_fma_f32 v226, v226, -0.5, -0.5\n\t"
      "v_fma_f32 v227, v227, -0.5, -0.5\n\t"
      "v_mfma_f32_16x16x32_bf16 v[108:111], a[108:111], a[148:151], v[108:111]\n\t"
      "ds_read_b128 a[136:139], %[lx0] offset:5632\n\t"
      "v_fma_f32 v68, v224, v68, v224\n\t"
      "v_mfma_f32_16x16x32_bf16 v[104:107], a[104:107], a[148:151], v[104:107]\n\t"
      "v_rcp_f32_e32 v68, v68\n\t"
      "v_mfma_f32_16x16x32_bf16 v[104:107], a[96:99], a[144:147], v[104:107]\n\t"
      "v_fma_f32 v69, v225, v69, v225\n\t"
      "v_fma_f32 v70, v226, v70, v226\n\t"
      "v_mfma_f32_16x16x32_bf16 v[104:107], a[88:91], a[172:175], v[104:107]\n\t"
      "v_rcp_f32_e32 v69, v69\n\t"
      "v_mfma_f32_16x16x32_bf16 v[104:107], a[80:83], a[168:171], v[104:107]\n\t"
      "v_rcp_f32_e32 v70, v70\n\t"
      "v_mfma_f32_16x16x32_bf16 v[104:107], a[72:75], a[164:167], v[104:107]\n\t"
      "v_fma_f32 v71, v227, v71, v227\n\t"
      "v_bfi_b32 v64, s75, v68, v64\n\t"
      "v_mfma_f32_16x16x32_bf16 v[112:115], a[72:75], a[168:171], v[112:115]\n\t"
      "ds_read_b128 a[140:143], %[lx0] offset:5888\n\t"
      "v_bfi_b32 v65, s75, v69, v65\n\t"
      "v_mfma_f32_16x16x32_bf16 v[112:115], a[80:83], a[172:175], v[112:115]\n\t"
      "v_rcp_f32_e32 v71, v71\n\t"
      "v_mfma_f32_16x16x32_bf16 v[112:115], a[88:91], a[144:147], v[112:115]\n\t"
      "v_bfi_b32 v66, s75, v70, v66\n\t"
      "v_bfi_b32 v67, s75, v71, v67\n\t"
      "v_mfma_f32_16x16x32_bf16 v[112:115], a[96:99], a[148:151], v[112:115]\n\t"
      "s_waitcnt lgkmcnt(5)\n\t"
      "v_fmac_f32_e32 v64, v224, v64\n\t"
      "v_fmac_f32_e32 v65, v225, v65\n\t"
      "v_mfma_f32_16x16x32_bf16 v[112:115], a[104:107], a[152:155], v[112:115]\n\t"
      "v_fmac_f32_e32 v66, v226, v66\n\t"
      "v_fmac_f32_e32 v67, v227, v67\n\t"
      "v_mfma_f32_16x16x32_bf16 v[116:119], a[108:111], a[152:155], v[116:119]\n\t"
      "v_cvt_pk_bf16_f32 v68, v64, v65\n\t"
      "v_cvt_pk_bf16_f32 v69, v66, v67\n\t"
      "v_mfma_f32_16x16x32_bf16 v[116:119], a[100:103], a[148:151], v[116:119]\n\t"
      "s_mov_b32 s68, 16384\n\t"
      "v_mul_f32_e64 v228, s84, |v72|\n\t"
      "v_mul_f32_e64 v229, s84, |v73|\n\t"
      "v_mfma_f32_16x16x32_bf16 v[116:119], a[92:95], a[144:147], v[116:119]\n\t"
      "buffer_store_dwordx2 v[68:69], %[vo], s[52:55], s68 offen offset:0 nt\n\t"
      "v_mfma_f32_16x16x32_bf16 v[116:119], a[84:87], a[172:175], v[116:119]\n\t"
      "v_exp_f32_e32 v228, v228\n\t"
      "v_mfma_f32_16x16x32_bf16 v[116:119], a[76:79], a[168:171], v[116:119]\n\t"
      "v_exp_f32_e32 v229, v229\n\t"
      "v_mfma_f32_16x16x32_bf16 v[124:127], a[76:79], a[172:175], v[124:127]\n\t"
      "v_mul_f32_e64 v230, s84, |v74|\n\t"
      "v_mul_f32_e64 v231, s84, |v75|\n\t"
      "v_mfma_f32_16x16x32_bf16 v[124:127], a[84:87], a[144:147], v[124:127]\n\t"
      "v_exp_f32_e32 v230, v230\n\t"
      "v_mfma_f32_16x16x32_bf16 v[124:127], a[92:95], a[148:151], v[124:127]\n\t"
      "v_exp_f32_e32 v231, v231\n\t"
      "v_mfma_f32_16x16x32_bf16 v[124:127], a[100:103], a[152:155], v[124:127]\n\t"
      "s_waitcnt lgkmcnt(4)\n\t"
      "v_mul_f32_e32 v76, s86, v76\n\t"
      "v_mul_f32_e32 v77, s86, v77\n\t"
      "v_mfma_f32_16x16x32_bf16 v[124:127], a[108:111], a[156:159], v[124:127]\n\t"
      "v_exp_f32_e32 v76, v76\n\t"
      "v_mfma_f32_16x16x32_bf16 v[120:123], a[104:107], a[156:159], v[120:123]\n\t"
      "v_exp_f32_e32 v77, v77\n\t"
      "v_mfma_f32_16x16x32_bf16 v[120:123], a[96:99], a[152:155], v[120:123]\n\t"
      "v_mul_f32_e32 v78, s86, v78\n\t"
      "v_mul_f32_e32 v79, s86, v79\n\t"
      "v_mfma_f32_16x16x32_bf16 v[120:123], a[88:91], a[148:151], v[120:123]\n\t"
      "v_exp_f32_e32 v78, v78\n\t"
      "v_mfma_f32_16x16x32_bf16 v[120:123], a[80:83], a[144:147], v[120:123]\n\t"
      "v_exp_f32_e32 v79, v79\n\t"
      "v_mfma_f32_16x16x32_bf16 v[120:123], a[72:75], a[172:175], v[120:123]\n\t"
      "v_fma_f32 v228, v228, -0.5, -0.5\n\t"
      "v_fma_f32 v229, v229, -0.5, -0.5\n\t"
      "v_mfma_f32_16x16x32_bf16 v[128:131], a[32:35], a[112:115], v[128:131]\n\t"
      "v_fma_f32 v230, v230, -0.5, -0.5\n\t"
      "v_fma_f32 v231, v231, -0.5, -0.5\n\t"
      "v_mfma_f32_16x16x32_bf16 v[128:131], a[40:43], a[116:119], v[128:131]\n\t"
      "v_fma_f32 v76, v228, v76, v228\n\t"
      "v_fma_f32 v77, v229, v77, v229\n\t"
      "v_mfma_f32_16x16x32_bf16 v[128:131], a[48:51], a[120:123], v[128:131]\n\t"
      "ds_read_b128 a[160:163], %[lx1] offset:5120\n\t"
      "v_fma_f32 v78, v230, v78, v230\n\t"
      "v_mfma_f32_16x16x32_bf16 v[128:131], a[56:59], a[124:127], v[128:131]\n\t"
      "s_waitcnt lgkmcnt(4)\n\t"
      "v_rcp_f32_e32 v76, v76\n\t"
      "v_mfma_f32_16x16x32_bf16 v[128:131], a[64:67], a[128:131], v[128:131]\n\t"
      "v_rcp_f32_e32 v77, v77\n\t"
      "v_mfma_f32_16x16x32_bf16 v[132:135], a[68:71], a[128:131], v[132:135]\n\t"
      "v_rcp_f32_e32 v78, v78\n\t"
      "v_mfma_f32_16x16x32_bf16 v[132:135], a[60:63], a[124:127], v[132:135]\n\t"
      "v_fma_f32 v79, v231, v79, v231\n\t"
      "v_bfi_b32 v72, s75, v76, v72\n\t"
      "v_mfma_f32_16x16x32_bf16 v[132:135], a[52:55], a[120:123], v[132:135]\n\t"
      "v_rcp_f32_e32 v79, v79\n\t"
      "v_mfma_f32_16x16x32_bf16 v[132:135], a[44:47], a[116:119], v[132:135]\n\t"
      "ds_read_b128 a[164:167], %[lx1] offset:5376\n\t"
      "v_bfi_b32 v73, s75, v77, v73\n\t"
      "v_mfma_f32_16x16x32_bf16 v[132:135], a[36:39], a[112:115], v[132:135]\n\t"
      "v_bfi_b32 v74, s75, v78, v74\n\t"
      "v_bfi_b32 v75, s75, v79, v75\n\t"
      "v_mfma_f32_16x16x32_bf16 v[140:143], a[36:39], a[116:119], v[140:143]\n\t"
      "v_fmac_f32_e32 v72, v228, v72\n\t"
      "v_fmac_f32_e32 v73, v229, v73\n\t"
      "v_mfma_f32_16x16x32_bf16 v[140:143], a[44:47], a[120:123], v[140:143]\n\t"
      "v_fmac_f32_e32 v74, v230, v74\n\t"
      "v_fmac_f32_e32 v75, v231, v75\n\t"
      "v_mfma_f32_16x16x32_bf16 v[140:143], a[52:55], a[124:127], v[140:143]\n\t"
      "v_cvt_pk_bf16_f32 v76, v72, v73\n\t"
      "v_cvt_pk_bf16_f32 v77, v74, v75\n\t"
      "v_mfma_f32_16x16x32_bf16 v[140:143], a[60:63], a[128:131], v[140:143]\n\t"
      "s_waitcnt lgkmcnt(4)\n\t"
      "v_mul_f32_e64 v224, s84, |v80|\n\t"
      "v_mul_f32_e64 v225, s84, |v81|\n\t"
      "v_mfma_f32_16x16x32_bf16 v[140:143], a[68:71], a[132:135], v[140:143]\n\t"
      "ds_read_b128 a[168:171], %[lx1] offset:5632\n\t"
      "v_mul_f32_e64 v226, s84, |v82|\n\t"
      "v_mfma_f32_16x16x32_bf16 v[136:139], a[64:67], a[132:135], v[136:139]\n\t"
      "s_mov_b32 s68, 16384\n\t"
      "v_exp_f32_e32 v224, v224\n\t"
      "v_mfma_f32_16x16x32_bf16 v[136:139], a[56:59], a[128:131], v[136:139]\n\t"
      "buffer_store_dwordx2 v[76:77], %[vo], s[52:55], s68 offen offset:2048 nt\n\t"
      "v_mfma_f32_16x16x32_bf16 v[136:139], a[48:51], a[124:127], v[136:139]\n\t"
      "v_exp_f32_e32 v225, v225\n\t"
      "v_mfma_f32_16x16x32_bf16 v[136:139], a[40:43], a[120:123], v[136:139]\n\t"
      "v_exp_f32_e32 v226, v226\n\t"
      "v_mfma_f32_16x16x32_bf16 v[136:139], a[32:35], a[116:119], v[136:139]\n\t"
      "v_mul_f32_e64 v227, s84, |v83|\n\t"
      "v_mul_f32_e32 v84, s86, v84\n\t"
      "v_mfma_f32_16x16x32_bf16 v[144:147], a[32:35], a[120:123], v[144:147]\n\t"
      "ds_read_b128 a[172:175], %[lx1] offset:5888\n\t"
      "v_mul_f32_e32 v85, s86, v85\n\t"
      "v_mfma_f32_16x16x32_bf16 v[144:147], a[40:43], a[124:127], v[144:147]\n\t"
      "v_exp_f32_e32 v227, v227\n\t"
      "v_mfma_f32_16x16x32_bf16 v[144:147], a[48:51], a[128:131], v[144:147]\n\t"
      "v_exp_f32_e32 v84, v84\n\t"
      "v_mfma_f32_16x16x32_bf16 v[144:147], a[56:59], a[132:135], v[144:147]\n\t"
      "s_waitcnt lgkmcnt(5)\n\t"
      "v_exp_f32_e32 v85, v85\n\t"
      "v_mfma_f32_16x16x32_bf16 v[144:147], a[64:67], a[136:139], v[144:147]\n\t"
      "v_mul_f32_e32 v86, s86, v86\n\t"
      "v_mul_f32_e32 v87, s86, v87\n\t"
      "v_mfma_f32_16x16x32_bf16 v[148:151], a[68:71], a[136:139], v[148:151]\n\t"
      "v_exp_f32_e32 v86, v86\n\t"
      "v_mfma_f32_16x16x32_bf16 v[148:151], a[60:63], a[132:135], v[148:151]\n\t"
      "v_exp_f32_e32 v87, v87\n\t"
      "v_mfma_f32_16x16x32_bf16 v[148:151], a[52:55], a[128:131], v[148:151]\n\t"
      "v_fma_f32 v224, v224, -0.5, -0.5\n\t"
      "v_fma_f32 v225, v225, -0.5, -0.5\n\t"
      "v_mfma_f32_16x16x32_bf16 v[148:151], a[44:47], a[124:127], v[148:151]\n\t"
      "v_fma_f32 v226, v226, -0.5, -0.5\n\t"
      "v_fma_f32 v227, v227, -0.5, -0.5\n\t"
      "v_mfma_f32_16x16x32_bf16 v[148:151], a[36:39], a[120:123], v[148:151]\n\t"
      "v_fma_f32 v84, v224, v84, v224\n\t"
      "v_fma_f32 v85, v225, v85, v225\n\t"
      "v_mfma_f32_16x16x32_bf16 v[156:159], a[36:39], a[124:127], v[156:159]\n\t"
      "v_rcp_f32_e32 v84, v84\n\t"
      "v_mfma_f32_16x16x32_bf16 v[156:159], a[44:47], a[128:131], v[156:159]\n\t"
      "v_rcp_f32_e32 v85, v85\n\t"
      "v_mfma_f32_16x16x32_bf16 v[156:159], a[52:55], a[132:135], v[156:159]\n\t"
      "v_fma_f32 v86, v226, v86, v226\n\t"
      "v_fma_f32 v87, v227, v87, v227\n\t"
      "v_mfma_f32_16x16x32_bf16 v[156:159], a[60:63], a[136:139], v[156:159]\n\t"
      "s_waitcnt lgkmcnt(4)\n\t"
      "v_rcp_f32_e32 v86, v86\n\t"
      "v_mfma_f32_16x16x32_bf16 v[156:159], a[68:71], a[140:143], v[156:159]\n\t"
      "v_rcp_f32_e32 v87, v87\n\t"
      "v_mfma_f32_16x16x32_bf16 v[152:155], a[64:67], a[140:143], v[152:155]\n\t"
      "v_bfi_b32 v80, s75, v84, v80\n\t"
      "v_bfi_b32 v81, s75, v85, v81\n\t"
      "v_mfma_f32_16x16x32_bf16 v[152:155], a[56:59], a[136:139], v[152:155]\n\t"
      "v_bfi_b32 v82, s75, v86, v82\n\t"
      "v_bfi_b32 v83, s75, v87, v83\n\t"
      "v_mfma_f32_16x16x32_bf16 v[152:155], a[48:51], a[132:135], v[152:155]\n\t"
      "v_fmac_f32_e32 v80, v224, v80\n\t"
      "v_fmac_f32_e32 v81, v225, v81\n\t"
      "v_mfma_f32_16x16x32_bf16 v[152:155], a[40:43], a[128:131], v[152:155]\n\t"
      "v_fmac_f32_e32 v82, v226, v82\n\t"
      "v_fmac_f32_e32 v83, v227, v83\n\t"
      "v_mfma_f32_16x16x32_bf16 v[152:155], a[32:35], a[124:127], v[152:155]\n\t"
      "s_waitcnt vmcnt(8) lgkmcnt(0)\n\t"
      "s_barrier\n\t"
      "v_cvt_pk_bf16_f32 v84, v80, v81\n\t"
      "v_mfma_f32_16x16x32_bf16 v[128:131], a[72:75], a[144:147], v[128:131]\n\t"
      "s_mov_b32 s68, 20480\n\t"
      "v_cvt_pk_bf16_f32 v85, v82, v83\n\t"
      "v_mul_f32_e64 v228, s84, |v88|\n\t"
      "v_mfma_f32_16x16x32_bf16 v[128:131], a[80:83], a[148:151], v[128:131]\n\t"
      "buffer_store_dwordx2 v[84:85], %[vo], s[52:55], s68 offen offset:0 nt\n\t"
      "v_mfma_f32_16x16x32_bf16 v[128:131], a[88:91], a[152:155], v[128:131]\n\t"
      "ds_read_b128 a[112:115], %[lx0] offset:6144\n\t"
      "v_mul_f32_e64 v229, s84, |v89|\n\t"
      "v_mfma_f32_16x16x32_bf16 v[128:131], a[96:99], a[156:159], v[128:131]\n\t"
      "v_exp_f32_e32 v228, v228\n\t"
      "v_mfma_f32_16x16x32_bf16 v[128:131], a[104:107], a[160:163], v[128:131]\n\t"
      "v_exp_f32_e32 v229, v229\n\t"
      "v_mfma_f32_16x16x32_bf16 v[132:135], a[108:111], a[160:163], v[132:135]\n\t"
      "v_mul_f32_e64 v230, s84, |v90|\n\t"
      "v_mul_f32_e64 v231, s84, |v91|\n\t"
      "v_mfma_f32_16x16x32_bf16 v[132:135], a[100:103], a[156:159], v[132:135]\n\t"
      "v_exp_f32_e32 v230, v230\n\t"
      "v_mfma_f32_16x16x32_bf16 v[132:135], a[92:95], a[152:155], v[132:135]\n\t"
      "v_exp_f32_e32 v231, v231\n\t"
      "v_mfma_f32_16x16x32_bf16 v[132:135], a[84:87], a[148:151], v[132:135]\n\t"
      "ds_read_b128 a[116:119], %[lx0] offset:6400\n\t"
      "v_mul_f32_e32 v92, s86, v92\n\t"
      "v_mfma_f32_16x16x32_bf16 v[132:135], a[76:79], a[144:147], v[132:135]\n\t"
      "v_exp_f32_e32 v92, v92\n\t"
      "v_mfma_f32_16x16x32_bf16 v[140:143], a[76:79], a[148:151], v[140:143]\n\t"
      "v_mul_f32_e32 v93, s86, v93\n\t"
      "v_mul_f32_e32 v94, s86, v94\n\t"
      "v_mfma_f32_16x16x32_bf16 v[140:143], a[84:87], a[152:155], v[140:143]\n\t"
      "v_exp_f32_e32 v93, v93\n\t"
      "v_mfma_f32_16x16x32_bf16 v[140:143], a[92:95], a[156:159], v[140:143]\n\t"
      "v_exp_f32_e32 v94, v94\n\t"
      "v_mfma_f32_16x16x32_bf16 v[140:143], a[100:103], a[160:163], v[140:143]\n\t"
      "v_mul_f32_e32 v95, s86, v95\n\t"
      "v_fma_f32 v228, v228, -0.5, -0.5\n\t"
      "v_mfma_f32_16x16x32_bf16 v[140:143], a[108:111], a[164:167], v[140:143]\n\t"
      "ds_read_b128 a[120:123], %[lx0] offset:6656\n\t"
      "v_fma_f32 v229, v229, -0.5, -0.5\n\t"
      "v_mfma_f32_16x16x32_bf16 v[136:139], a[104:107], a[164:167], v[136:139]\n\t"
      "v_exp_f32_e32 v95, v95\n\t"
      "v_mfma_f32_16x16x32_bf16 v[136:139], a[96:99], a[160:163], v[136:139]\n\t"
      "v_fma_f32 v230, v230, -0.5, -0.5\n\t"
      "v_fma_f32 v231, v231, -0.5, -0.5\n\t"
      "v_mfma_f32_16x16x32_bf16 v[136:139], a[88:91], a[156:159], v[136:139]\n\t"
      "v_fma_f32 v92, v228, v92, v228\n\t"
      "v_fma_f32 v93, v229, v93, v229\n\t"
      "v_mfma_f32_16x16x32_bf16 v[136:139], a[80:83], a[152:155], v[136:139]\n\t"
      "v_rcp_f32_e32 v92, v92\n\t"
      "v_mfma_f32_16x16x32_bf16 v[136:139], a[72:75], a[148:151], v[136:139]\n\t"
      "v_rcp_f32_e32 v93, v93\n\t"
      "v_mfma_f32_16x16x32_bf16 v[144:147], a[72:75], a[152:155], v[144:147]\n\t"
      "ds_read_b128 a[124:127], %[lx0] offset:6912\n\t"
      "v_fma_f32 v94, v230, v94, v230\n\t"
      "v_mfma_f32_16x16x32_bf16 v[144:147], a[80:83], a[156:159], v[144:147]\n\t"
      "v_rcp_f32_e32 v94, v94\n\t"
      "v_mfma_f32_16x16x32_bf16 v[144:147], a[88:91], a[160:163], v[144:147]\n\t"
      "v_fma_f32 v95, v231, v95, v231\n\t"
      "v_bfi_b32 v88, s75, v92, v88\n\t"
      "v_mfma_f32_16x16x32_bf16 v[144:147], a[96:99], a[164:167], v[144:147]\n\t"
      "v_rcp_f32_e32 v95, v95\n\t"
      "v_mfma_f32_16x16x32_bf16 v[144:147], a[104:107], a[168:171], v[144:147]\n\t"
      "v_bfi_b32 v89, s75, v93, v89\n\t"
      "v_bfi_b32 v90, s75, v94, v90\n\t"
      "v_mfma_f32_16x16x32_bf16 v[148:151], a[108:111], a[168:171], v[148:151]\n\t"
      "v_bfi_b32 v91, s75, v95, v91\n\t"
      "v_fmac_f32_e32 v88, v228, v88\n\t"
      "v_mfma_f32_16x16x32_bf16 v[148:151], a[100:103], a[164:167], v[148:151]\n\t"
      "v_fmac_f32_e32 v89, v229, v89\n\t"
      "v_fmac_f32_e32 v90, v230, v90\n\t"
      "v_mfma_f32_16x16x32_bf16 v[148:151], a[92:95], a[160:163], v[148:151]\n\t"
      "v_fmac_f32_e32 v91, v231, v91\n\t"
      "v_cvt_pk_bf16_f32 v92, v88, v89\n\t"
      "v_mfma_f32_16x16x32_bf16 v[148:151], a[84:87], a[156:159], v[148:151]\n\t"
      "s_mov_b32 s68, 20480\n\t"
      "v_cvt_pk_bf16_f32 v93, v90, v91\n\t"
      "v_mul_f32_e64 v224, s84, |v96|\n\t"
      "v_mfma_f32_16x16x32_bf16 v[148:151], a[76:79], a[152:155], v[148:151]\n\t"
      "buffer_store_dwordx2 v[92:93], %[vo], s[52:55], s68 offen offset:2048 nt\n\t"
      "v_mfma_f32_16x16x32_bf16 v[156:159], a[76:79], a[156:159], v[156:159]\n\t"
      "v_exp_f32_e32 v224, v224\n\t"
      "v_mfma_f32_16x16x32_bf16 v[156:159], a[84:87], a[160:163], v[156:159]\n\t"
      "v_mul_f32_e64 v225, s84, |v97|\n\t"
      "v_mul_f32_e64 v226, s84, |v98|\n\t"
      "v_mfma_f32_16x16x32_bf16 v[156:159], a[92:95], a[164:167], v[156:159]\n\t"
      "v_exp_f32_e32 v225, v225\n\t"
      "v_mfma_f32_16x16x32_bf16 v[156:159], a[100:103], a[168:171], v[156:159]\n\t"
      "v_exp_f32_e32 v226, v226\n\t"
      "v_mfma_f32_16x16x32_bf16 v[156:159], a[108:111], a[172:175], v[156:159]\n\t"
      "v_mul_f32_e64 v227, s84, |v99|\n\t"
      "v_mul_f32_e32 v100, s86, v100\n\t"
      "v_mfma_f32_16x16x32_bf16 v[152:155], a[104:107], a[172:175], v[152:155]\n\t"
      "v_exp_f32_e32 v227, v227\n\t"
      "v_mfma_f32_16x16x32_bf16 v[152:155], a[96:99], a[168:171], v[152:155]\n\t"
      "v_exp_f32_e32 v100, v100\n\t"
      "v_mfma_f32_16x16x32_bf16 v[152:155], a[88:91], a[164:167], v[152:155]\n\t"
      "v_mul_f32_e32 v101, s86, v101\n\t"
      "v_mul_f32_e32 v102, s86, v102\n\t"
      "v_mfma_f32_16x16x32_bf16 v[152:155], a[80:83], a[160:163], v[152:155]\n\t"
      "v_exp_f32_e32 v101, v101\n\t"
      "v_mfma_f32_16x16x32_bf16 v[152:155], a[72:75], a[156:159], v[152:155]\n\t"
      "v_exp_f32_e32 v102, v102\n\t"
      "v_mfma_f32_16x16x32_bf16 v[160:163], a[32:35], a[128:131], v[160:163]\n\t"
      "v_mul_f32_e32 v103, s86, v103\n\t"
      "v_fma_f32 v224, v224, -0.5, -0.5\n\t"
      "v_mfma_f32_16x16x32_bf16 v[160:163], a[40:43], a[132:135], v[160:163]\n\t"
      "v_exp_f32_e32 v103, v103\n\t"
      "v_mfma_f32_16x16x32_bf16 v[160:163], a[48:51], a[136:139], v[160:163]\n\t"
      "ds_read_b128 a[144:147], %[lx1] offset:6144\n\t"
      "v_fma_f32 v225, v225, -0.5, -0.5\n\t"
      "v_mfma_f32_16x16x32_bf16 v[160:163], a[56:59], a[140:143], v[160:163]\n\t"
      "s_waitcnt lgkmcnt(4)\n\t"
      "v_fma_f32 v226, v226, -0.5, -0.5\n\t"
      "v_fma_f32 v227, v227, -0.5, -0.5\n\t"
      "v_mfma_f32_16x16x32_bf16 v[160:163], a[64:67], a[112:115], v[160:163]\n\t"
      "v_fma_f32 v100, v224, v100, v224\n\t"
      "v_fma_f32 v101, v225, v101, v225\n\t"
      "v_mfma_f32_16x16x32_bf16 v[164:167], a[68:71], a[112:115], v[164:167]\n\t"
      "v_rcp_f32_e32 v100, v100\n\t"
      "v_mfma_f32_16x16x32_bf16 v[164:167], a[60:63], a[140:143], v[164:167]\n\t"
      "v_rcp_f32_e32 v101, v101\n\t"
      "v_mfma_f32_16x16x32_bf16 v[164:167], a[52:55], a[136:139], v[164:167]\n\t"
      "v_fma_f32 v102, v226, v102, v226\n\t"
      "v_fma_f32 v103, v227, v103, v227\n\t"
      "v_mfma_f32_16x16x32_bf16 v[164:167], a[44:47], a[132:135], v[164:167]\n\t"
      "ds_read_b128 a[148:151], %[lx1] offset:6400\n\t"
      "v_bfi_b32 v96, s75, v100, v96\n\t"
      "v_mfma_f32_16x16x32_bf16 v[164:167], a[36:39], a[128:131], v[164:167]\n\t"
      "v_rcp_f32_e32 v102, v102\n\t"
      "v_mfma_f32_16x16x32_bf16 v[172:175], a[36:39], a[132:135], v[172:175]\n\t"
      "v_rcp_f32_e32 v103, v103\n\t"
      "v_mfma_f32_16x16x32_bf16 v[172:175], a[44:47], a[136:139], v[172:175]\n\t"
      "v_bfi_b32 v97, s75, v101, v97\n\t"
      "v_bfi_b32 v98, s75, v102, v98\n\t"
      "v_mfma_f32_16x16x32_bf16 v[172:175], a[52:55], a[140:143], v[172:175]\n\t"
      "v_bfi_b32 v99, s75, v103, v99\n\t"
      "v_fmac_f32_e32 v96, v224, v96\n\t"
      "v_mfma_f32_16x16x32_bf16 v[172:175], a[60:63], a[112:115], v[172:175]\n\t"
      "s_waitcnt lgkmcnt(4)\n\t"
      "v_fmac_f32_e32 v97, v225, v97\n\t"
      "v_fmac_f32_e32 v98, v226, v98\n\t"
      "v_mfma_f32_16x16x32_bf16 v[172:175], a[68:71], a[116:119], v[172:175]\n\t"
      "ds_read_b128 a[152:155], %[lx1] offset:6656\n\t"
      "v_fmac_f32_e32 v99, v227, v99\n\t"
      "v_mfma_f32_16x16x32_bf16 v[168:171], a[64:67], a[116:119], v[168:171]\n\t"
      "v_cvt_pk_bf16_f32 v100, v96, v97\n\t"
      "v_cvt_pk_bf16_f32 v101, v98, v99\n\t"
      "v_mfma_f32_16x16x32_bf16 v[168:171], a[56:59], a[112:115], v[168:171]\n\t"
      "s_mov_b32 s68, 24576\n\t"
      "v_mul_f32_e64 v228, s84, |v104|\n\t"
      "v_mul_f32_e64 v229, s84, |v105|\n\t"
      "v_mfma_f32_16x16x32_bf16 v[168:171], a[48:51], a[140:143], v[168:171]\n\t"
      "buffer_store_dwordx2 v[100:101], %[vo], s[52:55], s68 offen offset:0 nt\n\t"
      "v_mfma_f32_16x16x32_bf16 v[168:171], a[40:43], a[136:139], v[168:171]\n\t"
      "v_exp_f32_e32 v228, v228\n\t"
      "v_mfma_f32_16x16x32_bf16 v[168:171], a[32:35], a[132:135], v[168:171]\n\t"
      "v_exp_f32_e32 v229, v229\n\t"
      "v_mfma_f32_16x16x32_bf16 v[176:179], a[32:35], a[136:139], v[176:179]\n\t"
      "ds_read_b128 a[156:159], %[lx1] offset:6912\n\t"
      "v_mul_f32_e64 v230, s84, |v106|\n\t"
      "v_mfma_f32_16x16x32_bf16 v[176:179], a[40:43], a[140:143], v[176:179]\n\t"
      "v_exp_f32_e32 v230, v230\n\t"
      "v_mfma_f32_16x16x32_bf16 v[176:179], a[48:51], a[112:115], v[176:179]\n\t"
      "v_mul_f32_e64 v231, s84, |v107|\n\t"
      "v_mul_f32_e32 v108, s86, v108\n\t"
      "v_mfma_f32_16x16x32_bf16 v[176:179], a[56:59], a[116:119], v[176:179]\n\t"
      "s_waitcnt lgkmcnt(5)\n\t"
      "v_exp_f32_e32 v231, v231\n\t"
      "v_mfma_f32_16x16x32_bf16 v[176:179], a[64:67], a[120:123], v[176:179]\n\t"
      "v_exp_f32_e32 v108, v108\n\t"
      "v_mfma_f32_16x16x32_bf16 v[180:183], a[68:71], a[120:123], v[180:183]\n\t"
      "v_mul_f32_e32 v109, s86, v109\n\t"
      "v_mul_f32_e32 v110, s86, v110\n\t"
      "v_mfma_f32_16x16x32_bf16 v[180:183], a[60:63], a[116:119], v[180:183]\n\t"
      "v_exp_f32_e32 v109, v109\n\t"
      "v_mfma_f32_16x16x32_bf16 v[180:183], a[52:55], a[112:115], v[180:183]\n\t"
      "v_exp_f32_e32 v110, v110\n\t"
      "v_mfma_f32_16x16x32_bf16 v[180:183], a[44:47], a[140:143], v[180:183]\n\t"
      "v_mul_f32_e32 v111, s86, v111\n\t"
      "v_fma_f32 v228, v228, -0.5, -0.5\n\t"
      "v_mfma_f32_16x16x32_bf16 v[180:183], a[36:39], a[136:139], v[180:183]\n\t"
      "v_exp_f32_e32 v111, v111\n\t"
      "v_mfma_f32_16x16x32_bf16 v[188:191], a[36:39], a[140:143], v[188:191]\n\t"
      "v_fma_f32 v229, v229, -0.5, -0.5\n\t"
      "v_fma_f32 v230, v230, -0.5, -0.5\n\t"
      "v_mfma_f32_16x16x32_bf16 v[188:191], a[44:47], a[112:115], v[188:191]\n\t"
      "v_fma_f32 v231, v231, -0.5, -0.5\n\t"
      "v_fma_f32 v108, v228, v108, v228\n\t"
      "v_mfma_f32_16x16x32_bf16 v[188:191], a[52:55], a[116:119], v[188:191]\n\t"
      "v_rcp_f32_e32 v108, v108\n\t"
      "v_mfma_f32_16x16x32_bf16 v[188:191], a[60:63], a[120:123], v[188:191]\n\t"
      "s_waitcnt lgkmcnt(4)\n\t"
      "v_fma_f32 v109, v229, v109, v229\n\t"
      "v_fma_f32 v110, v230, v110, v230\n\t"
      "v_mfma_f32_16x16x32_bf16 v[188:191], a[68:71], a[124:127], v[188:191]\n\t"
      "v_rcp_f32_e32 v109, v109\n\t"
      "v_mfma_f32_16x16x32_bf16 v[184:187], a[64:67], a[124:127], v[184:187]\n\t"
      "v_rcp_f32_e32 v110, v110\n\t"
      "v_mfma_f32_16x16x32_bf16 v[184:187], a[56:59], a[120:123], v[184:187]\n\t"
      "v_fma_f32 v111, v231, v111, v231\n\t"
      "v_bfi_b32 v104, s75, v108, v104\n\t"
      "v_mfma_f32_16x16x32_bf16 v[184:187], a[48:51], a[116:119], v[184:187]\n\t"
      "v_rcp_f32_e32 v111, v111\n\t"
      "v_mfma_f32_16x16x32_bf16 v[184:187], a[40:43], a[112:115], v[184:187]\n\t"
      "v_bfi_b32 v105, s75, v109, v105\n\t"
      "v_bfi_b32 v106, s75, v110, v106\n\t"
      "v_mfma_f32_16x16x32_bf16 v[184:187], a[32:35], a[140:143], v[184:187]\n\t"
      "v_bfi_b32 v107, s75, v111, v107\n\t"
      "v_fmac_f32_e32 v104, v228, v104\n\t"
      "v_mfma_f32_16x16x32_bf16 v[160:163], a[72:75], a[160:163], v[160:163]\n\t"
      "v_fmac_f32_e32 v105, v229, v105\n\t"
      "v_fmac_f32_e32 v106, v230, v106\n\t"
      "v_mfma_f32_16x16x32_bf16 v[160:163], a[80:83], a[164:167], v[160:163]\n\t"
      "v_fmac_f32_e32 v107, v231, v107\n\t"
      "v_cvt_pk_bf16_f32 v108, v104, v105\n\t"
      "v_mfma_f32_16x16x32_bf16 v[160:163], a[88:91], a[168:171], v[160:163]\n\t"
      "ds_read_b128 a[128:131], %[lx0] offset:7168\n\t"
      "v_cvt_pk_bf16_f32 v109, v106, v107\n\t"
      "v_mfma_f32_16x16x32_bf16 v[160:163], a[96:99], a[172:175], v[160:163]\n\t"
      "s_waitcnt lgkmcnt(4)\n\t"
      "v_mul_f32_e64 v224, s84, |v112|\n\t"
      "v_mul_f32_e64 v225, s84, |v113|\n\t"
      "v_mfma_f32_16x16x32_bf16 v[160:163], a[104:107], a[144:147], v[160:163]\n\t"
      "s_mov_b32 s68, 24576\n\t"
      "v_exp_f32_e32 v224, v224\n\t"
      "v_mfma_f32_16x16x32_bf16 v[164:167], a[108:111], a[144:147], v[164:167]\n\t"
      "buffer_store_dwordx2 v[108:109], %[vo], s[52:55], s68 offen offset:2048 nt\n\t"
      "v_mfma_f32_16x16x32_bf16 v[164:167], a[100:103], a[172:175], v[164:167]\n\t"
      "v_exp_f32_e32 v225, v225\n\t"
      "v_mfma_f32_16x16x32_bf16 v[164:167], a[92:95], a[168:171], v[164:167]\n\t"
      "v_mul_f32_e64 v226, s84, |v114|\n\t"
      "v_mul_f32_e64 v227, s84, |v115|\n\t"
      "v_mfma_f32_16x16x32_bf16 v[164:167], a[84:87], a[164:167], v[164:167]\n\t"
      "ds_read_b128 a[132:135], %[lx0] offset:7424\n\t"
      "v_mul_f32_e32 v116, s86, v116\n\t"
      "v_mfma_f32_16x16x32_bf16 v[164:167], a[76:79], a[160:163], v[164:167]\n\t"
      "v_exp_f32_e32 v226, v226\n\t"
      "v_mfma_f32_16x16x32_bf16 v[172:175], a[76:79], a[164:167], v[172:175]\n\t"
      "v_exp_f32_e32 v227, v227\n\t"
      "v_mfma_f32_16x16x32_bf16 v[172:175], a[84:87], a[168:171], v[172:175]\n\t"
      "v_exp_f32_e32 v116, v116\n\t"
      "v_mfma_f32_16x16x32_bf16 v[172:175], a[92:95], a[172:175], v[172:175]\n\t"
      "v_mul_f32_e32 v117, s86, v117\n\t"
      "v_mul_f32_e32 v118, s86, v118\n\t"
      "v_mfma_f32_16x16x32_bf16 v[172:175], a[100:103], a[144:147], v[172:175]\n\t"
      "s_waitcnt lgkmcnt(4)\n\t"
      "v_exp_f32_e32 v117, v117\n\t"
      "v_mfma_f32_16x16x32_bf16 v[172:175], a[108:111], a[148:151], v[172:175]\n\t"
      "ds_read_b128 a[136:139], %[lx0] offset:7680\n\t"
      "v_mul_f32_e32 v119, s86, v119\n\t"
      "v_mfma_f32_16x16x32_bf16 v[168:171], a[104:107], a[148:151], v[168:171]\n\t"
      "v_exp_f32_e32 v118, v118\n\t"
      "v_mfma_f32_16x16x32_bf16 v[168:171], a[96:99], a[144:147], v[168:171]\n\t"
      "v_exp_f32_e32 v119, v119\n\t"
      "v_mfma_f32_16x16x32_bf16 v[168:171], a[88:91], a[172:175], v[168:171]\n\t"
      "v_fma_f32 v224, v224, -0.5, -0.5\n\t"
      "v_fma_f32 v225, v225, -0.5, -0.5\n\t"
      "v_mfma_f32_16x16x32_bf16 v[168:171], a[80:83], a[168:171], v[168:171]\n\t"
      "v_fma_f32 v226, v226, -0.5, -0.5\n\t"
      "v_fma_f32 v227, v227, -0.5, -0.5\n\t"
      "v_mfma_f32_16x16x32_bf16 v[168:171], a[72:75], a[164:167], v[168:171]\n\t"
      "v_fma_f32 v116, v224, v116, v224\n\t"
      "v_fma_f32 v117, v225, v117, v225\n\t"
      "v_mfma_f32_16x16x32_bf16 v[176:179], a[72:75], a[168:171], v[176:179]\n\t"
      "ds_read_b128 a[140:143], %[lx0] offset:7936\n\t"
      "v_fma_f32 v118, v226, v118, v226\n\t"
      "v_mfma_f32_16x16x32_bf16 v[176:179], a[80:83], a[172:175], v[176:179]\n\t"
      "v_rcp_f32_e32 v116, v116\n\t"
      "v_mfma_f32_16x16x32_bf16 v[176:179], a[88:91], a[144:147], v[176:179]\n\t"
      "v_rcp_f32_e32 v117, v117\n\t"
      "v_mfma_f32_16x16x32_bf16 v[176:179], a[96:99], a[148:151], v[176:179]\n\t"
      "s_waitcnt lgkmcnt(5)\n\t"
      "v_rcp_f32_e32 v118, v118\n\t"
      "v_mfma_f32_16x16x32_bf16 v[176:179], a[104:107], a[152:155], v[176:179]\n\t"
      "v_fma_f32 v119, v227, v119, v227\n\t"
      "v_bfi_b32 v112, s75, v116, v112\n\t"
      "v_mfma_f32_16x16x32_bf16 v[180:183], a[108:111], a[152:155], v[180:183]\n\t"
      "v_rcp_f32_e32 v119, v119\n\t"
      "v_mfma_f32_16x16x32_bf16 v[180:183], a[100:103], a[148:151], v[180:183]\n\t"
      "v_bfi_b32 v113, s75, v117, v113\n\t"
      "v_bfi_b32 v114, s75, v118, v114\n\t"
      "v_mfma_f32_16x16x32_bf16 v[180:183], a[92:95], a[144:147], v[180:183]\n\t"
      "v_bfi_b32 v115, s75, v119, v115\n\t"
      "v_fmac_f32_e32 v112, v224, v112\n\t"
      "v_mfma_f32_16x16x32_bf16 v[180:183], a[84:87], a[172:175], v[180:183]\n\t"
      "v_fmac_f32_e32 v113, v225, v113\n\t"
      "v_fmac_f32_e32 v114, v226, v114\n\t"
      "v_mfma_f32_16x16x32_bf16 v[180:183], a[76:79], a[168:171], v[180:183]\n\t"
      "v_fmac_f32_e32 v115, v227, v115\n\t"
      "v_cvt_pk_bf16_f32 v116, v112, v113\n\t"
      "v_mfma_f32_16x16x32_bf16 v[188:191], a[76:79], a[172:175], v[188:191]\n\t"
      "s_mov_b32 s68, 28672\n\t"
      "v_cvt_pk_bf16_f32 v117, v114, v115\n\t"
      "v_mul_f32_e64 v228, s84, |v120|\n\t"
      "v_mfma_f32_16x16x32_bf16 v[188:191], a[84:87], a[144:147], v[188:191]\n\t"
      "buffer_store_dwordx2 v[116:117], %[vo], s[52:55], s68 offen offset:0 nt\n\t"
      "v_mfma_f32_16x16x32_bf16 v[188:191], a[92:95], a[148:151], v[188:191]\n\t"
      "v_exp_f32_e32 v228, v228\n\t"
      "v_mfma_f32_16x16x32_bf16 v[188:191], a[100:103], a[152:155], v[188:191]\n\t"
      "s_waitcnt lgkmcnt(4)\n\t"
      "v_mul_f32_e64 v229, s84, |v121|\n\t"
      "v_mul_f32_e64 v230, s84, |v122|\n\t"
      "v_mfma_f32_16x16x32_bf16 v[188:191], a[108:111], a[156:159], v[188:191]\n\t"
      "v_exp_f32_e32 v229, v229\n\t"
      "v_mfma_f32_16x16x32_bf16 v[184:187], a[104:107], a[156:159], v[184:187]\n\t"
      "v_exp_f32_e32 v230, v230\n\t"
      "v_mfma_f32_16x16x32_bf16 v[184:187], a[96:99], a[152:155], v[184:187]\n\t"
      "v_mul_f32_e64 v231, s84, |v123|\n\t"
      "v_mul_f32_e32 v124, s86, v124\n\t"
      "v_mfma_f32_16x16x32_bf16 v[184:187], a[88:91], a[148:151], v[184:187]\n\t"
      "v_exp_f32_e32 v231, v231\n\t"
      "v_mfma_f32_16x16x32_bf16 v[184:187], a[80:83], a[144:147], v[184:187]\n\t"
      "v_exp_f32_e32 v124, v124\n\t"
      "v_mfma_f32_16x16x32_bf16 v[184:187], a[72:75], a[172:175], v[184:187]\n\t"
      "v_mul_f32_e32 v125, s86, v125\n\t"
      "v_mul_f32_e32 v126, s86, v126\n\t"
      "v_mfma_f32_16x16x32_bf16 v[192:195], a[32:35], a[112:115], v[192:195]\n\t"
      "v_exp_f32_e32 v125, v125\n\t"
      "v_mfma_f32_16x16x32_bf16 v[192:195], a[40:43], a[116:119], v[192:195]\n\t"
      "v_exp_f32_e32 v126, v126\n\t"
      "v_mfma_f32_16x16x32_bf16 v[192:195], a[48:51], a[120:123], v[192:195]\n\t"
      "v_mul_f32_e32 v127, s86, v127\n\t"
      "v_fma_f32 v228, v228, -0.5, -0.5\n\t"
      "v_mfma_f32_16x16x32_bf16 v[192:195], a[56:59], a[124:127], v[192:195]\n\t"
      "s_waitcnt lgkmcnt(3)\n\t"
      "v_exp_f32_e32 v127, v127\n\t"
      "v_mfma_f32_16x16x32_bf16 v[192:195], a[64:67], a[128:131], v[192:195]\n\t"
      "ds_read_b128 a[160:163], %[lx1] offset:7168\n\t"
      "v_fma_f32 v229, v229, -0.5, -0.5\n\t"
      "v_mfma_f32_16x16x32_bf16 v[196:199], a[68:71], a[128:131], v[196:199]\n\t"
      "v_fma_f32 v230, v230, -0.5, -0.5\n\t"
      "v_fma_f32 v231, v231, -0.5, -0.5\n\t"
      "v_mfma_f32_16x16x32_bf16 v[196:199], a[60:63], a[124:127], v[196:199]\n\t"
      "v_fma_f32 v124, v228, v124, v228\n\t"
      "v_fma_f32 v125, v229, v125, v229\n\t"
      "v_mfma_f32_16x16x32_bf16 v[196:199], a[52:55], a[120:123], v[196:199]\n\t"
      "v_rcp_f32_e32 v124, v124\n\t"
      "v_mfma_f32_16x16x32_bf16 v[196:199], a[44:47], a[116:119], v[196:199]\n\t"
      "v_rcp_f32_e32 v125, v125\n\t"
      "v_mfma_f32_16x16x32_bf16 v[196:199], a[36:39], a[112:115], v[196:199]\n\t"
      "v_fma_f32 v126, v230, v126, v230\n\t"
      "v_fma_f32 v127, v231, v127, v231\n\t"
      "v_mfma_f32_16x16x32_bf16 v[204:207], a[36:39], a[116:119], v[204:207]\n\t"
      "ds_read_b128 a[112:115], %[lx0] offset:8192\n\t"
      "v_bfi_b32 v120, s75, v124, v120\n\t"
      "v_mfma_f32_16x16x32_bf16 v[204:207], a[44:47], a[120:123], v[204:207]\n\t"
      "v_rcp_f32_e32 v126, v126\n\t"
      "v_mfma_f32_16x16x32_bf16 v[204:207], a[52:55], a[124:127], v[204:207]\n\t"
      "v_rcp_f32_e32 v127, v127\n\t"
      "v_mfma_f32_16x16x32_bf16 v[204:207], a[60:63], a[128:131], v[204:207]\n\t"
      "s_waitcnt lgkmcnt(4)\n\t"
      "v_bfi_b32 v121, s75, v125, v121\n\t"
      "v_bfi_b32 v122, s75, v126, v122\n\t"
      "v_mfma_f32_16x16x32_bf16 v[204:207], a[68:71], a[132:135], v[204:207]\n\t"
      "ds_read_b128 a[164:167], %[lx1] offset:7424\n\t"
      "v_bfi_b32 v123, s75, v127, v123\n\t"
      "v_mfma_f32_16x16x32_bf16 v[200:203], a[64:67], a[132:135], v[200:203]\n\t"
      "v_fmac_f32_e32 v120, v228, v120\n\t"
      "v_fmac_f32_e32 v121, v229, v121\n\t"
      "v_mfma_f32_16x16x32_bf16 v[200:203], a[56:59], a[128:131], v[200:203]\n\t"
      "v_fmac_f32_e32 v122, v230, v122\n\t"
      "v_fmac_f32_e32 v123, v231, v123\n\t"
      "v_mfma_f32_16x16x32_bf16 v[200:203], a[48:51], a[124:127], v[200:203]\n\t"
      "v_cvt_pk_bf16_f32 v124, v120, v121\n\t"
      "v_cvt_pk_bf16_f32 v125, v122, v123\n\t"
      "v_mfma_f32_16x16x32_bf16 v[200:203], a[40:43], a[120:123], v[200:203]\n\t"
      "s_mov_b32 s68, 28672\n\t"
      "v_mul_f32_e64 v224, s84, |v128|\n\t"
      "v_mul_f32_e64 v225, s84, |v129|\n\t"
      "v_mfma_f32_16x16x32_bf16 v[200:203], a[32:35], a[116:119], v[200:203]\n\t"
      "buffer_store_dwordx2 v[124:125], %[vo], s[52:55], s68 offen offset:2048 nt\n\t"
      "v_mfma_f32_16x16x32_bf16 v[208:211], a[32:35], a[120:123], v[208:211]\n\t"
      "ds_read_b128 a[116:119], %[lx0] offset:8448\n\t"
      "v_mul_f32_e64 v226, s84, |v130|\n\t"
      "v_mfma_f32_16x16x32_bf16 v[208:211], a[40:43], a[124:127], v[208:211]\n\t"
      "v_exp_f32_e32 v224, v224\n\t"
      "v_mfma_f32_16x16x32_bf16 v[208:211], a[48:51], a[128:131], v[208:211]\n\t"
      "v_exp_f32_e32 v225, v225\n\t"
      "v_mfma_f32_16x16x32_bf16 v[208:211], a[56:59], a[132:135], v[208:211]\n\t"
      "s_waitcnt lgkmcnt(5)\n\t"
      "v_exp_f32_e32 v226, v226\n\t"
      "v_mfma_f32_16x16x32_bf16 v[208:211], a[64:67], a[136:139], v[208:211]\n\t"
      "ds_read_b128 a[168:171], %[lx1] offset:7680\n\t"
      "v_mul_f32_e64 v227, s84, |v131|\n\t"
      "v_mfma_f32_16x16x32_bf16 v[212:215], a[68:71], a[136:139], v[212:215]\n\t"
      "v_exp_f32_e32 v227, v227\n\t"
      "v_mfma_f32_16x16x32_bf16 v[212:215], a[60:63], a[132:135], v[212:215]\n\t"
      "v_mul_f32_e32 v132, s86, v132\n\t"
      "v_mul_f32_e32 v133, s86, v133\n\t"
      "v_mfma_f32_16x16x32_bf16 v[212:215], a[52:55], a[128:131], v[212:215]\n\t"
      "v_exp_f32_e32 v132, v132\n\t"
      "v_mfma_f32_16x16x32_bf16 v[212:215], a[44:47], a[124:127], v[212:215]\n\t"
      "v_exp_f32_e32 v133, v133\n\t"
      "v_mfma_f32_16x16x32_bf16 v[212:215], a[36:39], a[120:123], v[212:215]\n\t"
      "v_mul_f32_e32 v134, s86, v134\n\t"
      "v_mul_f32_e32 v135, s86, v135\n\t"
      "v_mfma_f32_16x16x32_bf16 v[220:223], a[36:39], a[124:127], v[220:223]\n\t"
      "ds_read_b128 a[120:123], %[lx0] offset:8704\n\t"
      "v_fma_f32 v224, v224, -0.5, -0.5\n\t"
      "v_mfma_f32_16x16x32_bf16 v[220:223], a[44:47], a[128:131], v[220:223]\n\t"
      "v_exp_f32_e32 v134, v134\n\t"
      "v_mfma_f32_16x16x32_bf16 v[220:223], a[52:55], a[132:135], v[220:223]\n\t"
      "v_exp_f32_e32 v135, v135\n\t"
      "v_mfma_f32_16x16x32_bf16 v[220:223], a[60:63], a[136:139], v[220:223]\n\t"
      "s_waitcnt lgkmcnt(6)\n\t"
      "v_fma_f32 v225, v225, -0.5, -0.5\n\t"
      "v_fma_f32 v226, v226, -0.5, -0.5\n\t"
      "v_mfma_f32_16x16x32_bf16 v[220:223], a[68:71], a[140:143], v[220:223]\n\t"
      "ds_read_b128 a[172:175], %[lx1] offset:7936\n\t"
      "v_fma_f32 v227, v227, -0.5, -0.5\n\t"
      "v_mfma_f32_16x16x32_bf16 v[216:219], a[64:67], a[140:143], v[216:219]\n\t"
      "v_fma_f32 v132, v224, v132, v224\n\t"
      "v_fma_f32 v133, v225, v133, v225\n\t"
      "v_mfma_f32_16x16x32_bf16 v[216:219], a[56:59], a[136:139], v[216:219]\n\t"
      "v_rcp_f32_e32 v132, v132\n\t"
      "v_mfma_f32_16x16x32_bf16 v[216:219], a[48:51], a[132:135], v[216:219]\n\t"
      "v_rcp_f32_e32 v133, v133\n\t"
      "v_mfma_f32_16x16x32_bf16 v[216:219], a[40:43], a[128:131], v[216:219]\n\t"
      "v_fma_f32 v134, v226, v134, v226\n\t"
      "v_fma_f32 v135, v227, v135, v227\n\t"
      "v_mfma_f32_16x16x32_bf16 v[216:219], a[32:35], a[124:127], v[216:219]\n\t"
      "v_rcp_f32_e32 v134, v134\n\t"
      "v_mfma_f32_16x16x32_bf16 a[0:3], a[32:35], a[128:131], a[0:3]\n\t"
      "ds_read_b128 a[124:127], %[lx0] offset:8960\n\t"
      "v_bfi_b32 v128, s75, v132, v128\n\t"
      "v_mfma_f32_16x16x32_bf16 a[0:3], a[40:43], a[132:135], a[0:3]\n\t"
      "v_rcp_f32_e32 v135, v135\n\t"
      "v_mfma_f32_16x16x32_bf16 a[0:3], a[48:51], a[136:139], a[0:3]\n\t"
      "v_bfi_b32 v129, s75, v133, v129\n\t"
      "v_bfi_b32 v130, s75, v134, v130\n\t"
      "v_mfma_f32_16x16x32_bf16 a[0:3], a[56:59], a[140:143], a[0:3]\n\t"
      "s_waitcnt lgkmcnt(6)\n\t"
      "v_bfi_b32 v131, s75, v135, v131\n\t"
      "v_fmac_f32_e32 v128, v224, v128\n\t"
      "v_mfma_f32_16x16x32_bf16 a[0:3], a[64:67], a[112:115], a[0:3]\n\t"
      "v_fmac_f32_e32 v129, v225, v129\n\t"
      "v_fmac_f32_e32 v130, v226, v130\n\t"
      "v_mfma_f32_16x16x32_bf16 a[4:7], a[68:71], a[112:115], a[4:7]\n\t"
      "v_fmac_f32_e32 v131, v227, v131\n\t"
      "v_cvt_pk_bf16_f32 v132, v128, v129\n\t"
      "v_mfma_f32_16x16x32_bf16 a[4:7], a[60:63], a[140:143], a[4:7]\n\t"
      "s_mov_b32 s68, 32768\n\t"
      "v_cvt_pk_bf16_f32 v133, v130, v131\n\t"
      "v_mul_f32_e64 v228, s84, |v136|\n\t"
      "v_mfma_f32_16x16x32_bf16 a[4:7], a[52:55], a[136:139], a[4:7]\n\t"
      "buffer_store_dwordx2 v[132:133], %[vo], s[52:55], s68 offen offset:0 nt\n\t"
      "v_mfma_f32_16x16x32_bf16 a[4:7], a[44:47], a[132:135], a[4:7]\n\t"
      "v_exp_f32_e32 v228, v228\n\t"
      "v_mfma_f32_16x16x32_bf16 a[4:7], a[36:39], a[128:131], a[4:7]\n\t"
      "v_mul_f32_e64 v229, s84, |v137|\n\t"
      "v_mul_f32_e64 v230, s84, |v138|\n\t"
      "v_mfma_f32_16x16x32_bf16 a[12:15], a[36:39], a[132:135], a[12:15]\n\t"
      "v_exp_f32_e32 v229, v229\n\t"
      "v_mfma_f32_16x16x32_bf16 a[12:15], a[44:47], a[136:139], a[12:15]\n\t"
      "v_exp_f32_e32 v230, v230\n\t"
      "v_mfma_f32_16x16x32_bf16 a[12:15], a[52:55], a[140:143], a[12:15]\n\t"
      "v_mul_f32_e64 v231, s84, |v139|\n\t"
      "v_mul_f32_e32 v140, s86, v140\n\t"
      "v_mfma_f32_16x16x32_bf16 a[12:15], a[60:63], a[112:115], a[12:15]\n\t"
      "s_waitcnt lgkmcnt(4)\n\t"
      "v_exp_f32_e32 v231, v231\n\t"
      "v_mfma_f32_16x16x32_bf16 a[12:15], a[68:71], a[116:119], a[12:15]\n\t"
      "v_exp_f32_e32 v140, v140\n\t"
      "v_mfma_f32_16x16x32_bf16 a[8:11], a[64:67], a[116:119], a[8:11]\n\t"
      "v_mul_f32_e32 v141, s86, v141\n\t"
      "v_mul_f32_e32 v142, s86, v142\n\t"
      "v_mfma_f32_16x16x32_bf16 a[8:11], a[56:59], a[112:115], a[8:11]\n\t"
      "v_exp_f32_e32 v141, v141\n\t"
      "v_mfma_f32_16x16x32_bf16 a[8:11], a[48:51], a[140:143], a[8:11]\n\t"
      "v_exp_f32_e32 v142, v142\n\t"
      "v_mfma_f32_16x16x32_bf16 a[8:11], a[40:43], a[136:139], a[8:11]\n\t"
      "s_cmp_le_u32 s78, 30\n\t"
      "v_mfma_f32_16x16x32_bf16 a[8:11], a[32:35], a[132:135], a[8:11]\n\t"
      "s_cbranch_scc1 LSKSL480_%=\n\t"
      "v_mfma_f32_16x16x32_bf16 a[16:19], a[32:35], a[136:139], a[16:19]\n\t"
      "v_mfma_f32_16x16x32_bf16 a[16:19], a[40:43], a[140:143], a[16:19]\n\t"
      "v_mfma_f32_16x16x32_bf16 a[16:19], a[48:51], a[112:115], a[16:19]\n\t"
      "v_mfma_f32_16x16x32_bf16 a[16:19], a[56:59], a[116:119], a[16:19]\n\t"
      "s_waitcnt lgkmcnt(2)\n\t"
      "v_mfma_f32_16x16x32_bf16 a[16:19], a[64:67], a[120:123], a[16:19]\n\t"
      "v_mfma_f32_16x16x32_bf16 a[20:23], a[68:71], a[120:123], a[20:23]\n\t"
      "v_mfma_f32_16x16x32_bf16 a[20:23], a[60:63], a[116:119], a[20:23]\n\t"
      "v_mfma_f32_16x16x32_bf16 a[20:23], a[52:55], a[112:115], a[20:23]\n\t"
      "v_mfma_f32_16x16x32_bf16 a[20:23], a[44:47], a[140:143], a[20:23]\n\t"
      "s_cmp_le_u32 s78, 31\n\t"
      "v_mfma_f32_16x16x32_bf16 a[20:23], a[36:39], a[136:139], a[20:23]\n\t"
      "s_cbranch_scc1 LSKSL480_%=\n\t"
      "v_mfma_f32_16x16x32_bf16 a[28:31], a[36:39], a[140:143], a[28:31]\n\t"
      "v_mfma_f32_16x16x32_bf16 a[28:31], a[44:47], a[112:115], a[28:31]\n\t"
      "v_mfma_f32_16x16x32_bf16 a[28:31], a[52:55], a[116:119], a[28:31]\n\t"
      "v_mfma_f32_16x16x32_bf16 a[28:31], a[60:63], a[120:123], a[28:31]\n\t"
      "s_waitcnt lgkmcnt(0)\n\t"
      "v_mfma_f32_16x16x32_bf16 a[28:31], a[68:71], a[124:127], a[28:31]\n\t"
      "v_mfma_f32_16x16x32_bf16 a[24:27], a[64:67], a[124:127], a[24:27]\n\t"
      "v_mfma_f32_16x16x32_bf16 a[24:27], a[56:59], a[120:123], a[24:27]\n\t"
      "v_mfma_f32_16x16x32_bf16 a[24:27], a[48:51], a[116:119], a[24:27]\n\t"
      "v_mfma_f32_16x16x32_bf16 a[24:27], a[40:43], a[112:115], a[24:27]\n\t"
      "v_mfma_f32_16x16x32_bf16 a[24:27], a[32:35], a[140:143], a[24:27]\n\t"
      "LSKSL480_%=:\n\t"
      "v_mfma_f32_16x16x32_bf16 v[192:195], a[72:75], a[144:147], v[192:195]\n\t"
      "v_mul_f32_e32 v143, s86, v143\n\t"
      "v_fma_f32 v228, v228, -0.5, -0.5\n\t"
      "v_mfma_f32_16x16x32_bf16 v[192:195], a[80:83], a[148:151], v[192:195]\n\t"
      "v_exp_f32_e32 v143, v143\n\t"
      "v_mfma_f32_16x16x32_bf16 v[192:195], a[88:91], a[152:155], v[192:195]\n\t"
      "v_fma_f32 v229, v229, -0.5, -0.5\n\t"
      "v_fma_f32 v230, v230, -0.5, -0.5\n\t"
      "v_mfma_f32_16x16x32_bf16 v[192:195], a[96:99], a[156:159], v[192:195]\n\t"
      "v_fma_f32 v231, v231, -0.5, -0.5\n\t"
      "v_fma_f32 v140, v228, v140, v228\n\t"
      "v_mfma_f32_16x16x32_bf16 v[192:195], a[104:107], a[160:163], v[192:195]\n\t"
      "v_rcp_f32_e32 v140, v140\n\t"
      "v_mfma_f32_16x16x32_bf16 v[196:199], a[108:111], a[160:163], v[196:199]\n\t"
      "v_fma_f32 v141, v229, v141, v229\n\t"
      "v_fma_f32 v142, v230, v142, v230\n\t"
      "v_mfma_f32_16x16x32_bf16 v[196:199], a[100:103], a[156:159], v[196:199]\n\t"
      "v_rcp_f32_e32 v141, v141\n\t"
      "v_mfma_f32_16x16x32_bf16 v[196:199], a[92:95], a[152:155], v[196:199]\n\t"
      "v_rcp_f32_e32 v142, v142\n\t"
      "v_mfma_f32_16x16x32_bf16 v[196:199], a[84:87], a[148:151], v[196:199]\n\t"
      "v_fma_f32 v143, v231, v143, v231\n\t"
      "v_bfi_b32 v136, s75, v140, v136\n\t"
      "v_mfma_f32_16x16x32_bf16 v[196:199], a[76:79], a[144:147], v[196:199]\n\t"
      "v_rcp_f32_e32 v143, v143\n\t"
      "v_mfma_f32_16x16x32_bf16 v[204:207], a[76:79], a[148:151], v[204:207]\n\t"
      "ds_read_b128 a[144:147], %[lx1] offset:8192\n\t"
      "v_bfi_b32 v137, s75, v141, v137\n\t"
      "v_mfma_f32_16x16x32_bf16 v[204:207], a[84:87], a[152:155], v[204:207]\n\t"
      "v_bfi_b32 v138, s75, v142, v138\n\t"
      "v_bfi_b32 v139, s75, v143, v139\n\t"
      "v_mfma_f32_16x16x32_bf16 v[204:207], a[92:95], a[156:159], v[204:207]\n\t"
      "v_fmac_f32_e32 v136, v228, v136\n\t"
      "v_fmac_f32_e32 v137, v229, v137\n\t"
      "v_mfma_f32_16x16x32_bf16 v[204:207], a[100:103], a[160:163], v[204:207]\n\t"
      "v_fmac_f32_e32 v138, v230, v138\n\t"
      "v_fmac_f32_e32 v139, v231, v139\n\t"
      "v_mfma_f32_16x16x32_bf16 v[204:207], a[108:111], a[164:167], v[204:207]\n\t"
      "v_cvt_pk_bf16_f32 v140, v136, v137\n\t"
      "v_cvt_pk_bf16_f32 v141, v138, v139\n\t"
      "v_mfma_f32_16x16x32_bf16 v[200:203], a[104:107], a[164:167], v[200:203]\n\t"
      "s_mov_b32 s68, 32768\n\t"
      "v_mul_f32_e64 v224, s84, |v144|\n\t"
      "v_mul_f32_e64 v225, s84, |v145|\n\t"
      "v_mfma_f32_16x16x32_bf16 v[200:203], a[96:99], a[160:163], v[200:203]\n\t"
      "buffer_store_dwordx2 v[140:141], %[vo], s[52:55], s68 offen offset:2048 nt\n\t"
      "v_mfma_f32_16x16x32_bf16 v[200:203], a[88:91], a[156:159], v[200:203]\n\t"
      "v_exp_f32_e32 v224, v224\n\t"
      "v_mfma_f32_16x16x32_bf16 v[200:203], a[80:83], a[152:155], v[200:203]\n\t"
      "v_exp_f32_e32 v225, v225\n\t"
      "v_mfma_f32_16x16x32_bf16 v[200:203], a[72:75], a[148:151], v[200:203]\n\t"
      "v_mul_f32_e64 v226, s84, |v146|\n\t"
      "v_mul_f32_e64 v227, s84, |v147|\n\t"
      "v_mfma_f32_16x16x32_bf16 v[208:211], a[72:75], a[152:155], v[208:211]\n\t"
      "ds_read_b128 a[148:151], %[lx1] offset:8448\n\t"
      "v_mul_f32_e32 v148, s86, v148\n\t"
      "v_mfma_f32_16x16x32_bf16 v[208:211], a[80:83], a[156:159], v[208:211]\n\t"
      "v_exp_f32_e32 v226, v226\n\t"
      "v_mfma_f32_16x16x32_bf16 v[208:211], a[88:91], a[160:163], v[208:211]\n\t"
      "v_exp_f32_e32 v227, v227\n\t"
      "v_mfma_f32_16x16x32_bf16 v[208:211], a[96:99], a[164:167], v[208:211]\n\t"
      "s_waitcnt lgkmcnt(5)\n\t"
      "v_exp_f32_e32 v148, v148\n\t"
      "v_mfma_f32_16x16x32_bf16 v[208:211], a[104:107], a[168:171], v[208:211]\n\t"
      "v_mul_f32_e32 v149, s86, v149\n\t"
      "v_mul_f32_e32 v150, s86, v150\n\t"
      "v_mfma_f32_16x16x32_bf16 v[212:215], a[108:111], a[168:171], v[212:215]\n\t"
      "v_exp_f32_e32 v149, v149\n\t"
      "v_mfma_f32_16x16x32_bf16 v[212:215], a[100:103], a[164:167], v[212:215]\n\t"
      "v_exp_f32_e32 v150, v150\n\t"
      "v_mfma_f32_16x16x32_bf16 v[212:215], a[92:95], a[160:163], v[212:215]\n\t"
      "v_mul_f32_e32 v151, s86, v151\n\t"
      "v_fma_f32 v224, v224, -0.5, -0.5\n\t"
      "v_mfma_f32_16x16x32_bf16 v[212:215], a[84:87], a[156:159], v[212:215]\n\t"
      "v_exp_f32_e32 v151, v151\n\t"
      "v_mfma_f32_16x16x32_bf16 v[212:215], a[76:79], a[152:155], v[212:215]\n\t"
      "v_fma_f32 v225, v225, -0.5, -0.5\n\t"
      "v_fma_f32 v226, v226, -0.5, -0.5\n\t"
      "v_mfma_f32_16x16x32_bf16 v[220:223], a[76:79], a[156:159], v[220:223]\n\t"
      "ds_read_b128 a[152:155], %[lx1] offset:8704\n\t"
      "v_fma_f32 v227, v227, -0.5, -0.5\n\t"
      "v_mfma_f32_16x16x32_bf16 v[220:223], a[84:87], a[160:163], v[220:223]\n\t"
      "v_fma_f32 v148, v224, v148, v224\n\t"
      "v_fma_f32 v149, v225, v149, v225\n\t"
      "v_mfma_f32_16x16x32_bf16 v[220:223], a[92:95], a[164:167], v[220:223]\n\t"
      "v_rcp_f32_e32 v148, v148\n\t"
      "v_mfma_f32_16x16x32_bf16 v[220:223], a[100:103], a[168:171], v[220:223]\n\t"
      "s_waitcnt lgkmcnt(4)\n\t"
      "v_rcp_f32_e32 v149, v149\n\t"
      "v_mfma_f32_16x16x32_bf16 v[220:223], a[108:111], a[172:175], v[220:223]\n\t"
      "v_fma_f32 v150, v226, v150, v226\n\t"
      "v_fma_f32 v151, v227, v151, v227\n\t"
      "v_mfma_f32_16x16x32_bf16 v[216:219], a[104:107], a[172:175], v[216:219]\n\t"
      "v_rcp_f32_e32 v150, v150\n\t"
      "v_mfma_f32_16x16x32_bf16 v[216:219], a[96:99], a[168:171], v[216:219]\n\t"
      "v_rcp_f32_e32 v151, v151\n\t"
      "v_mfma_f32_16x16x32_bf16 v[216:219], a[88:91], a[164:167], v[216:219]\n\t"
      "v_bfi_b32 v144, s75, v148, v144\n\t"
      "v_bfi_b32 v145, s75, v149, v145\n\t"
      "v_mfma_f32_16x16x32_bf16 v[216:219], a[80:83], a[160:163], v[216:219]\n\t"
      "v_bfi_b32 v146, s75, v150, v146\n\t"
      "v_bfi_b32 v147, s75, v151, v147\n\t"
      "v_mfma_f32_16x16x32_bf16 v[216:219], a[72:75], a[156:159], v[216:219]\n\t"
      "v_fmac_f32_e32 v144, v224, v144\n\t"
      "v_fmac_f32_e32 v145, v225, v145\n\t"
      "v_mfma_f32_16x16x32_bf16 a[0:3], a[72:75], a[160:163], a[0:3]\n\t"
      "ds_read_b128 a[156:159], %[lx1] offset:8960\n\t"
      "v_fmac_f32_e32 v146, v226, v146\n\t"
      "v_mfma_f32_16x16x32_bf16 a[0:3], a[80:83], a[164:167], a[0:3]\n\t"
      "v_fmac_f32_e32 v147, v227, v147\n\t"
      "v_cvt_pk_bf16_f32 v148, v144, v145\n\t"
      "v_mfma_f32_16x16x32_bf16 a[0:3], a[88:91], a[168:171], a[0:3]\n\t"
      "s_mov_b32 s68, 36864\n\t"
      "v_cvt_pk_bf16_f32 v149, v146, v147\n\t"
      "v_mul_f32_e64 v228, s84, |v152|\n\t"
      "v_mfma_f32_16x16x32_bf16 a[0:3], a[96:99], a[172:175], a[0:3]\n\t"
      "s_waitcnt lgkmcnt(3)\n\t"
      "buffer_store_dwordx2 v[148:149], %[vo], s[52:55], s68 offen offset:0 nt\n\t"
      "v_mfma_f32_16x16x32_bf16 a[0:3], a[104:107], a[144:147], a[0:3]\n\t"
      "v_exp_f32_e32 v228, v228\n\t"
      "v_mfma_f32_16x16x32_bf16 a[4:7], a[108:111], a[144:147], a[4:7]\n\t"
      "v_mul_f32_e64 v229, s84, |v153|\n\t"
      "v_mul_f32_e64 v230, s84, |v154|\n\t"
      "v_mfma_f32_16x16x32_bf16 a[4:7], a[100:103], a[172:175], a[4:7]\n\t"
      "v_exp_f32_e32 v229, v229\n\t"
      "v_mfma_f32_16x16x32_bf16 a[4:7], a[92:95], a[168:171], a[4:7]\n\t"
      "v_exp_f32_e32 v230, v230\n\t"
      "v_mfma_f32_16x16x32_bf16 a[4:7], a[84:87], a[164:167], a[4:7]\n\t"
      "v_mul_f32_e64 v231, s84, |v155|\n\t"
      "v_mul_f32_e32 v156, s86, v156\n\t"
      "v_mfma_f32_16x16x32_bf16 a[4:7], a[76:79], a[160:163], a[4:7]\n\t"
      "v_exp_f32_e32 v231, v231\n\t"
      "v_mfma_f32_16x16x32_bf16 a[12:15], a[76:79], a[164:167], a[12:15]\n\t"
      "v_exp_f32_e32 v156, v156\n\t"
      "v_mfma_f32_16x16x32_bf16 a[12:15], a[84:87], a[168:171], a[12:15]\n\t"
      "v_mul_f32_e32 v157, s86, v157\n\t"
      "v_mul_f32_e32 v158, s86, v158\n\t"
      "v_mfma_f32_16x16x32_bf16 a[12:15], a[92:95], a[172:175], a[12:15]\n\t"
      "v_exp_f32_e32 v157, v157\n\t"
      "v_mfma_f32_16x16x32_bf16 a[12:15], a[100:103], a[144:147], a[12:15]\n\t"
      "s_waitcnt lgkmcnt(2)\n\t"
      "v_exp_f32_e32 v158, v158\n\t"
      "v_mfma_f32_16x16x32_bf16 a[12:15], a[108:111], a[148:151], a[12:15]\n\t"
      "v_mul_f32_e32 v159, s86, v159\n\t"
      "v_fma_f32 v228, v228, -0.5, -0.5\n\t"
      "v_mfma_f32_16x16x32_bf16 a[8:11], a[104:107], a[148:151], a[8:11]\n\t"
      "v_exp_f32_e32 v159, v159\n\t"
      "v_mfma_f32_16x16x32_bf16 a[8:11], a[96:99], a[144:147], a[8:11]\n\t"
      "v_fma_f32 v229, v229, -0.5, -0.5\n\t"
      "v_fma_f32 v230, v230, -0.5, -0.5\n\t"
      "v_mfma_f32_16x16x32_bf16 a[8:11], a[88:91], a[172:175], a[8:11]\n\t"
      "v_fma_f32 v231, v231, -0.5, -0.5\n\t"
      "v_fma_f32 v156, v228, v156, v228\n\t"
      "v_mfma_f32_16x16x32_bf16 a[8:11], a[80:83], a[168:171], a[8:11]\n\t"
      "s_cmp_le_u32 s78, 30\n\t"
      "v_mfma_f32_16x16x32_bf16 a[8:11], a[72:75], a[164:167], a[8:11]\n\t"
      "s_cbranch_scc1 LSKSL560_%=\n\t"
      "v_mfma_f32_16x16x32_bf16 a[16:19], a[72:75], a[168:171], a[16:19]\n\t"
      "v_mfma_f32_16x16x32_bf16 a[16:19], a[80:83], a[172:175], a[16:19]\n\t"
      "v_mfma_f32_16x16x32_bf16 a[16:19], a[88:91], a[144:147], a[16:19]\n\t"
      "v_mfma_f32_16x16x32_bf16 a[16:19], a[96:99], a[148:151], a[16:19]\n\t"
      "s_waitcnt lgkmcnt(1)\n\t"
      "v_mfma_f32_16x16x32_bf16 a[16:19], a[104:107], a[152:155], a[16:19]\n\t"
      "v_mfma_f32_16x16x32_bf16 a[20:23], a[108:111], a[152:155], a[20:23]\n\t"
      "v_mfma_f32_16x16x32_bf16 a[20:23], a[100:103], a[148:151], a[20:23]\n\t"
      "v_mfma_f32_16x16x32_bf16 a[20:23], a[92:95], a[144:147], a[20:23]\n\t"
      "v_mfma_f32_16x16x32_bf16 a[20:23], a[84:87], a[172:175], a[20:23]\n\t"
      "s_cmp_le_u32 s78, 31\n\t"
      "v_mfma_f32_16x16x32_bf16 a[20:23], a[76:79], a[168:171], a[20:23]\n\t"
      "s_cbranch_scc1 LSKSL560_%=\n\t"
      "v_mfma_f32_16x16x32_bf16 a[28:31], a[76:79], a[172:175], a[28:31]\n\t"
      "v_mfma_f32_16x16x32_bf16 a[28:31], a[84:87], a[144:147], a[28:31]\n\t"
      "v_mfma_f32_16x16x32_bf16 a[28:31], a[92:95], a[148:151], a[28:31]\n\t"
      "v_mfma_f32_16x16x32_bf16 a[28:31], a[100:103], a[152:155], a[28:31]\n\t"
      "s_waitcnt lgkmcnt(0)\n\t"
      "v_mfma_f32_16x16x32_bf16 a[28:31], a[108:111], a[156:159], a[28:31]\n\t"
      "v_mfma_f32_16x16x32_bf16 a[24:27], a[104:107], a[156:159], a[24:27]\n\t"
      "v_mfma_f32_16x16x32_bf16 a[24:27], a[96:99], a[152:155], a[24:27]\n\t"
      "v_mfma_f32_16x16x32_bf16 a[24:27], a[88:91], a[148:151], a[24:27]\n\t"
      "v_mfma_f32_16x16x32_bf16 a[24:27], a[80:83], a[144:147], a[24:27]\n\t"
      "v_mfma_f32_16x16x32_bf16 a[24:27], a[72:75], a[172:175], a[24:27]\n\t"
      "LSKSL560_%=:\n\t"
      "LTL_%=:\n\t"
      "s_nop 7\n\t"
      "v_fma_f32 v157, v229, v157, v229\n\t"
      "v_fma_f32 v158, v230, v158, v230\n\t"
      "v_fma_f32 v159, v231, v159, v231\n\t"
      "v_rcp_f32_e32 v156, v156\n\t"
      "v_rcp_f32_e32 v157, v157\n\t"
      "v_rcp_f32_e32 v158, v158\n\t"
      "v_rcp_f32_e32 v159, v159\n\t"
      "v_bfi_b32 v152, s75, v156, v152\n\t"
      "v_bfi_b32 v153, s75, v157, v153\n\t"
      "v_bfi_b32 v154, s75, v158, v154\n\t"
      "v_bfi_b32 v155, s75, v159, v155\n\t"
      "v_fmac_f32_e32 v152, v228, v152\n\t"
      "v_fmac_f32_e32 v153, v229, v153\n\t"
      "v_fmac_f32_e32 v154, v230, v154\n\t"
      "v_fmac_f32_e32 v155, v231, v155\n\t"
      "v_cvt_pk_bf16_f32 v156, v152, v153\n\t"
      "v_cvt_pk_bf16_f32 v157, v154, v155\n\t"
      "s_mov_b32 s68, 36864\n\t"
      "buffer_store_dwordx2 v[156:157], %[vo], s[52:55], s68 offen offset:2048 nt\n\t"
      "v_pk_mul_f32 v[224:225], v[160:161], s[84:85]\n\t"
      "v_pk_mul_f32 v[226:227], v[162:163], s[84:85]\n\t"
      "v_pk_mul_f32 v[164:165], v[164:165], s[86:87]\n\t"
      "v_pk_mul_f32 v[166:167], v[166:167], s[86:87]\n\t"
      "v_exp_f32_e64 v224, -|v224|\n\t"
      "v_exp_f32_e64 v225, -|v225|\n\t"
      "v_exp_f32_e64 v226, -|v226|\n\t"
      "v_exp_f32_e64 v227, -|v227|\n\t"
      "v_exp_f32_e32 v164, v164\n\t"
      "v_exp_f32_e32 v165, v165\n\t"
      "v_exp_f32_e32 v166, v166\n\t"
      "v_exp_f32_e32 v167, v167\n\t"
      "v_pk_fma_f32 v[224:225], v[224:225], s[88:89], s[88:89]\n\t"
      "v_pk_fma_f32 v[226:227], v[226:227], s[88:89], s[88:89]\n\t"
      "v_pk_fma_f32 v[164:165], v[224:225], v[164:165], v[224:225]\n\t"
      "v_pk_fma_f32 v[166:167], v[226:227], v[166:167], v[226:227]\n\t"
      "v_rcp_f32_e32 v164, v164\n\t"
      "v_rcp_f32_e32 v165, v165\n\t"
      "v_rcp_f32_e32 v166, v166\n\t"
      "v_rcp_f32_e32 v167, v167\n\t"
      "v_bfi_b32 v160, s75, v164, v160\n\t"
      "v_bfi_b32 v161, s75, v165, v161\n\t"
      "v_bfi_b32 v162, s75, v166, v162\n\t"
      "v_bfi_b32 v163, s75, v167, v163\n\t"
      "v_pk_fma_f32 v[160:161], v[224:225], v[160:161], v[160:161] neg_lo:[1,0,0] neg_hi:[1,0,0]\n\t"
      "v_pk_fma_f32 v[162:163], v[226:227], v[162:163], v[162:163] neg_lo:[1,0,0] neg_hi:[1,0,0]\n\t"
      "v_cvt_pk_bf16_f32 v164, v160, v161\n\t"
      "v_cvt_pk_bf16_f32 v165, v162, v163\n\t"
      "s_mov_b32 s68, 40960\n\t"
      "buffer_store_dwordx2 v[164:165], %[vo], s[52:55], s68 offen offset:0 nt\n\t"
      "v_pk_mul_f32 v[228:229], v[168:169], s[84:85]\n\t"
      "v_pk_mul_f32 v[230:231], v[170:171], s[84:85]\n\t"
      "v_pk_mul_f32 v[172:173], v[172:173], s[86:87]\n\t"
      "v_pk_mul_f32 v[174:175], v[174:175], s[86:87]\n\t"
      "v_exp_f32_e64 v228, -|v228|\n\t"
      "v_exp_f32_e64 v229, -|v229|\n\t"
      "v_exp_f32_e64 v230, -|v230|\n\t"
      "v_exp_f32_e64 v231, -|v231|\n\t"
      "v_exp_f32_e32 v172, v172\n\t"
      "v_exp_f32_e32 v173, v173\n\t"
      "v_exp_f32_e32 v174, v174\n\t"
      "v_exp_f32_e32 v175, v175\n\t"
      "v_pk_fma_f32 v[228:229], v[228:229], s[88:89], s[88:89]\n\t"
      "v_pk_fma_f32 v[230:231], v[230:231], s[88:89], s[88:89]\n\t"
      "v_pk_fma_f32 v[172:173], v[228:229], v[172:173], v[228:229]\n\t"
      "v_pk_fma_f32 v[174:175], v[230:231], v[174:175], v[230:231]\n\t"
      "v_rcp_f32_e32 v172, v172\n\t"
      "v_rcp_f32_e32 v173, v173\n\t"
      "v_rcp_f32_e32 v174, v174\n\t"
      "v_rcp_f32_e32 v175, v175\n\t"
      "v_bfi_b32 v168, s75, v172, v168\n\t"
      "v_bfi_b32 v169, s75, v173, v169\n\t"
      "v_bfi_b32 v170, s75, v174, v170\n\t"
      "v_bfi_b32 v171, s75, v175, v171\n\t"
      "v_pk_fma_f32 v[168:169], v[228:229], v[168:169], v[168:169] neg_lo:[1,0,0] neg_hi:[1,0,0]\n\t"
      "v_pk_fma_f32 v[170:171], v[230:231], v[170:171], v[170:171] neg_lo:[1,0,0] neg_hi:[1,0,0]\n\t"
      "v_cvt_pk_bf16_f32 v172, v168, v169\n\t"
      "v_cvt_pk_bf16_f32 v173, v170, v171\n\t"
      "s_mov_b32 s68, 40960\n\t"
      "buffer_store_dwordx2 v[172:173], %[vo], s[52:55], s68 offen offset:2048 nt\n\t"
      "v_pk_mul_f32 v[224:225], v[176:177], s[84:85]\n\t"
      "v_pk_mul_f32 v[226:227], v[178:179], s[84:85]\n\t"
      "v_pk_mul_f32 v[180:181], v[180:181], s[86:87]\n\t"
      "v_pk_mul_f32 v[182:183], v[182:183], s[86:87]\n\t"
      "v_exp_f32_e64 v224, -|v224|\n\t"
      "v_exp_f32_e64 v225, -|v225|\n\t"
      "v_exp_f32_e64 v226, -|v226|\n\t"
      "v_exp_f32_e64 v227, -|v227|\n\t"
      "v_exp_f32_e32 v180, v180\n\t"
      "v_exp_f32_e32 v181, v181\n\t"
      "v_exp_f32_e32 v182, v182\n\t"
      "v_exp_f32_e32 v183, v183\n\t"
      "v_pk_fma_f32 v[224:225], v[224:225], s[88:89], s[88:89]\n\t"
      "v_pk_fma_f32 v[226:227], v[226:227], s[88:89], s[88:89]\n\t"
      "v_pk_fma_f32 v[180:181], v[224:225], v[180:181], v[224:225]\n\t"
      "v_pk_fma_f32 v[182:183], v[226:227], v[182:183], v[226:227]\n\t"
      "v_rcp_f32_e32 v180, v180\n\t"
      "v_rcp_f32_e32 v181, v181\n\t"
      "v_rcp_f32_e32 v182, v182\n\t"
      "v_rcp_f32_e32 v183, v183\n\t"
      "v_bfi_b32 v176, s75, v180, v176\n\t"
      "v_bfi_b32 v177, s75, v181, v177\n\t"
      "v_bfi_b32 v178, s75, v182, v178\n\t"
      "v_bfi_b32 v179, s75, v183, v179\n\t"
      "v_pk_fma_f32 v[176:177], v[224:225], v[176:177], v[176:177] neg_lo:[1,0,0] neg_hi:[1,0,0]\n\t"
      "v_pk_fma_f32 v[178:179], v[226:227], v[178:179], v[178:179] neg_lo:[1,0,0] neg_hi:[1,0,0]\n\t"
      "v_cvt_pk_bf16_f32 v180, v176, v177\n\t"
      "v_cvt_pk_bf16_f32 v181, v178, v179\n\t"
      "s_mov_b32 s68, 45056\n\t"
      "buffer_store_dwordx2 v[180:181], %[vo], s[52:55], s68 offen offset:0 nt\n\t"
      "v_pk_mul_f32 v[228:229], v[184:185], s[84:85]\n\t"
      "v_pk_mul_f32 v[230:231], v[186:187], s[84:85]\n\t"
      "v_pk_mul_f32 v[188:189], v[188:189], s[86:87]\n\t"
      "v_pk_mul_f32 v[190:191], v[190:191], s[86:87]\n\t"
      "v_exp_f32_e64 v228, -|v228|\n\t"
      "v_exp_f32_e64 v229, -|v229|\n\t"
      "v_exp_f32_e64 v230, -|v230|\n\t"
      "v_exp_f32_e64 v231, -|v231|\n\t"
      "v_exp_f32_e32 v188, v188\n\t"
      "v_exp_f32_e32 v189, v189\n\t"
      "v_exp_f32_e32 v190, v190\n\t"
      "v_exp_f32_e32 v191, v191\n\t"
      "v_pk_fma_f32 v[228:229], v[228:229], s[88:89], s[88:89]\n\t"
      "v_pk_fma_f32 v[230:231], v[230:231], s[88:89], s[88:89]\n\t"
      "v_pk_fma_f32 v[188:189], v[228:229], v[188:189], v[228:229]\n\t"
      "v_pk_fma_f32 v[190:191], v[230:231], v[190:191], v[230:231]\n\t"
      "v_rcp_f32_e32 v188, v188\n\t"
      "v_rcp_f32_e32 v189, v189\n\t"
      "v_rcp_f32_e32 v190, v190\n\t"
      "v_rcp_f32_e32 v191, v191\n\t"
      "v_bfi_b32 v184, s75, v188, v184\n\t"
      "v_bfi_b32 v185, s75, v189, v185\n\t"
      "v_bfi_b32 v186, s75, v190, v186\n\t"
      "v_bfi_b32 v187, s75, v191, v187\n\t"
      "v_pk_fma_f32 v[184:185], v[228:229], v[184:185], v[184:185] neg_lo:[1,0,0] neg_hi:[1,0,0]\n\t"
      "v_pk_fma_f32 v[186:187], v[230:231], v[186:187], v[186:187] neg_lo:[1,0,0] neg_hi:[1,0,0]\n\t"
      "v_cvt_pk_bf16_f32 v188, v184, v185\n\t"
      "v_cvt_pk_bf16_f32 v189, v186, v187\n\t"
      "s_mov_b32 s68, 45056\n\t"
      "buffer_store_dwordx2 v[188:189], %[vo], s[52:55], s68 offen offset:2048 nt\n\t"
      "v_pk_mul_f32 v[224:225], v[192:193], s[84:85]\n\t"
      "v_pk_mul_f32 v[226:227], v[194:195], s[84:85]\n\t"
      "v_pk_mul_f32 v[196:197], v[196:197], s[86:87]\n\t"
      "v_pk_mul_f32 v[198:199], v[198:199], s[86:87]\n\t"
      "v_exp_f32_e64 v224, -|v224|\n\t"
      "v_exp_f32_e64 v225, -|v225|\n\t"
      "v_exp_f32_e64 v226, -|v226|\n\t"
      "v_exp_f32_e64 v227, -|v227|\n\t"
      "v_exp_f32_e32 v196, v196\n\t"
      "v_exp_f32_e32 v197, v197\n\t"
      "v_exp_f32_e32 v198, v198\n\t"
      "v_exp_f32_e32 v199, v199\n\t"
      "v_pk_fma_f32 v[224:225], v[224:225], s[88:89], s[88:89]\n\t"
      "v_pk_fma_f32 v[226:227], v[226:227], s[88:89], s[88:89]\n\t"
      "v_pk_fma_f32 v[196:197], v[224:225], v[196:197], v[224:225]\n\t"
      "v_pk_fma_f32 v[198:199], v[226:227], v[198:199], v[226:227]\n\t"
      "v_rcp_f32_e32 v196, v196\n\t"
      "v_rcp_f32_e32 v197, v197\n\t"
      "v_rcp_f32_e32 v198, v198\n\t"
      "v_rcp_f32_e32 v199, v199\n\t"
      "v_bfi_b32 v192, s75, v196, v192\n\t"
      "v_bfi_b32 v193, s75, v197, v193\n\t"
      "v_bfi_b32 v194, s75, v198, v194\n\t"
      "v_bfi_b32 v195, s75, v199, v195\n\t"
      "v_pk_fma_f32 v[192:193], v[224:225], v[192:193], v[192:193] neg_lo:[1,0,0] neg_hi:[1,0,0]\n\t"
      "v_pk_fma_f32 v[194:195], v[226:227], v[194:195], v[194:195] neg_lo:[1,0,0] neg_hi:[1,0,0]\n\t"
      "v_cvt_pk_bf16_f32 v196, v192, v193\n\t"
      "v_cvt_pk_bf16_f32 v197, v194, v195\n\t"
      "s_mov_b32 s68, 49152\n\t"
      "buffer_store_dwordx2 v[196:197], %[vo], s[52:55], s68 offen offset:0 nt\n\t"
      "v_pk_mul_f32 v[228:229], v[200:201], s[84:85]\n\t"
      "v_pk_mul_f32 v[230:231], v[202:203], s[84:85]\n\t"
      "v_pk_mul_f32 v[204:205], v[204:205], s[86:87]\n\t"
      "v_pk_mul_f32 v[206:207], v[206:207], s[86:87]\n\t"
      "v_exp_f32_e64 v228, -|v228|\n\t"
      "v_exp_f32_e64 v229, -|v229|\n\t"
      "v_exp_f32_e64 v230, -|v230|\n\t"
      "v_exp_f32_e64 v231, -|v231|\n\t"
      "v_exp_f32_e32 v204, v204\n\t"
      "v_exp_f32_e32 v205, v205\n\t"
      "v_exp_f32_e32 v206, v206\n\t"
      "v_exp_f32_e32 v207, v207\n\t"
      "v_pk_fma_f32 v[228:229], v[228:229], s[88:89], s[88:89]\n\t"
      "v_pk_fma_f32 v[230:231], v[230:231], s[88:89], s[88:89]\n\t"
      "v_pk_fma_f32 v[204:205], v[228:229], v[204:205], v[228:229]\n\t"
      "v_pk_fma_f32 v[206:207], v[230:231], v[206:207], v[230:231]\n\t"
      "v_rcp_f32_e32 v204, v204\n\t"
      "v_rcp_f32_e32 v205, v205\n\t"
      "v_rcp_f32_e32 v206, v206\n\t"
      "v_rcp_f32_e32 v207, v207\n\t"
      "v_bfi_b32 v200, s75, v204, v200\n\t"
      "v_bfi_b32 v201, s75, v205, v201\n\t"
      "v_bfi_b32 v202, s75, v206, v202\n\t"
      "v_bfi_b32 v203, s75, v207, v203\n\t"
      "v_pk_fma_f32 v[200:201], v[228:229], v[200:201], v[200:201] neg_lo:[1,0,0] neg_hi:[1,0,0]\n\t"
      "v_pk_fma_f32 v[202:203], v[230:231], v[202:203], v[202:203] neg_lo:[1,0,0] neg_hi:[1,0,0]\n\t"
      "v_cvt_pk_bf16_f32 v204, v200, v201\n\t"
      "v_cvt_pk_bf16_f32 v205, v202, v203\n\t"
      "s_mov_b32 s68, 49152\n\t"
      "buffer_store_dwordx2 v[204:205], %[vo], s[52:55], s68 offen offset:2048 nt\n\t"
      "v_pk_mul_f32 v[224:225], v[208:209], s[84:85]\n\t"
      "v_pk_mul_f32 v[226:227], v[210:211], s[84:85]\n\t"
      "v_pk_mul_f32 v[212:213], v[212:213], s[86:87]\n\t"
      "v_pk_mul_f32 v[214:215], v[214:215], s[86:87]\n\t"
      "v_exp_f32_e64 v224, -|v224|\n\t"
      "v_exp_f32_e64 v225, -|v225|\n\t"
      "v_exp_f32_e64 v226, -|v226|\n\t"
      "v_exp_f32_e64 v227, -|v227|\n\t"
      "v_exp_f32_e32 v212, v212\n\t"
      "v_exp_f32_e32 v213, v213\n\t"
      "v_exp_f32_e32 v214, v214\n\t"
      "v_exp_f32_e32 v215, v215\n\t"
      "v_pk_fma_f32 v[224:225], v[224:225], s[88:89], s[88:89]\n\t"
      "v_pk_fma_f32 v[226:227], v[226:227], s[88:89], s[88:89]\n\t"
      "v_pk_fma_f32 v[212:213], v[224:225], v[212:213], v[224:225]\n\t"
      "v_pk_fma_f32 v[214:215], v[226:227], v[214:215], v[226:227]\n\t"
      "v_rcp_f32_e32 v212, v212\n\t"
      "v_rcp_f32_e32 v213, v213\n\t"
      "v_rcp_f32_e32 v214, v214\n\t"
      "v_rcp_f32_e32 v215, v215\n\t"
      "v_bfi_b32 v208, s75, v212, v208\n\t"
      "v_bfi_b32 v209, s75, v213, v209\n\t"
      "v_bfi_b32 v210, s75, v214, v210\n\t"
      "v_bfi_b32 v211, s75, v215, v211\n\t"
      "v_pk_fma_f32 v[208:209], v[224:225], v[208:209], v[208:209] neg_lo:[1,0,0] neg_hi:[1,0,0]\n\t"
      "v_pk_fma_f32 v[210:211], v[226:227], v[210:211], v[210:211] neg_lo:[1,0,0] neg_hi:[1,0,0]\n\t"
      "v_cvt_pk_bf16_f32 v212, v208, v209\n\t"
      "v_cvt_pk_bf16_f32 v213, v210, v211\n\t"
      "s_mov_b32 s68, 53248\n\t"
      "buffer_store_dwordx2 v[212:213], %[vo], s[52:55], s68 offen offset:0 nt\n\t"
      "v_pk_mul_f32 v[228:229], v[216:217], s[84:85]\n\t"
      "v_pk_mul_f32 v[230:231], v[218:219], s[84:85]\n\t"
      "v_pk_mul_f32 v[220:221], v[220:221], s[86:87]\n\t"
      "v_pk_mul_f32 v[222:223], v[222:223], s[86:87]\n\t"
      "v_exp_f32_e64 v228, -|v228|\n\t"
      "v_exp_f32_e64 v229, -|v229|\n\t"
      "v_exp_f32_e64 v230, -|v230|\n\t"
      "v_exp_f32_e64 v231, -|v231|\n\t"
      "v_exp_f32_e32 v220, v220\n\t"
      "v_exp_f32_e32 v221, v221\n\t"
      "v_exp_f32_e32 v222, v222\n\t"
      "v_exp_f32_e32 v223, v223\n\t"
      "v_pk_fma_f32 v[228:229], v[228:229], s[88:89], s[88:89]\n\t"
      "v_pk_fma_f32 v[230:231], v[230:231], s[88:89], s[88:89]\n\t"
      "v_pk_fma_f32 v[220:221], v[228:229], v[220:221], v[228:229]\n\t"
      "v_pk_fma_f32 v[222:223], v[230:231], v[222:223], v[230:231]\n\t"
      "v_rcp_f32_e32 v220, v220\n\t"
      "v_rcp_f32_e32 v221, v221\n\t"
      "v_rcp_f32_e32 v222, v222\n\t"
      "v_rcp_f32_e32 v223, v223\n\t"
      "v_bfi_b32 v216, s75, v220, v216\n\t"
      "v_bfi_b32 v217, s75, v221, v217\n\t"
      "v_bfi_b32 v218, s75, v222, v218\n\t"
      "v_bfi_b32 v219, s75, v223, v219\n\t"
      "v_pk_fma_f32 v[216:217], v[228:229], v[216:217], v[216:217] neg_lo:[1,0,0] neg_hi:[1,0,0]\n\t"
      "v_pk_fma_f32 v[218:219], v[230:231], v[218:219], v[218:219] neg_lo:[1,0,0] neg_hi:[1,0,0]\n\t"
      "v_cvt_pk_bf16_f32 v220, v216, v217\n\t"
      "v_cvt_pk_bf16_f32 v221, v218, v219\n\t"
      "s_mov_b32 s68, 53248\n\t"
      "buffer_store_dwordx2 v[220:221], %[vo], s[52:55], s68 offen offset:2048 nt\n\t"
      "v_accvgpr_read_b32 v232, a0\n\t"
      "v_accvgpr_read_b32 v233, a1\n\t"
      "v_accvgpr_read_b32 v234, a2\n\t"
      "v_accvgpr_read_b32 v235, a3\n\t"
      "v_accvgpr_read_b32 v236, a4\n\t"
      "v_accvgpr_read_b32 v237, a5\n\t"
      "v_accvgpr_read_b32 v238, a6\n\t"
      "v_accvgpr_read_b32 v239, a7\n\t"
      "v_pk_mul_f32 v[224:225], v[232:233], s[84:85]\n\t"
      "v_pk_mul_f32 v[226:227], v[234:235], s[84:85]\n\t"
      "v_pk_mul_f32 v[236:237], v[236:237], s[86:87]\n\t"
      "v_pk_mul_f32 v[238:239], v[238:239], s[86:87]\n\t"
      "v_exp_f32_e64 v224, -|v224|\n\t"
      "v_exp_f32_e64 v225, -|v225|\n\t"
      "v_exp_f32_e64 v226, -|v226|\n\t"
      "v_exp_f32_e64 v227, -|v227|\n\t"
      "v_exp_f32_e32 v236, v236\n\t"
      "v_exp_f32_e32 v237, v237\n\t"
      "v_exp_f32_e32 v238, v238\n\t"
      "v_exp_f32_e32 v239, v239\n\t"
      "v_pk_fma_f32 v[224:225], v[224:225], s[88:89], s[88:89]\n\t"
      "v_pk_fma_f32 v[226:227], v[226:227], s[88:89], s[88:89]\n\t"
      "v_pk_fma_f32 v[236:237], v[224:225], v[236:237], v[224:225]\n\t"
      "v_pk_fma_f32 v[238:239], v[226:227], v[238:239], v[226:227]\n\t"
      "v_rcp_f32_e32 v236, v236\n\t"
      "v_rcp_f32_e32 v237, v237\n\t"
      "v_rcp_f32_e32 v238, v238\n\t"
      "v_rcp_f32_e32 v239, v239\n\t"
      "v_bfi_b32 v232, s75, v236, v232\n\t"
      "v_bfi_b32 v233, s75, v237, v233\n\t"
      "v_bfi_b32 v234, s75, v238, v234\n\t"
      "v_bfi_b32 v235, s75, v239, v235\n\t"
      "v_pk_fma_f32 v[232:233], v[224:225], v[232:233], v[232:233] neg_lo:[1,0,0] neg_hi:[1,0,0]\n\t"
      "v_pk_fma_f32 v[234:235], v[226:227], v[234:235], v[234:235] neg_lo:[1,0,0] neg_hi:[1,0,0]\n\t"
      "v_cvt_pk_bf16_f32 v236, v232, v233\n\t"
      "v_cvt_pk_bf16_f32 v237, v234, v235\n\t"
      "s_mov_b32 s68, 57344\n\t"
      "buffer_store_dwordx2 v[236:237], %[vo], s[52:55], s68 offen offset:0 nt\n\t"
      "v_accvgpr_read_b32 v240, a8\n\t"
      "v_accvgpr_read_b32 v241, a9\n\t"
      "v_accvgpr_read_b32 v242, a10\n\t"
      "v_accvgpr_read_b32 v243, a11\n\t"
      "v_accvgpr_read_b32 v244, a12\n\t"
      "v_accvgpr_read_b32 v245, a13\n\t"
      "v_accvgpr_read_b32 v246, a14\n\t"
      "v_accvgpr_read_b32 v247, a15\n\t"
      "v_pk_mul_f32 v[228:229], v[240:241], s[84:85]\n\t"
      "v_pk_mul_f32 v[230:231], v[242:243], s[84:85]\n\t"
      "v_pk_mul_f32 v[244:245], v[244:245], s[86:87]\n\t"
      "v_pk_mul_f32 v[246:247], v[246:247], s[86:87]\n\t"
      "v_exp_f32_e64 v228, -|v228|\n\t"
      "v_exp_f32_e64 v229, -|v229|\n\t"
      "v_exp_f32_e64 v230, -|v230|\n\t"
      "v_exp_f32_e64 v231, -|v231|\n\t"
      "v_exp_f32_e32 v244, v244\n\t"
      "v_exp_f32_e32 v245, v245\n\t"
      "v_exp_f32_e32 v246, v246\n\t"
      "v_exp_f32_e32 v247, v247\n\t"
      "v_pk_fma_f32 v[228:229], v[228:229], s[88:89], s[88:89]\n\t"
      "v_pk_fma_f32 v[230:231], v[230:231], s[88:89], s[88:89]\n\t"
      "v_pk_fma_f32 v[244:245], v[228:229], v[244:245], v[228:229]\n\t"
      "v_pk_fma_f32 v[246:247], v[230:231], v[246:247], v[230:231]\n\t"
      "v_rcp_f32_e32 v244, v244\n\t"
      "v_rcp_f32_e32 v245, v245\n\t"
      "v_rcp_f32_e32 v246, v246\n\t"
      "v_rcp_f32_e32 v247, v247\n\t"
      "v_bfi_b32 v240, s75, v244, v240\n\t"
      "v_bfi_b32 v241, s75, v245, v241\n\t"
      "v_bfi_b32 v242, s75, v246, v242\n\t"
      "v_bfi_b32 v243, s75, v247, v243\n\t"
      "v_pk_fma_f32 v[240:241], v[228:229], v[240:241], v[240:241] neg_lo:[1,0,0] neg_hi:[1,0,0]\n\t"
      "v_pk_fma_f32 v[242:243], v[230:231], v[242:243], v[242:243] neg_lo:[1,0,0] neg_hi:[1,0,0]\n\t"
      "v_cvt_pk_bf16_f32 v244, v240, v241\n\t"
      "v_cvt_pk_bf16_f32 v245, v242, v243\n\t"
      "s_mov_b32 s68, 57344\n\t"
      "buffer_store_dwordx2 v[244:245], %[vo], s[52:55], s68 offen offset:2048 nt\n\t"
      "s_cmp_le_u32 s79, 30\n\t"
      "s_cbranch_scc1 LEND_%=\n\t"
      "v_accvgpr_read_b32 v232, a16\n\t"
      "v_accvgpr_read_b32 v233, a17\n\t"
      "v_accvgpr_read_b32 v234, a18\n\t"
      "v_accvgpr_read_b32 v235, a19\n\t"
      "v_accvgpr_read_b32 v236, a20\n\t"
      "v_accvgpr_read_b32 v237, a21\n\t"
      "v_accvgpr_read_b32 v238, a22\n\t"
      "v_accvgpr_read_b32 v239, a23\n\t"
      "v_pk_mul_f32 v[224:225], v[232:233], s[84:85]\n\t"
      "v_pk_mul_f32 v[226:227], v[234:235], s[84:85]\n\t"
      "v_pk_mul_f32 v[236:237], v[236:237], s[86:87]\n\t"
      "v_pk_mul_f32 v[238:239], v[238:239], s[86:87]\n\t"
      "v_exp_f32_e64 v224, -|v224|\n\t"
      "v_exp_f32_e64 v225, -|v225|\n\t"
      "v_exp_f32_e64 v226, -|v226|\n\t"
      "v_exp_f32_e64 v227, -|v227|\n\t"
      "v_exp_f32_e32 v236, v236\n\t"
      "v_exp_f32_e32 v237, v237\n\t"
      "v_exp_f32_e32 v238, v238\n\t"
      "v_exp_f32_e32 v239, v239\n\t"
      "v_pk_fma_f32 v[224:225], v[224:225], s[88:89], s[88:89]\n\t"
      "v_pk_fma_f32 v[226:227], v[226:227], s[88:89], s[88:89]\n\t"
      "v_pk_fma_f32 v[236:237], v[224:225], v[236:237], v[224:225]\n\t"
      "v_pk_fma_f32 v[238:239], v[226:227], v[238:239], v[226:227]\n\t"
      "v_rcp_f32_e32 v236, v236\n\t"
      "v_rcp_f32_e32 v237, v237\n\t"
      "v_rcp_f32_e32 v238, v238\n\t"
      "v_rcp_f32_e32 v239, v239\n\t"
      "v_bfi_b32 v232, s75, v236, v232\n\t"
      "v_bfi_b32 v233, s75, v237, v233\n\t"
      "v_bfi_b32 v234, s75, v238, v234\n\t"
      "v_bfi_b32 v235, s75, v239, v235\n\t"
      "v_pk_fma_f32 v[232:233], v[224:225], v[232:233], v[232:233] neg_lo:[1,0,0] neg_hi:[1,0,0]\n\t"
      "v_pk_fma_f32 v[234:235], v[226:227], v[234:235], v[234:235] neg_lo:[1,0,0] neg_hi:[1,0,0]\n\t"
      "v_cvt_pk_bf16_f32 v236, v232, v233\n\t"
      "v_cvt_pk_bf16_f32 v237, v234, v235\n\t"
      "s_cmp_gt_u32 s79, 30\n\ts_cselect_b32 s68, 61440, s80\n\t"
      "buffer_store_dwordx2 v[236:237], %[vo], s[52:55], s68 offen offset:0 nt\n\t"
      "s_cmp_le_u32 s79, 31\n\t"
      "s_cbranch_scc1 LEND_%=\n\t"
      "v_accvgpr_read_b32 v240, a24\n\t"
      "v_accvgpr_read_b32 v241, a25\n\t"
      "v_accvgpr_read_b32 v242, a26\n\t"
      "v_accvgpr_read_b32 v243, a27\n\t"
      "v_accvgpr_read_b32 v244, a28\n\t"
      "v_accvgpr_read_b32 v245, a29\n\t"
      "v_accvgpr_read_b32 v246, a30\n\t"
      "v_accvgpr_read_b32 v247, a31\n\t"
      "v_pk_mul_f32 v[228:229], v[240:241], s[84:85]\n\t"
      "v_pk_mul_f32 v[230:231], v[242:243], s[84:85]\n\t"
      "v_pk_mul_f32 v[244:245], v[244:245], s[86:87]\n\t"
      "v_pk_mul_f32 v[246:247], v[246:247], s[86:87]\n\t"
      "v_exp_f32_e64 v228, -|v228|\n\t"
      "v_exp_f32_e64 v229, -|v229|\n\t"
      "v_exp_f32_e64 v230, -|v230|\n\t"
      "v_exp_f32_e64 v231, -|v231|\n\t"
      "v_exp_f32_e32 v244, v244\n\t"
      "v_exp_f32_e32 v245, v245\n\t"
      "v_exp_f32_e32 v246, v246\n\t"
      "v_exp_f32_e32 v247, v247\n\t"
      "v_pk_fma_f32 v[228:229], v[228:229], s[88:89], s[88:89]\n\t"
      "v_pk_fma_f32 v[230:231], v[230:231], s[88:89], s[88:89]\n\t"
      "v_pk_fma_f32 v[244:245], v[228:229], v[244:245], v[228:229]\n\t"
      "v_pk_fma_f32 v[246:247], v[230:231], v[246:247], v[230:231]\n\t"
      "v_rcp_f32_e32 v244, v244\n\t"
      "v_rcp_f32_e32 v245, v245\n\t"
      "v_rcp_f32_e32 v246, v246\n\t"
      "v_rcp_f32_e32 v247, v247\n\t"
      "v_bfi_b32 v240, s75, v244, v240\n\t"
      "v_bfi_b32 v241, s75, v245, v241\n\t"
      "v_bfi_b32 v242, s75, v246, v242\n\t"
      "v_bfi_b32 v243, s75, v247, v243\n\t"
      "v_pk_fma_f32 v[240:241], v[228:229], v[240:241], v[240:241] neg_lo:[1,0,0] neg_hi:[1,0,0]\n\t"
      "v_pk_fma_f32 v[242:243], v[230:231], v[242:243], v[242:243] neg_lo:[1,0,0] neg_hi:[1,0,0]\n\t"
      "v_cvt_pk_bf16_f32 v244, v240, v241\n\t"
      "v_cvt_pk_bf16_f32 v245, v242, v243\n\t"
      "s_cmp_gt_u32 s79, 31\n\ts_cselect_b32 s68, 61440, s80\n\t"
      "buffer_store_dwordx2 v[244:245], %[vo], s[52:55], s68 offen offset:2048 nt\n\t"
      "LEND_%=:\n\t"
      "wavernn_code_end:\n\t"
      : [vx] "+v"(vx), [vo] "+v"(vo), "+{v247}"(vcw), "+{s[48:51]}"(srdx), "+{s[52:55]}"(srdo), "+{s71}"(sx0), "+{s72}"(sw), "+{s74}"(islow),
        "+{s76}"(sx1), "+{s77}"(sxh), "+{s78}"(ija)
      : "{s[44:47]}"(srdc), "{s[56:59]}"(srdw), [ijb] "s"(ijb), [idx] "s"(idx_), [ismx] "s"(ismx), [ismw] "s"(ismw),
        [vw] "v"(vw), [lx0] "v"(lx0), [lx1] "v"(lx1), [lw0] "v"(lw0), [lw1] "v"(lw1), [vod] "v"(vod)
      : "memory", "scc", "m0", "v0", "v1", "v2", "v3", "v4", "v5", "v6", "v7", "v8", "v9", "v10", "v11", "v12", "v13", "v14", "v15", "v16", "v17", "v18", "v19", "v20", "v21", "v22", "v23", "v24", "v25", "v26", "v27", "v28", "v29", "v30", "v31", "v32", "v33", "v34", "v35", "v36", "v37", "v38", "v39", "v40", "v41", "v42", "v43", "v44", "v45", "v46", "v47", "v48", "v49", "v50", "v51", "v52", "v53", "v54", "v55", "v56", "v57", "v58", "v59", "v60", "v61", "v62", "v63", "v64", "v65", "v66", "v67", "v68", "v69", "v70", "v71", "v72", "v73", "v74", "v75", "v76", "v77", "v78", "v79", "v80", "v81", "v82", "v83", "v84", "v85", "v86", "v87", "v88", "v89", "v90", "v91", "v92", "v93", "v94", "v95", "v96", "v97", "v98", "v99", "v100", "v101", "v102", "v103", "v104", "v105", "v106", "v107", "v108", "v109", "v110", "v111", "v112", "v113", "v114", "v115", "v116", "v117", "v118", "v119", "v120", "v121", "v122", "v123", "v124", "v125", "v126", "v127", "v128", "v129", "v130", "v131", "v132", "v133", "v134", "v135", "v136", "v137", "v138", "v139", "v140", "v141", "v142", "v143", "v144", "v145", "v146", "v147", "v148", "v149", "v150", "v151", "v152", "v153", "v154", "v155", "v156", "v157", "v158", "v159", "v160", "v161", "v162", "v163", "v164", "v165", "v166", "v167", "v168", "v169", "v170", "v171", "v172", "v173", "v174", "v175", "v176", "v177", "v178", "v179", "v180", "v181", "v182", "v183", "v184", "v185", "v186", "v187", "v188", "v189", "v190", "v191", "v192", "v193", "v194", "v195", "v196", "v197", "v198", "v199", "v200", "v201", "v202", "v203", "v204", "v205", "v206", "v207", "v208", "v209", "v210", "v211", "v212", "v213", "v214", "v215", "v216", "v217", "v218", "v219", "v220", "v221", "v222", "v223", "v224", "v225", "v226", "v227", "v228", "v229", "v230", "v231", "v232", "v233", "v234", "v235", "v236", "v237", "v238", "v239", "v240", "v241", "v242", "v243", "v244", "v245", "v246", "a0", "a1", "a2", "a3", "a4", "a5", "a6", "a7", "a8", "a9", "a10", "a11", "a12", "a13", "a14", "a15", "a16", "a17", "a18", "a19", "a20", "a21", "a22", "a23", "a24", "a25", "a26", "a27", "a28", "a29", "a30", "a31", "a32", "a33", "a34", "a35", "a36", "a37", "a38", "a39", "a40", "a41", "a42", "a43", "a44", "a45", "a46", "a47", "a48", "a49", "a50", "a51", "a52", "a53", "a54", "a55", "a56", "a57", "a58", "a59", "a60", "a61", "a62", "a63", "a64", "a65", "a66", "a67", "a68", "a69", "a70", "a71", "a72", "a73", "a74", "a75", "a76", "a77", "a78", "a79", "a80", "a81", "a82", "a83", "a84", "a85", "a86", "a87", "a88", "a89", "a90", "a91", "a92", "a93", "a94", "a95", "a96", "a97", "a98", "a99", "a100", "a101", "a102", "a103", "a104", "a105", "a106", "a107", "a108", "a109", "a110", "a111", "a112", "a113", "a114", "a115", "a116", "a117", "a118", "a119", "a120", "a121", "a122", "a123", "a124", "a125", "a126", "a127", "a128", "a129", "a130", "a131", "a132", "a133", "a134", "a135", "a136", "a137", "a138", "a139", "a140", "a141", "a142", "a143", "a144", "a145", "a146", "a147", "a148", "a149", "a150", "a151", "a152", "a153", "a154", "a155", "a156", "a157", "a158", "a159", "a160", "a161", "a162", "a163", "a164", "a165", "a166", "a167", "a168", "a169", "a170", "a171", "a172", "a173", "a174", "a175", "a176", "a177", "a178", "s60", "s61", "s62", "s63", "s64", "s65", "s66", "s67", "s68", "s69", "s70", "s73", "s75", "s79", "s80", "s81", "s82", "s83", "s84", "s85", "s86", "s87", "s88", "s89");
}

extern "C" void kernel_launch(void* const* d_in, const int* in_sizes, int n_in,
                              void* d_out, int out_size, void* d_ws, size_t ws_size,
                              hipStream_t stream) {
  (void)in_sizes; (void)n_in; (void)out_size; (void)ws_size; (void)d_ws;
  wavernn_conv_main<<<256, 256, 0, stream>>>((const char*)d_in[0], (const char*)d_in[1], (char*)d_out );
}
```
